```python
import torch

def _softmax(x, axis=-1):
    return torch.softmax(x, dim=axis)
def _gelu(x):
    return torch.nn.functional.gelu(x, approximate="tanh")
def _silu(x):
    return torch.nn.functional.silu(x)
def _sigmoid(x):
    return torch.sigmoid(x)
def _layernorm(x, axis=-1, eps=1e-5):
    m = x.mean(dim=axis, keepdim=True)
    v = x.var(dim=axis, keepdim=True, unbiased=False)
    return (x - m) / torch.sqrt(v + eps)
def _scatter_add(tgt, idx, src, axis=0):
    return tgt.clone().index_add_(axis, idx, src)
def _segment_sum(src, seg, n):
    out = torch.zeros((n,) + src.shape[1:], dtype=src.dtype)
    return out.index_add_(0, seg, src)
def _pad(x, pw):
    flat = tuple(v for pair in reversed(pw) for v in pair)
    return torch.nn.functional.pad(x, flat)

kernel_name = 'interleave_layers__ssm_conv_h3__linear_attn_block__prompt'

def setup_inputs():

    _w = (lambda a: (lambda t: t.to(torch.bfloat16) if t.is_floating_point() else t)(torch.as_tensor(a)))
    def _A_setup():
        B, S, D = 1, 2048, 1024
        ks = 4
        return {
            "q": _w((lambda *s: torch.randn(*s, dtype=torch.bfloat16) * 0.05)(B, S, D)),
            "k": _w((lambda *s: torch.randn(*s, dtype=torch.bfloat16) * 0.4)(B, S, D)),
            "v": _w((lambda *s: torch.randn(*s, dtype=torch.bfloat16) * 0.4)(B, S, D)),
            "shift_k": _w((lambda *s: torch.randn(*s, dtype=torch.bfloat16) * 0.4)(D, ks) * 0.5),
            "ssm_k": _w((lambda *s: torch.randn(*s, dtype=torch.bfloat16) * 0.4)(D, S) * (1.0 / S ** 0.5)),
            "ssm_D": _w((lambda *s: torch.randn(*s, dtype=torch.bfloat16) * 0.4)(D)),
        }
    _A_in = _A_setup()

    def _B_setup():
        B, S, D = 1, 2048, 1024
        return {
            "x": _w((lambda *s: torch.randn(*s, dtype=torch.bfloat16) * 0.05)(B, S, D)),
            "wqkv": _w((lambda *s: torch.randn(*s, dtype=torch.bfloat16) * 0.1)(D, 3 * D)),
            "wo": _w((lambda *s: torch.randn(*s, dtype=torch.bfloat16) * 0.05)(D, D)),
        }
    _B_in = _B_setup()

    return {
        "A_q": _A_in["q"],
        "A_k": _A_in["k"],
        "A_v": _A_in["v"],
        "A_shift_k": _A_in["shift_k"],
        "A_ssm_k": _A_in["ssm_k"],
        "A_ssm_D": _A_in["ssm_D"],
        "B_x": _B_in["x"],
        "B_wqkv": _B_in["wqkv"],
        "B_wo": _B_in["wo"],
    }

def reference(A_q, A_k, A_v, A_shift_k, A_ssm_k, A_ssm_D, B_x, B_wqkv, B_wo):
    def _A_ref(q, k, v, shift_k, ssm_k, ssm_D):
        B, S, D = q.shape
        ks = shift_k.shape[-1]
        k_pad = _pad(k, ((0, 0), (ks - 1, 0), (0, 0)))
        k_shift = torch.zeros_like(k)
        for t in range(ks):
            k_shift = k_shift + k_pad[:, t:t + S, :] * shift_k[:, ks - 1 - t][None, None, :]
        kv = k_shift * v
        L = 2 * S
        kv_pad = _pad(kv, ((0, 0), (0, S), (0, 0)))
        kf_pad = _pad(ssm_k, ((0, 0), (0, S)))
        kv_f = (lambda x, axes=(-2,-1): torch.fft.rfft2(x.float(), dim=axes))(kv_pad[:, None, :, :], axes=(1, 2))
        kf_f = (lambda x, axes=(-2,-1): torch.fft.rfft2(x.float(), dim=axes))(kf_pad[:, None, :], axes=(1, 2))
        kf_f = (lambda x, *d: x.permute(*d))(kf_f, 1, 2, 0)
        y_f = kv_f * kf_f[None, :, :, :]
        y = (lambda x, s, axes=(-2,-1): torch.fft.irfft2(x, s=s, dim=axes).to(torch.bfloat16))(y_f, (1, L), axes=(1, 2))[:, 0, :S, :]
        y = y + kv * ssm_D[None, None, :]
        return q * y

    def _B_ref(x, wqkv, wo):
        B, S, D = x.shape
        H = 32; Dh = D // H
        qkv = (lambda x, *d: x.permute(*d))((x @ wqkv).reshape(B, S, 3, H, Dh), 2, 0, 3, 1, 4)
        q, k, v = qkv[0], qkv[1], qkv[2]
        phi = lambda t: torch.where(t > 0, t + 1, torch.exp(t)) + 1e-6
        pq, pk = phi(q), phi(k)
        kv = torch.einsum("bhsd,bhse->bhde", pk, v)
        z = torch.einsum("bhsd->bhd", pk)
        num = torch.einsum("bhqd,bhde->bhqe", pq, kv)
        den = torch.einsum("bhqd,bhd->bhq", pq, z)[..., None] + 1e-6
        o = num / den
        return (lambda x, *d: x.permute(*d))(o, 0, 2, 1, 3).reshape(B, S, D) @ wo

    x = A_q
    x = x + _A_ref(_layernorm(x, axis=-1), A_k, A_v, A_shift_k, A_ssm_k, A_ssm_D)
    x = x + _B_ref(_layernorm(x, axis=-1), B_wqkv, B_wo)
    x = x + _A_ref(_layernorm(x, axis=-1), A_k, A_v, A_shift_k, A_ssm_k, A_ssm_D)
    x = x + _B_ref(_layernorm(x, axis=-1), B_wqkv, B_wo)
    return x
```

```cpp
#include <hip/hip_runtime.h>
#include <stdint.h>

typedef __attribute__((ext_vector_type(8))) short bf16x8;
typedef __attribute__((ext_vector_type(4))) float f32x4;
typedef __attribute__((ext_vector_type(2))) float f32x2;
typedef __attribute__((ext_vector_type(16))) float f32x16;
typedef __attribute__((ext_vector_type(4))) unsigned u32x4;
typedef __attribute__((ext_vector_type(2))) unsigned u32x2;
typedef __attribute__((ext_vector_type(2))) __bf16 bf16x2_t;
#define DI __device__ __forceinline__
#define LAS __attribute__((address_space(3)))

constexpr int S = 2048, D = 1024, H = 32;
constexpr size_t MB = 1024 * 1024;
constexpr size_t OFF_UB = 0, OFF_X0 = 4 * MB, OFF_W8 = 8 * MB, OFF_WOF = 14 * MB, OFF_Y = 16 * MB, OFF_X1 = 20 * MB,
                 OFF_PQ = 24 * MB, OFF_X2 = 28 * MB, OFF_X3 = 32 * MB, OFF_SM = 36 * MB;
constexpr size_t OFF_ST0 = OFF_SM, OFF_ST1 = OFF_SM + 16384, OFF_ST3 = OFF_SM + 32768, OFF_CSP = OFF_SM + 49152,
                 OFF_KV = OFF_SM + 49152 + 98304;
constexpr int KV_LAYER = 32 * 1024 + 32 * 32;

DI float bflo(unsigned u) { return __uint_as_float(u << 16); }
DI float bfhi(unsigned u) { return __uint_as_float(u & 0xffff0000u); }
DI unsigned pk2(float a, float b) {
  f32x2 v = {a, b};
  bf16x2_t r = __builtin_convertvector(v, bf16x2_t);
  return __builtin_bit_cast(unsigned, r);
}
DI float rbf(float a) { return bflo(pk2(a, a)); }
DI bf16x8 as_frag(u32x4 v) { return __builtin_bit_cast(bf16x8, v); }
#define MFMA32(a, b, c) __builtin_amdgcn_mfma_f32_32x32x16_bf16((a), (b), (c), 0, 0, 0)
#define MFMA16(a, b, c) __builtin_amdgcn_mfma_f32_16x16x32_bf16((a), (b), (c), 0, 0, 0)

DI f32x2 ln_stats(float s, float q) {
  float m = s * (1.0f / 1024.0f);
  float v = fmaxf(q * (1.0f / 1024.0f) - m * m, 0.0f);
  float sd = rbf(sqrtf(rbf(rbf(v) + 1e-5f)));
  f32x2 r = {rbf(m), 1.0f / sd};
  return r;
}

__global__ __launch_bounds__(256) void prep_kernel(const uint16_t* __restrict__ q, const uint16_t* __restrict__ k,
                                                   const uint16_t* __restrict__ v, const uint16_t* __restrict__ shift_k,
                                                   const uint16_t* __restrict__ wqkv, const uint16_t* __restrict__ wo,
                                                   char* __restrict__ ws) {
  __shared__ float red[4][128];
  const int tid = threadIdx.x, lane = tid & 63, w = tid >> 6, bid = blockIdx.x;
  if (bid < 256) {
    const int s0 = bid * 8;
    u32x2 kk[11], vv[8], qq[8];
#pragma unroll
    for (int r = 0; r < 11; ++r) {
      const int s = s0 - 3 + r;
      kk[r] = (s >= 0) ? *(const u32x2*)(k + (size_t)s * D + tid * 4) : (u32x2){0u, 0u};
    }
#pragma unroll
    for (int r = 0; r < 8; ++r) {
      vv[r] = *(const u32x2*)(v + (size_t)(s0 + r) * D + tid * 4);
      qq[r] = *(const u32x2*)(q + (size_t)(s0 + r) * D + tid * 4);
    }
    const u32x4 wa = *(const u32x4*)(shift_k + tid * 16), wb = *(const u32x4*)(shift_k + tid * 16 + 8);
    u32x4* ub = (u32x4*)(ws + OFF_UB) + (size_t)bid * 1024 + tid * 4;
#pragma unroll
    for (int c = 0; c < 4; ++c) {
      const unsigned w01 = (c == 0) ? wa[0] : (c == 1) ? wa[2] : (c == 2) ? wb[0] : wb[2];
      const unsigned w23 = (c == 0) ? wa[1] : (c == 1) ? wa[3] : (c == 2) ? wb[1] : wb[3];
      const float w0 = bflo(w01), w1 = bfhi(w01), w2 = bflo(w23), w3 = bfhi(w23);
      float kf[11], u[8];
#pragma unroll
      for (int r = 0; r < 11; ++r) kf[r] = (c & 1) ? bfhi(kk[r][c >> 1]) : bflo(kk[r][c >> 1]);
#pragma unroll
      for (int r = 0; r < 8; ++r) {
        const float ks = kf[r] * w3 + kf[r + 1] * w2 + kf[r + 2] * w1 + kf[r + 3] * w0;
        u[r] = ks * ((c & 1) ? bfhi(vv[r][c >> 1]) : bflo(vv[r][c >> 1]));
      }
      u32x4 o = {pk2(u[7], u[6]), pk2(u[5], u[4]), pk2(u[3], u[2]), pk2(u[1], u[0])};
      ub[c] = o;
    }
    u32x4* x0 = (u32x4*)(ws + OFF_X0) + ((size_t)bid * 256 + tid) * 4;
#pragma unroll
    for (int r = 0; r < 4; ++r) x0[r] = (u32x4){qq[2 * r][0], qq[2 * r][1], qq[2 * r + 1][0], qq[2 * r + 1][1]};
    float a[16];
#pragma unroll
    for (int r = 0; r < 8; ++r) {
      const float x0f = bflo(qq[r][0]), x1f = bfhi(qq[r][0]), x2f = bflo(qq[r][1]), x3f = bfhi(qq[r][1]);
      a[2 * r] = (x0f + x1f) + (x2f + x3f);
      a[2 * r + 1] = (x0f * x0f + x1f * x1f) + (x2f * x2f + x3f * x3f);
    }
#pragma unroll
    for (int st = 0; st < 4; ++st) {
      const int m = 1 << st, n = 8 >> st;
      const bool up = (lane & m) != 0;
#pragma unroll
      for (int i = 0; i < n; ++i) {
        const float keep = up ? a[2 * i + 1] : a[2 * i], send = up ? a[2 * i] : a[2 * i + 1];
        a[i] = keep + __shfl_xor(send, m);
      }
    }
    float t = a[0];
    t += __shfl_xor(t, 16);
    t += __shfl_xor(t, 32);
    if (lane < 16) red[w][lane] = t;
    __syncthreads();
    if (tid < 8) {
      const float sm = (red[0][2 * tid] + red[1][2 * tid]) + (red[2][2 * tid] + red[3][2 * tid]);
      const float sq = (red[0][2 * tid + 1] + red[1][2 * tid + 1]) + (red[2][2 * tid + 1] + red[3][2 * tid + 1]);
      ((f32x2*)(ws + OFF_ST0))[s0 + tid] = ln_stats(sm, sq);
    }
  } else {
    const int b = bid - 256, nbk = tid & 15, oo = tid >> 4;
    if (tid < 66) ((f32x4*)(ws + OFF_KV))[b * 66 + tid] = (f32x4){0.f, 0.f, 0.f, 0.f};
    u32x4 a[8];
    u32x4* dst;
    const bool isw = b < 192;
    int og, ng;
    if (isw) {
      og = b / 24; ng = b % 24;
      const int o = og * 16 + oo, n0 = ng * 128 + nbk * 8;
#pragma unroll
      for (int r = 0; r < 8; ++r) a[r] = *(const u32x4*)(wqkv + (size_t)(8 * o + r) * 3072 + n0);
      const int part = n0 >> 10, nn = n0 & 1023;
      dst = (u32x4*)(ws + OFF_W8) + ((size_t)((nn >> 6) * 128 + o) * 192 + part * 64 + (nn & 63));
    } else {
      og = (b - 192) >> 3; ng = (b - 192) & 7;
      const int ko = og * 16 + oo, n0 = ng * 128 + nbk * 8, h = ko >> 2, t = (ko >> 1) & 1, hf = ko & 1;
#pragma unroll
      for (int r = 0; r < 8; ++r) a[r] = *(const u32x4*)(wo + (size_t)(32 * h + 16 * t + 4 * hf + (r & 3) + 8 * (r >> 2)) * 1024 + n0);
      dst = (u32x4*)(ws + OFF_WOF) + ((size_t)(((n0 >> 5) * 32 + h) * 2 + t) * 64 + hf * 32 + (n0 & 31));
    }
#pragma unroll
    for (int c = 0; c < 8; ++c) {
      u32x4 o;
#pragma unroll
      for (int p = 0; p < 4; ++p) o[p] = __builtin_amdgcn_perm(a[2 * p + 1][c >> 1], a[2 * p][c >> 1], (c & 1) ? 0x07060302u : 0x05040100u);
      dst[c] = o;
    }
    if (isw) {
      float cs[8];
#pragma unroll
      for (int c = 0; c < 8; ++c) {
        float t = 0.f;
#pragma unroll
        for (int r = 0; r < 8; ++r) t += (c & 1) ? bfhi(a[r][c >> 1]) : bflo(a[r][c >> 1]);
        t += __shfl_xor(t, 16);
        t += __shfl_xor(t, 32);
        cs[c] = t;
      }
      if (lane < 16) {
#pragma unroll
        for (int c = 0; c < 8; ++c) red[w][lane * 8 + c] = cs[c];
      }
      __syncthreads();
      if (tid < 128) {
        const int n = ng * 128 + tid, part = n >> 10, nn = n & 1023;
        ((float*)(ws + OFF_CSP))[og * 3072 + (nn >> 6) * 192 + part * 64 + (nn & 63)] =
            (red[0][tid] + red[1][tid]) + (red[2][tid] + red[3][tid]);
      }
    }
  }
}

constexpr int CV_U = 286 * 16;
constexpr int CV_HA = CV_U;
constexpr int CV_HB = CV_HA + 4160;
constexpr int CV_CH = CV_HB + 4128 + 32;
__global__ __launch_bounds__(256) void conv_kernel(const uint16_t* __restrict__ ssm_k, const uint16_t* __restrict__ ssm_D,
                                                   char* __restrict__ ws) {
  __shared__ __attribute__((aligned(16))) char smem[4 * CV_CH];
  const int tid = threadIdx.x, lane = tid & 63, w = __builtin_amdgcn_readfirstlane(tid >> 6);
  const int j = (blockIdx.x & 7) * 32 + (blockIdx.x >> 3);
  u32x4 uu[4], hh[4];
  unsigned hn[4];
  const u32x4* ub = (const u32x4*)(ws + OFF_UB);
#pragma unroll
  for (int i = 0; i < 4; ++i) uu[i] = ub[(size_t)(w * 64 + i * 16 + (lane >> 2)) * 1024 + 4 * j + (lane & 3)];
  const uint16_t* hrow = ssm_k + (size_t)(4 * j + w) * S;
#pragma unroll
  for (int i = 0; i < 4; ++i) {
    const int c = i * 64 + lane;
    hh[i] = *(const u32x4*)(hrow + c * 8);
    hn[i] = (c < 255) ? *(const unsigned*)(hrow + c * 8 + 8) : 0u;
  }
  const float Dc = bflo((unsigned)ssm_D[4 * j + w]);
  u32x4 x0v[4];
  f32x4 stv[4];
#pragma unroll
  for (int i = 0; i < 4; ++i) {
    const int p = tid + 256 * i;
    x0v[i] = ((const u32x4*)(ws + OFF_X0))[((size_t)(p >> 2) * 256 + j) * 4 + (p & 3)];
    stv[i] = ((const f32x4*)(ws + OFF_ST0))[p];
  }
  char* my = smem + w * CV_CH;
  if (lane < 30) *(u32x4*)(my + lane * 16) = (u32x4){0u, 0u, 0u, 0u};
  if (lane >= 32 && lane < 34) *(u32x4*)(my + CV_HA + (lane - 32) * 16) = (u32x4){0u, 0u, 0u, 0u};
  if (lane == 34) *(u32x4*)(my + CV_HB) = (u32x4){0u, 0u, 0u, 0u};
  if (lane == 0) *(u32x4*)(my + CV_HB + 16) = (u32x4){0u, 0u, 0u, hh[0][0] << 16};
#pragma unroll
  for (int i = 0; i < 4; ++i) {
    const int c = i * 64 + lane;
    *(u32x4*)(my + CV_HA + 32 + c * 16) = hh[i];
    u32x4 o = {__builtin_amdgcn_alignbyte(hh[i][1], hh[i][0], 2), __builtin_amdgcn_alignbyte(hh[i][2], hh[i][1], 2),
               __builtin_amdgcn_alignbyte(hh[i][3], hh[i][2], 2), __builtin_amdgcn_alignbyte(hn[i], hh[i][3], 2)};
    *(u32x4*)(my + CV_HB + 32 + c * 16) = o;
    *(u32x4*)(smem + (lane & 3) * CV_CH + (30 + w * 64 + i * 16 + (lane >> 2)) * 16) = uu[i];
  }
  __syncthreads();
  const int t0 = lane & 15, kq = lane >> 4;
  const unsigned* fb = (const unsigned*)(my + ((t0 & 1) ? CV_HA : CV_HB)) + 4 * kq + ((1 + t0) >> 1);
  const char* gb = my + (2 * t0 + 3 - kq) * 16;
  f32x4 acc[8];
#pragma unroll
  for (int b = 0; b < 8; ++b) acc[b] = (f32x4){0.f, 0.f, 0.f, 0.f};
#pragma unroll
  for (int g0 = 0; g0 < 8; ++g0) {
    bf16x8 F[8], G[8];
#pragma unroll
    for (int e1 = 0; e1 < 8; ++e1) {
      const unsigned* p = fb + 16 * (8 * e1 + g0);
      u32x4 f = {p[0], p[1], p[2], p[3]};
      F[e1] = as_frag(f);
      G[e1] = *(const bf16x8*)(gb + (32 * e1 + 4 * (7 - g0)) * 16);
    }
#pragma unroll
    for (int b = 0; b < 8; ++b)
#pragma unroll
      for (int g1 = 0; g1 <= b; ++g1) acc[b] = MFMA16(F[b - g1], G[g1], acc[b]);
  }
#pragma unroll
  for (int b = 0; b < 8; ++b) {
    const int s = 256 * b + 16 * t0 + 4 * kq;
    const u32x2 ur = *(const u32x2*)(my + (30 + (s >> 3)) * 16 + ((s & 4) ? 0 : 8));
    const float y0 = rbf(acc[b][0]) + rbf(bfhi(ur[1]) * Dc), y1 = rbf(acc[b][1]) + rbf(bflo(ur[1]) * Dc);
    const float y2 = rbf(acc[b][2]) + rbf(bfhi(ur[0]) * Dc), y3 = rbf(acc[b][3]) + rbf(bflo(ur[0]) * Dc);
    *(u32x2*)(my + CV_HA + s * 2) = (u32x2){pk2(y0, y1), pk2(y2, y3)};
  }
  __syncthreads();
#pragma unroll
  for (int i = 0; i < 4; ++i) {
    const int p = tid + 256 * i;
    unsigned yy[4];
#pragma unroll
    for (int c = 0; c < 4; ++c) yy[c] = *(const unsigned*)(smem + c * CV_CH + CV_HA + p * 4);
    const u32x4 yo = {__builtin_amdgcn_perm(yy[1], yy[0], 0x05040100u), __builtin_amdgcn_perm(yy[3], yy[2], 0x05040100u),
                      __builtin_amdgcn_perm(yy[1], yy[0], 0x07060302u), __builtin_amdgcn_perm(yy[3], yy[2], 0x07060302u)};
    ((u32x4*)(ws + OFF_Y))[(size_t)j * 1024 + p] = yo;
#pragma unroll
    for (int r = 0; r < 2; ++r) {
      const float m = stv[i][2 * r], rs = stv[i][2 * r + 1];
      float o[4];
#pragma unroll
      for (int c = 0; c < 4; ++c) {
        const unsigned xw = x0v[i][2 * r + (c >> 1)], yw = yo[2 * r + (c >> 1)];
        const float x = (c & 1) ? bfhi(xw) : bflo(xw), y = (c & 1) ? bfhi(yw) : bflo(yw);
        o[c] = x + (x - m) * rs * y;
      }
      *(u32x2*)(ws + OFF_X1 + ((size_t)(j >> 1) * S + 2 * p + r) * 16 + (j & 1) * 8) = (u32x2){pk2(o[0], o[1]), pk2(o[2], o[3])};
    }
  }
}

template <int MODE>
__global__ __launch_bounds__(256) void row_kernel(const u32x4* __restrict__ xin, const u32x2* __restrict__ y4,
                                                  u32x4* __restrict__ xout, f32x2* __restrict__ stats) {
  __shared__ float red[2][4][16];
  const int tid = threadIdx.x, lane = tid & 63, w = tid >> 6, row = tid & 7, og = tid >> 3, s = blockIdx.x * 8 + row;
  u32x4 x[4];
  u32x2 ya[4], yb[4];
#pragma unroll
  for (int i = 0; i < 4; ++i) {
    const int o = og + 32 * i;
    x[i] = xin[(size_t)o * S + s];
    if (MODE == 1) { ya[i] = y4[(size_t)(2 * o) * S + s]; yb[i] = y4[(size_t)(2 * o + 1) * S + s]; }
  }
  float sm = 0.f, sq = 0.f;
#pragma unroll
  for (int i = 0; i < 4; ++i)
#pragma unroll
    for (int c = 0; c < 4; ++c) { const float a = bflo(x[i][c]), b = bfhi(x[i][c]); sm += a + b; sq += a * a + b * b; }
#pragma unroll
  for (int m = 8; m < 64; m <<= 1) { sm += __shfl_xor(sm, m); sq += __shfl_xor(sq, m); }
  if (lane < 8) { red[0][w][2 * lane] = sm; red[0][w][2 * lane + 1] = sq; }
  __syncthreads();
  sm = (red[0][0][2 * row] + red[0][1][2 * row]) + (red[0][2][2 * row] + red[0][3][2 * row]);
  sq = (red[0][0][2 * row + 1] + red[0][1][2 * row + 1]) + (red[0][2][2 * row + 1] + red[0][3][2 * row + 1]);
  f32x2 st = ln_stats(sm, sq);
  if (MODE == 1) {
    sm = 0.f; sq = 0.f;
#pragma unroll
    for (int i = 0; i < 4; ++i) {
      u32x4 o;
#pragma unroll
      for (int c = 0; c < 4; ++c) {
        const unsigned yw = (c < 2) ? ya[i][c] : yb[i][c - 2];
        const float a = bflo(x[i][c]), b = bfhi(x[i][c]);
        o[c] = pk2(a + (a - st[0]) * st[1] * bflo(yw), b + (b - st[0]) * st[1] * bfhi(yw));
        const float a2 = bflo(o[c]), b2 = bfhi(o[c]);
        sm += a2 + b2; sq += a2 * a2 + b2 * b2;
      }
      xout[(size_t)(og + 32 * i) * S + s] = o;
    }
#pragma unroll
    for (int m = 8; m < 64; m <<= 1) { sm += __shfl_xor(sm, m); sq += __shfl_xor(sq, m); }
    if (lane < 8) { red[1][w][2 * lane] = sm; red[1][w][2 * lane + 1] = sq; }
    __syncthreads();
    sm = (red[1][0][2 * row] + red[1][1][2 * row]) + (red[1][2][2 * row] + red[1][3][2 * row]);
    sq = (red[1][0][2 * row + 1] + red[1][1][2 * row + 1]) + (red[1][2][2 * row + 1] + red[1][3][2 * row + 1]);
    st = ln_stats(sm, sq);
  }
  if (tid < 8) stats[s] = st;
}

constexpr int G1_STAGE = 40960, G1_NS = 4, G1_LDS = G1_STAGE * G1_NS;
#define WAIT_VM(n) asm volatile("s_waitcnt vmcnt(" #n ")" ::: "memory")
#define WAIT_LGKM0() asm volatile("s_waitcnt lgkmcnt(0)" ::: "memory")
#define BAR() __builtin_amdgcn_s_barrier()
DI void glds16(const void* g, char* l) {
  __builtin_amdgcn_global_load_lds((const __attribute__((address_space(1))) void*)g, (LAS void*)l, 16, 0, 0);
}
DI float phi(float t) {
  const float e = __builtin_amdgcn_fmed3f(__builtin_amdgcn_exp2f(t * 1.44269504088896f), 0.0f, 1.0f);
  return (fmaxf(t, 0.0f) + e) + 1e-6f;
}
__global__ __launch_bounds__(512, 2) void g1_kernel(const u32x4* __restrict__ X8, const u32x4* __restrict__ W8,
                                                    const float* __restrict__ csp, const f32x2* __restrict__ stats,
                                                    u32x4* __restrict__ pqf, float* __restrict__ kvacc) {
  extern __shared__ __attribute__((aligned(16))) char smem[];
  const int tid = threadIdx.x, lane = tid & 63, w = __builtin_amdgcn_readfirstlane(tid >> 6);
  const int xcd = blockIdx.x & 7, idx = blockIdx.x >> 3;
  const int band = (xcd & 1) * 8 + (idx & 7), hp = (xcd >> 1) * 4 + (idx >> 3);
  float* Rs = (float*)smem;
  float* MRs = Rs + 128;
  float* CSs = Rs + 256;
  if (w >= 4) {
    const int lw = w - 4;
    const u32x4* ga = X8 + (size_t)(2 * lw) * S + band * 128 + lane;
    const u32x4* gb = W8 + (size_t)hp * 128 * 192 + lw * 384 + lane;
    auto issue = [&](int st) {
      char* base = smem + (st % G1_NS) * G1_STAGE;
#pragma unroll
      for (int p = 0; p < 4; ++p) glds16(ga + (size_t)(st * 8 + (p >> 1)) * S + (p & 1) * 64, base + (4 * lw + p) * 1024);
#pragma unroll
      for (int p = 0; p < 6; ++p) glds16(gb + st * 1536 + p * 64, base + 16384 + (6 * lw + p) * 1024);
    };
#pragma unroll
    for (int st = 0; st < G1_NS; ++st) issue(st);
    WAIT_VM(30);
    BAR();
#pragma unroll
    for (int i = 0; i < 15; ++i) {
      if (i < 13) WAIT_VM(20); else if (i == 13) WAIT_VM(10); else WAIT_VM(0);
      BAR();
      if (i + G1_NS < 16) issue(i + G1_NS);
    }
    const int t = tid - 256;
    if (t < 128) { const f32x2 st = stats[band * 128 + t]; Rs[t] = st[1]; MRs[t] = st[0] * st[1]; }
    if (t < 192) {
      float c = 0.f;
#pragma unroll
      for (int g = 0; g < 8; ++g) c += csp[g * 3072 + hp * 192 + t];
      CSs[t] = c;
    }
    WAIT_LGKM0();
    BAR();
    return;
  }
  const int rh = w >> 1, hh = w & 1, r = lane & 31, hf = lane >> 5;
  const char* ab = smem + hf * 2048 + (64 * rh + r) * 16;
  const char* bb = smem + 16384 + hf * 3072 + (32 * hh + r) * 16;
  f32x16 acc[2][3];
#pragma unroll
  for (int m = 0; m < 2; ++m)
#pragma unroll
    for (int p = 0; p < 3; ++p)
#pragma unroll
      for (int i = 0; i < 16; ++i) acc[m][p][i] = 0.f;
  bf16x8 A[2][2], B[2][3];
  auto rd = [&](int buf, int st, int t) {
#pragma unroll
    for (int m = 0; m < 2; ++m) A[buf][m] = *(const bf16x8*)(ab + st * G1_STAGE + t * 4096 + m * 512);
#pragma unroll
    for (int p = 0; p < 3; ++p) B[buf][p] = *(const bf16x8*)(bb + st * G1_STAGE + t * 6144 + p * 1024);
  };
  auto mm = [&](int buf) {
#pragma unroll
    for (int m = 0; m < 2; ++m) {
      acc[m][0] = MFMA32(B[buf][0], A[buf][m], acc[m][0]);
      acc[m][1] = MFMA32(A[buf][m], B[buf][1], acc[m][1]);
      acc[m][2] = MFMA32(A[buf][m], B[buf][2], acc[m][2]);
    }
  };
  BAR();
  asm volatile("" ::: "memory");
  rd(0, 0, 0);
  for (int i4 = 0; i4 < 4; ++i4) {
#pragma unroll
    for (int st = 0; st < 4; ++st) {
      rd(1, st, 1); __builtin_amdgcn_sched_barrier(0); mm(0); __builtin_amdgcn_sched_barrier(0);
      rd(0, st, 2); __builtin_amdgcn_sched_barrier(0); mm(1); __builtin_amdgcn_sched_barrier(0);
      rd(1, st, 3); __builtin_amdgcn_sched_barrier(0); mm(0); __builtin_amdgcn_sched_barrier(0);
      WAIT_LGKM0();
      BAR();
      asm volatile("" ::: "memory");
      rd(0, (st + 1) & 3, 0);
      __builtin_amdgcn_sched_barrier(0); mm(1); __builtin_amdgcn_sched_barrier(0);
    }
  }
  const int h = hp * 2 + hh;
  {
    f32x4 cq[4];
#pragma unroll
    for (int g = 0; g < 4; ++g) cq[g] = *(const f32x4*)(CSs + 32 * hh + 8 * g + 4 * hf);
#pragma unroll
    for (int m = 0; m < 2; ++m) {
      const float rs = Rs[64 * rh + 32 * m + r], mr = MRs[64 * rh + 32 * m + r];
      float p[16];
#pragma unroll
      for (int i = 0; i < 16; ++i) p[i] = phi(acc[m][0][i] * rs - mr * cq[i >> 2][i & 3]);
      u32x4* dst = pqf + ((size_t)((band * 4 + rh * 2 + m) * 32 + h) * 2) * 64 + lane;
      dst[0] = (u32x4){pk2(p[0], p[1]), pk2(p[2], p[3]), pk2(p[4], p[5]), pk2(p[6], p[7])};
      dst[64] = (u32x4){pk2(p[8], p[9]), pk2(p[10], p[11]), pk2(p[12], p[13]), pk2(p[14], p[15])};
    }
  }
  f32x16 kv;
#pragma unroll
  for (int i = 0; i < 16; ++i) kv[i] = 0.f;
  float z = 0.f;
  {
    const float ck = CSs[64 + 32 * hh + r], cv = CSs[128 + 32 * hh + r];
#pragma unroll
    for (int m = 0; m < 2; ++m) {
      unsigned pkk[8], pvv[8];
#pragma unroll
      for (int g = 0; g < 4; ++g) {
        const f32x4 rs = *(const f32x4*)(Rs + 64 * rh + 32 * m + 8 * g + 4 * hf);
        const f32x4 mr = *(const f32x4*)(MRs + 64 * rh + 32 * m + 8 * g + 4 * hf);
        float a[4], b[4];
#pragma unroll
        for (int i = 0; i < 4; ++i) {
          a[i] = phi(acc[m][1][4 * g + i] * rs[i] - mr[i] * ck);
          b[i] = acc[m][2][4 * g + i] * rs[i] - mr[i] * cv;
        }
        pkk[2 * g] = pk2(a[0], a[1]); pkk[2 * g + 1] = pk2(a[2], a[3]);
        pvv[2 * g] = pk2(b[0], b[1]); pvv[2 * g + 1] = pk2(b[2], b[3]);
        z += (bflo(pkk[2 * g]) + bfhi(pkk[2 * g])) + (bflo(pkk[2 * g + 1]) + bfhi(pkk[2 * g + 1]));
      }
#pragma unroll
      for (int t = 0; t < 2; ++t)
        kv = MFMA32(as_frag((u32x4){pkk[4 * t], pkk[4 * t + 1], pkk[4 * t + 2], pkk[4 * t + 3]}),
                    as_frag((u32x4){pvv[4 * t], pvv[4 * t + 1], pvv[4 * t + 2], pvv[4 * t + 3]}), kv);
    }
  }
  float* P = (float*)(smem + G1_STAGE);
  {
    float* mine = P + (rh * 2 + hh) * 1088;
#pragma unroll
    for (int g = 0; g < 4; ++g) *(f32x4*)(mine + g * 256 + lane * 4) = (f32x4){kv[4 * g], kv[4 * g + 1], kv[4 * g + 2], kv[4 * g + 3]};
    mine[1024 + lane] = z;
  }
  WAIT_LGKM0();
  BAR();
  asm volatile("" ::: "memory");
#pragma unroll
  for (int i = 0; i < 8; ++i) {
    const int e = tid + 256 * (i & 3), hd = i >> 2;
    atomicAdd(kvacc + (hp * 2 + hd) * 1024 + e, P[hd * 1088 + e] + P[(2 + hd) * 1088 + e]);
  }
  if (tid < 64) {
    const int hd = tid >> 5, d = tid & 31;
    atomicAdd(kvacc + 32 * 1024 + (hp * 2 + hd) * 32 + d,
              (P[hd * 1088 + 1024 + d] + P[hd * 1088 + 1056 + d]) + (P[(2 + hd) * 1088 + 1024 + d] + P[(2 + hd) * 1088 + 1056 + d]));
  }
}

template <int FINAL>
__global__ __launch_bounds__(256, 1) void g2_kernel(const u32x4* __restrict__ pqf, const float* __restrict__ kvacc,
                                                    const u32x4* __restrict__ wof, const char* __restrict__ xres,
                                                    char* __restrict__ xout) {
  extern __shared__ __attribute__((aligned(16))) char smem[];
  const int tid = threadIdx.x, lane = tid & 63, w = __builtin_amdgcn_readfirstlane(tid >> 6);
  const int xcd = blockIdx.x & 7, idx = blockIdx.x >> 3;
  const int sb = (xcd >> 1) * 8 + (idx & 7), nb = (xcd & 1) * 4 + (idx >> 3);
  const int r = lane & 31, hf = lane >> 5;
  f32x16 acc[4][2];
#pragma unroll
  for (int n = 0; n < 4; ++n)
#pragma unroll
    for (int st = 0; st < 2; ++st)
#pragma unroll
      for (int i = 0; i < 16; ++i) acc[n][st][i] = 0.f;
  struct Ld { u32x4 pq[2][2]; f32x4 kv[4]; f32x4 z[4]; u32x4 wo[4][2]; };
  auto load = [&](Ld& L, int h) {
#pragma unroll
    for (int g = 0; g < 4; ++g) L.kv[g] = *(const f32x4*)(kvacc + h * 1024 + g * 256 + lane * 4);
#pragma unroll
    for (int st = 0; st < 2; ++st)
#pragma unroll
      for (int t = 0; t < 2; ++t) L.pq[st][t] = pqf[((size_t)((sb * 2 + st) * 32 + h) * 2 + t) * 64 + lane];
#pragma unroll
    for (int g = 0; g < 4; ++g) L.z[g] = *(const f32x4*)(kvacc + 32 * 1024 + h * 32 + 8 * g + 4 * hf);
#pragma unroll
    for (int n = 0; n < 4; ++n)
#pragma unroll
      for (int t = 0; t < 2; ++t) L.wo[n][t] = wof[((size_t)((nb * 4 + n) * 32 + h) * 2 + t) * 64 + lane];
  };
  auto comp = [&](const Ld& L) {
    bf16x8 kb[2];
#pragma unroll
    for (int t = 0; t < 2; ++t)
      kb[t] = as_frag((u32x4){pk2(L.kv[2 * t][0], L.kv[2 * t][1]), pk2(L.kv[2 * t][2], L.kv[2 * t][3]),
                              pk2(L.kv[2 * t + 1][0], L.kv[2 * t + 1][1]), pk2(L.kv[2 * t + 1][2], L.kv[2 * t + 1][3])});
    float zf[16];
#pragma unroll
    for (int i = 0; i < 16; ++i) zf[i] = rbf(L.z[i >> 2][i & 3]);
    bf16x8 ob[2][2];
#pragma unroll
    for (int st = 0; st < 2; ++st) {
      f32x16 x;
#pragma unroll
      for (int i = 0; i < 16; ++i) x[i] = 0.f;
      x = MFMA32(kb[0], as_frag(L.pq[st][0]), x);
      x = MFMA32(kb[1], as_frag(L.pq[st][1]), x);
      float den = 0.f;
#pragma unroll
      for (int t = 0; t < 2; ++t)
#pragma unroll
        for (int c = 0; c < 4; ++c) den += bflo(L.pq[st][t][c]) * zf[8 * t + 2 * c] + bfhi(L.pq[st][t][c]) * zf[8 * t + 2 * c + 1];
      den += __shfl_xor(den, 32);
      const float inv = 1.0f / rbf(rbf(den) + 1e-6f);
#pragma unroll
      for (int t = 0; t < 2; ++t)
        ob[st][t] = as_frag((u32x4){pk2(x[8 * t] * inv, x[8 * t + 1] * inv), pk2(x[8 * t + 2] * inv, x[8 * t + 3] * inv),
                                    pk2(x[8 * t + 4] * inv, x[8 * t + 5] * inv), pk2(x[8 * t + 6] * inv, x[8 * t + 7] * inv)});
    }
#pragma unroll
    for (int n = 0; n < 4; ++n)
#pragma unroll
      for (int st = 0; st < 2; ++st)
#pragma unroll
        for (int t = 0; t < 2; ++t) acc[n][st] = MFMA32(as_frag(L.wo[n][t]), ob[st][t], acc[n][st]);
  };
  Ld L0, L1, L2;
  const int h0 = 8 * w;
  load(L0, h0); load(L1, h0 + 1);
  load(L2, h0 + 2); __builtin_amdgcn_sched_barrier(0); comp(L0); __builtin_amdgcn_sched_barrier(0);
  load(L0, h0 + 3); __builtin_amdgcn_sched_barrier(0); comp(L1); __builtin_amdgcn_sched_barrier(0);
  load(L1, h0 + 4); __builtin_amdgcn_sched_barrier(0); comp(L2); __builtin_amdgcn_sched_barrier(0);
  load(L2, h0 + 5); __builtin_amdgcn_sched_barrier(0); comp(L0); __builtin_amdgcn_sched_barrier(0);
  load(L0, h0 + 6); __builtin_amdgcn_sched_barrier(0); comp(L1); __builtin_amdgcn_sched_barrier(0);
  load(L1, h0 + 7); __builtin_amdgcn_sched_barrier(0); comp(L2); __builtin_amdgcn_sched_barrier(0);
  const int n0 = nb * 128 + w * 32, s0 = sb * 64;
  u32x2 res[2][4];
#pragma unroll
  for (int st = 0; st < 2; ++st)
#pragma unroll
    for (int g = 0; g < 4; ++g) res[st][g] = *(const u32x2*)(xres + ((size_t)((n0 >> 3) + g) * S + s0 + 32 * st + r) * 16 + hf * 8);
  __builtin_amdgcn_sched_barrier(0);
  comp(L0); __builtin_amdgcn_sched_barrier(0);
  comp(L1);
  f32x4* P = (f32x4*)smem;
#pragma unroll
  for (int n = 0; n < 4; ++n) {
    if (n != w) {
      const int slot = (w - n - 1) & 3;
#pragma unroll
      for (int st = 0; st < 2; ++st)
#pragma unroll
        for (int g = 0; g < 4; ++g)
          P[(((n * 3 + slot) * 2 + st) * 4 + g) * 64 + lane] =
              (f32x4){acc[n][st][4 * g], acc[n][st][4 * g + 1], acc[n][st][4 * g + 2], acc[n][st][4 * g + 3]};
    }
  }
  __syncthreads();
#pragma unroll
  for (int st = 0; st < 2; ++st) {
    f32x16 mine = (w == 0) ? acc[0][st] : (w == 1) ? acc[1][st] : (w == 2) ? acc[2][st] : acc[3][st];
#pragma unroll
    for (int g = 0; g < 4; ++g) {
      f32x4 o = {mine[4 * g], mine[4 * g + 1], mine[4 * g + 2], mine[4 * g + 3]};
#pragma unroll
      for (int sl = 0; sl < 3; ++sl) o += P[(((w * 3 + sl) * 2 + st) * 4 + g) * 64 + lane];
      const u32x2 x = {pk2(bflo(res[st][g][0]) + rbf(o[0]), bfhi(res[st][g][0]) + rbf(o[1])),
                       pk2(bflo(res[st][g][1]) + rbf(o[2]), bfhi(res[st][g][1]) + rbf(o[3]))};
      if (FINAL) *(u32x2*)(xout + ((size_t)(s0 + 32 * st + r) * D + n0 + 8 * g + 4 * hf) * 2) = x;
      else *(u32x2*)(xout + ((size_t)((n0 >> 3) + g) * S + s0 + 32 * st + r) * 16 + hf * 8) = x;
    }
  }
}

extern "C" void kernel_launch(void* const* d_in, const int* in_sizes, int n_in, void* d_out, int out_size, void* d_ws,
                              size_t ws_size, hipStream_t stream) {
  const uint16_t* q = (const uint16_t*)d_in[0];
  const uint16_t* k = (const uint16_t*)d_in[1];
  const uint16_t* v = (const uint16_t*)d_in[2];
  const uint16_t* shift_k = (const uint16_t*)d_in[3];
  const uint16_t* ssm_k = (const uint16_t*)d_in[4];
  const uint16_t* ssm_D = (const uint16_t*)d_in[5];
  const uint16_t* wqkv = (const uint16_t*)d_in[7];
  const uint16_t* wo = (const uint16_t*)d_in[8];
  char* ws = (char*)d_ws;
  (void)hipFuncSetAttribute((const void*)g1_kernel, hipFuncAttributeMaxDynamicSharedMemorySize, G1_LDS);
  (void)hipFuncSetAttribute((const void*)g2_kernel<0>, hipFuncAttributeMaxDynamicSharedMemorySize, 98304);
  (void)hipFuncSetAttribute((const void*)g2_kernel<1>, hipFuncAttributeMaxDynamicSharedMemorySize, 98304);
  float* kv0 = (float*)(ws + OFF_KV);
  float* kv1 = kv0 + KV_LAYER;
  prep_kernel<<<512, 256, 0, stream>>>(q, k, v, shift_k, wqkv, wo, ws);
  conv_kernel<<<256, 256, 0, stream>>>(ssm_k, ssm_D, ws);
  row_kernel<0><<<256, 256, 0, stream>>>((const u32x4*)(ws + OFF_X1), nullptr, nullptr, (f32x2*)(ws + OFF_ST1));
  g1_kernel<<<256, 512, G1_LDS, stream>>>((const u32x4*)(ws + OFF_X1), (const u32x4*)(ws + OFF_W8), (const float*)(ws + OFF_CSP),
                                          (const f32x2*)(ws + OFF_ST1), (u32x4*)(ws + OFF_PQ), kv0);
  g2_kernel<0><<<256, 256, 98304, stream>>>((const u32x4*)(ws + OFF_PQ), kv0, (const u32x4*)(ws + OFF_WOF), ws + OFF_X1, ws + OFF_X2);
  row_kernel<1><<<256, 256, 0, stream>>>((const u32x4*)(ws + OFF_X2), (const u32x2*)(ws + OFF_Y), (u32x4*)(ws + OFF_X3),
                                         (f32x2*)(ws + OFF_ST3));
  g1_kernel<<<256, 512, G1_LDS, stream>>>((const u32x4*)(ws + OFF_X3), (const u32x4*)(ws + OFF_W8), (const float*)(ws + OFF_CSP),
                                          (const f32x2*)(ws + OFF_ST3), (u32x4*)(ws + OFF_PQ), kv1);
  g2_kernel<1><<<256, 256, 98304, stream>>>((const u32x4*)(ws + OFF_PQ), kv1, (const u32x4*)(ws + OFF_WOF), ws + OFF_X3, (char*)d_out);
}
```

```cpp
#include <hip/hip_runtime.h>
#include <stdint.h>

typedef __attribute__((ext_vector_type(8))) short bf16x8;
typedef __attribute__((ext_vector_type(4))) short s16x4;
typedef __attribute__((ext_vector_type(4))) float f32x4;
typedef __attribute__((ext_vector_type(2))) float f32x2;
typedef __attribute__((ext_vector_type(4))) unsigned u32x4;
typedef __attribute__((ext_vector_type(2))) unsigned u32x2;
typedef __attribute__((ext_vector_type(2))) __bf16 bf16x2_t;
#define DI __device__ __forceinline__
#define LAS __attribute__((address_space(3)))
#define STAMPW(kid, slot, wv) do {} while (0)
#define CSTAMP(kid, slot) do {} while (0)
#define DRAIN() do {} while (0)
#define STAMP(kid, slot) STAMPW(kid, slot, 0)

constexpr int S = 2048, D = 1024;
constexpr unsigned MB = 1024 * 1024;
constexpr unsigned OFF_UB = 0, OFF_X0 = 4 * MB, OFF_W8 = 8 * MB, OFF_WOF = 14 * MB, OFF_Y = 16 * MB, OFF_X1 = 20 * MB,
                   OFF_PQ = 24 * MB, OFF_X2 = 28 * MB, OFF_X3 = 32 * MB, OFF_SM = 36 * MB, WS_USED = 37 * MB;
constexpr unsigned OFF_ST0 = OFF_SM, OFF_CSP = OFF_SM + 16384,
                   OFF_KV = OFF_CSP + 16 * 3072 * 4,
                   OFF_GX = OFF_KV + 2 * 33792 * 4,
                   OFF_ST3 = OFF_GX + 65536;
constexpr int KV_LAYER = 32 * 1024 + 32 * 32;

DI float bflo(unsigned u) { return __uint_as_float(u << 16); }
DI float bfhi(unsigned u) { return __uint_as_float(u & 0xffff0000u); }
DI unsigned pk2(float a, float b) {
  f32x2 v = {a, b};
  bf16x2_t r = __builtin_convertvector(v, bf16x2_t);
  return __builtin_bit_cast(unsigned, r);
}
DI unsigned pk2v(f32x2 v) { return __builtin_bit_cast(unsigned, __builtin_convertvector(v, bf16x2_t)); }
DI float rbf(float a) { return bflo(pk2(a, a)); }
DI bf16x8 as_frag(u32x4 v) { return __builtin_bit_cast(bf16x8, v); }
DI float dot2(unsigned a, unsigned b, float c) {
  return __builtin_amdgcn_fdot2_f32_bf16(__builtin_bit_cast(bf16x2_t, a), __builtin_bit_cast(bf16x2_t, b), c, false);
}
#define MFMA16(a, b, c) __builtin_amdgcn_mfma_f32_16x16x32_bf16((a), (b), (c), 0, 0, 0)
#define WAIT_VM(n) asm volatile("s_waitcnt vmcnt(" #n ")" ::: "memory")
#define WAIT_LGKM0() asm volatile("s_waitcnt lgkmcnt(0)" ::: "memory")
#define BAR() do { __builtin_amdgcn_s_barrier(); asm volatile("" ::: "memory"); } while (0)
#define SB() __builtin_amdgcn_sched_barrier(0)
DI void glds16(const void* g, char* l) {
  __builtin_amdgcn_global_load_lds((const __attribute__((address_space(1))) void*)g, (LAS void*)l, 16, 0, 0);
}
typedef __amdgpu_buffer_rsrc_t rsrc_t;
DI rsrc_t mk_rsrc(void* p, unsigned bytes) { return __builtin_amdgcn_make_buffer_rsrc(p, 0, bytes, 0x00020000); }
DI void st16(rsrc_t r, unsigned off, u32x4 v) { __builtin_amdgcn_raw_buffer_store_b128(v, r, off, 0, 16); }

template <int N> DI float row_ror(float x) {
  return __int_as_float(__builtin_amdgcn_update_dpp(0, __float_as_int(x), 0x120 + N, 0xf, 0xf, false));
}
DI float sum_x16(float x) { const auto p = __builtin_amdgcn_permlane16_swap(__float_as_uint(x), __float_as_uint(x), false, false); return __uint_as_float(p[0]) + __uint_as_float(p[1]); }
DI float sum_x32(float x) { const auto p = __builtin_amdgcn_permlane32_swap(__float_as_uint(x), __float_as_uint(x), false, false); return __uint_as_float(p[0]) + __uint_as_float(p[1]); }

DI f32x2 ln_stats(float s, float q) {
  float m = s * (1.0f / 1024.0f);
  float v = fmaxf(q * (1.0f / 1024.0f) - m * m, 0.0f);
  float sd = rbf(sqrtf(rbf(rbf(v) + 1e-5f)));
  f32x2 r = {rbf(m), 1.0f / sd};
  return r;
}

__global__ __launch_bounds__(256) void prep_kernel(const uint16_t* __restrict__ q, const uint16_t* __restrict__ k,
                                                   const uint16_t* __restrict__ v, const uint16_t* __restrict__ shift_k,
                                                   char* __restrict__ ws) {
  __shared__ __attribute__((aligned(16))) u32x4 bounce[4][2][256];
  __shared__ float red[4][16];
  STAMP(0, 0);
  const int tid = threadIdx.x, lane = tid & 63, w = tid >> 6, bid = blockIdx.x;
  const int s0 = bid * 8;
  u32x2 kk[11], vv[8], qq[8];
#pragma unroll
  for (int r = 0; r < 8; ++r) qq[r] = *(const u32x2*)(q + (size_t)(s0 + r) * D + tid * 4);
#pragma unroll
  for (int r = 0; r < 11; ++r) {
    const int s = s0 - 3 + r;
    kk[r] = (s >= 0) ? *(const u32x2*)(k + (size_t)s * D + tid * 4) : (u32x2){0u, 0u};
  }
#pragma unroll
  for (int r = 0; r < 8; ++r) vv[r] = *(const u32x2*)(v + (size_t)(s0 + r) * D + tid * 4);
  const u32x4 wa = *(const u32x4*)(shift_k + tid * 16), wb = *(const u32x4*)(shift_k + tid * 16 + 8);
  const rsrc_t rw = mk_rsrc(ws, WS_USED);
  const int sw = (lane >> 1) & 3;
  if (tid < 16) st16(rw, OFF_GX + bid * 256 + tid * 16, (u32x4){~0u, ~0u, ~0u, ~0u});
  asm volatile("" :: "v"(qq[7]));
  STAMP(0, 1);
#pragma unroll
  for (int r = 0; r < 4; ++r) bounce[w][0][lane * 4 + (r ^ sw)] = (u32x4){qq[2 * r][0], qq[2 * r][1], qq[2 * r + 1][0], qq[2 * r + 1][1]};
  float a[16];
#pragma unroll
  for (int r = 0; r < 8; ++r) {
    a[2 * r] = dot2(qq[r][0], 0x3f803f80u, dot2(qq[r][1], 0x3f803f80u, 0.f));
    a[2 * r + 1] = dot2(qq[r][0], qq[r][0], dot2(qq[r][1], qq[r][1], 0.f));
  }
#pragma unroll
  for (int i = 0; i < 4; ++i) {
    const int u = 64 * i + lane;
    st16(rw, OFF_X0 + (bid * 1024 + w * 256 + u) * 16, bounce[w][0][(u & ~3) | ((u & 3) ^ ((u >> 3) & 3))]);
  }
#pragma unroll
  for (int st = 0; st < 4; ++st) {
    const int m = 1 << st, n = 8 >> st;
    const bool up = (lane & m) != 0;
#pragma unroll
    for (int i = 0; i < n; ++i) {
      const float keep = up ? a[2 * i + 1] : a[2 * i], send = up ? a[2 * i] : a[2 * i + 1];
      a[i] = keep + __shfl_xor(send, m);
    }
  }
  float t = a[0];
  t += __shfl_xor(t, 16);
  t += __shfl_xor(t, 32);
  if (lane < 16) red[w][lane] = t;
  STAMP(0, 2);
  asm volatile("" :: "v"(vv[7]), "v"(wb));
  STAMP(0, 3);
#pragma unroll
  for (int c = 0; c < 4; ++c) {
    const unsigned w01 = (c == 0) ? wa[0] : (c == 1) ? wa[2] : (c == 2) ? wb[0] : wb[2];
    const unsigned w23 = (c == 0) ? wa[1] : (c == 1) ? wa[3] : (c == 2) ? wb[1] : wb[3];
    const float w0 = bflo(w01), w1 = bfhi(w01), w2 = bflo(w23), w3 = bfhi(w23);
    float kf[11], u[8];
#pragma unroll
    for (int r = 0; r < 11; ++r) kf[r] = (c & 1) ? bfhi(kk[r][c >> 1]) : bflo(kk[r][c >> 1]);
#pragma unroll
    for (int r = 0; r < 8; ++r) {
      const float ks = kf[r] * w3 + kf[r + 1] * w2 + kf[r + 2] * w1 + kf[r + 3] * w0;
      u[r] = ks * ((c & 1) ? bfhi(vv[r][c >> 1]) : bflo(vv[r][c >> 1]));
    }
    bounce[w][1][lane * 4 + (c ^ sw)] = (u32x4){pk2(u[7], u[6]), pk2(u[5], u[4]), pk2(u[3], u[2]), pk2(u[1], u[0])};
  }
#pragma unroll
  for (int i = 0; i < 4; ++i) {
    const int u = 64 * i + lane;
    st16(rw, OFF_UB + (bid * 1024 + w * 256 + u) * 16, bounce[w][1][(u & ~3) | ((u & 3) ^ ((u >> 3) & 3))]);
  }
  __syncthreads();
  if (tid < 8) {
    const float sm = (red[0][2 * tid] + red[1][2 * tid]) + (red[2][2 * tid] + red[3][2 * tid]);
    const float sq = (red[0][2 * tid + 1] + red[1][2 * tid + 1]) + (red[2][2 * tid + 1] + red[3][2 * tid + 1]);
    ((f32x2*)(ws + OFF_ST0))[s0 + tid] = ln_stats(sm, sq);
  }
  STAMP(0, 6); DRAIN(); STAMP(0, 7);
}

constexpr int CV_U = 286 * 16;
constexpr int CV_HA = CV_U;
constexpr int CV_HB = CV_HA + 4160;
constexpr int CV_CH = CV_HB + 4128 + 32;
constexpr int CV_PAD = 64;
constexpr int CV_LDS = CV_PAD + 4 * CV_CH + 4 * 8192;
__global__ __launch_bounds__(512, 2) void conv_kernel(const uint16_t* __restrict__ ssm_k, const uint16_t* __restrict__ ssm_D,
                                                      const uint16_t* __restrict__ wqkv, const uint16_t* __restrict__ wo,
                                                      char* __restrict__ ws) {
  extern __shared__ __attribute__((aligned(16))) char smem_raw[];
  char* smem = smem_raw + CV_PAD;
  STAMP(1, 0);
  const int tid = threadIdx.x, lane = tid & 63, w = __builtin_amdgcn_readfirstlane(tid >> 6);
  const rsrc_t rw = mk_rsrc(ws, WS_USED);
  if (w >= 4) {
    const int gw = blockIdx.x * 4 + (w - 4);
    char* buf = smem + 4 * CV_CH + (w - 4) * 8192;
    const int g = lane >> 4, qq = (lane >> 2) & 3, p = lane & 3;
    if (lane < 17) { if (lane < 16 || (gw & 1) == 0) { const unsigned u = gw * 33 / 2 + ((gw & 1) ? 1 : 0) + lane; st16(rw, OFF_KV + u * 16, (u32x4){0u, 0u, 0u, 0u}); } }
    if (gw < 768) {
      const int cg = gw % 48, og = gw / 48;
      const uint16_t* src = wqkv + (size_t)(og * 64 + (lane >> 3)) * 3072 + cg * 64 + (lane & 7) * 8;
#pragma unroll
      for (int i = 0; i < 8; ++i) glds16(src + (size_t)i * 8 * 3072, buf + i * 1024);
      BAR();
      WAIT_VM(0);
      const unsigned dst = OFF_W8 + (((cg & 15) * 128 + og * 8) * 192 + (cg >> 4) * 64 + lane) * 16;
      float cs = 0.f;
#pragma unroll
      for (int i = 0; i < 8; ++i) {
        const char* a = buf + i * 1024 + qq * 128 + g * 32 + p * 8;
        const u32x2 lo = __builtin_bit_cast(u32x2, __builtin_amdgcn_ds_read_tr16_b64_v4i16((LAS s16x4*)a));
        const u32x2 hi = __builtin_bit_cast(u32x2, __builtin_amdgcn_ds_read_tr16_b64_v4i16((LAS s16x4*)(a + 512)));
        st16(rw, dst + i * 3072, (u32x4){lo[0], lo[1], hi[0], hi[1]});
        cs = dot2(lo[0], 0x3f803f80u, dot2(lo[1], 0x3f803f80u, dot2(hi[0], 0x3f803f80u, dot2(hi[1], 0x3f803f80u, cs))));
      }
      ((float*)(ws + OFF_CSP))[og * 3072 + (cg & 15) * 192 + (cg >> 4) * 64 + lane] = cs;
    } else {
      const int cg = (gw - 768) & 15, og = (gw - 768) >> 4;
      const int rr = lane >> 3;
      const uint16_t* src = wo + (size_t)(og * 64 + (rr & 3) + 16 * (rr >> 2)) * 1024 + cg * 64 + (lane & 7) * 8;
#pragma unroll
      for (int i = 0; i < 8; ++i) glds16(src + (size_t)((i >> 2) * 32 + (i & 3) * 4) * 1024, buf + i * 1024);
      BAR();
      WAIT_VM(0);
      const unsigned dst = OFF_WOF + ((((cg * 2 + (g >> 1)) * 32 + og * 2) * 2 + (g & 1)) * 64 + (lane & 15)) * 16;
#pragma unroll
      for (int i = 0; i < 8; ++i) {
        const char* a = buf + i * 1024 + qq * 128 + (g >> 1) * 64 + p * 16 + (g & 1) * 8;
        const u32x2 lo = __builtin_bit_cast(u32x2, __builtin_amdgcn_ds_read_tr16_b64_v4i16((LAS s16x4*)a));
        const u32x2 hi = __builtin_bit_cast(u32x2, __builtin_amdgcn_ds_read_tr16_b64_v4i16((LAS s16x4*)(a + 512)));
        st16(rw, dst + (i >> 2) * 2048 + (i & 3) * 256, (u32x4){lo[0], lo[1], hi[0], hi[1]});
      }
    }
    STAMPW(1, 4, 4); DRAIN(); STAMPW(1, 5, 4);
    return;
  }
  const int j = (blockIdx.x & 7) * 32 + (blockIdx.x >> 3);
  u32x4 uu[4], hh[4];
  unsigned hn[4];
  const uint16_t* hrow = ssm_k + (size_t)(4 * j + w) * S;
#pragma unroll
  for (int i = 0; i < 4; ++i) {
    const int c = i * 64 + lane;
    hh[i] = *(const u32x4*)(hrow + c * 8);
    hn[i] = (c < 255) ? *(const unsigned*)(hrow + c * 8 + 8) : 0u;
  }
  const u32x4* ub = (const u32x4*)(ws + OFF_UB);
#pragma unroll
  for (int i = 0; i < 4; ++i) uu[i] = ub[(size_t)(w * 64 + i * 16 + (lane >> 2)) * 1024 + 4 * j + (lane & 3)];
  const float Dc = bflo((unsigned)ssm_D[4 * j + w]);
  u32x4 x0v[4];
  f32x4 stv[4];
#pragma unroll
  for (int i = 0; i < 4; ++i) {
    const int p = tid + 256 * i;
    x0v[i] = ((const u32x4*)(ws + OFF_X0))[((size_t)(p >> 2) * 256 + j) * 4 + (p & 3)];
    stv[i] = ((const f32x4*)(ws + OFF_ST0))[p];
  }
  char* my = smem + w * CV_CH;
  if (lane < 30) *(u32x4*)(my + lane * 16) = (u32x4){0u, 0u, 0u, 0u};
  if (lane >= 32 && lane < 34) *(u32x4*)(my + CV_HA + (lane - 32) * 16) = (u32x4){0u, 0u, 0u, 0u};
  if (lane == 34) *(u32x4*)(my + CV_HB) = (u32x4){0u, 0u, 0u, 0u};
  if (lane == 0) *(u32x4*)(my + CV_HB + 16) = (u32x4){0u, 0u, 0u, hh[0][0] << 16};
#pragma unroll
  for (int i = 0; i < 4; ++i) {
    const int c = i * 64 + lane;
    *(u32x4*)(my + CV_HA + 32 + c * 16) = hh[i];
    u32x4 o = {__builtin_amdgcn_alignbyte(hh[i][1], hh[i][0], 2), __builtin_amdgcn_alignbyte(hh[i][2], hh[i][1], 2),
               __builtin_amdgcn_alignbyte(hh[i][3], hh[i][2], 2), __builtin_amdgcn_alignbyte(hn[i], hh[i][3], 2)};
    *(u32x4*)(my + CV_HB + 32 + c * 16) = o;
  }
#pragma unroll
  for (int i = 0; i < 4; ++i) *(u32x4*)(smem + (lane & 3) * CV_CH + (30 + w * 64 + i * 16 + (lane >> 2)) * 16) = uu[i];
  const int t0 = lane & 15, kq = lane >> 4;
  const unsigned* fb = (const unsigned*)(my + ((t0 & 1) ? CV_HA : CV_HB)) + 4 * kq + ((1 + t0) >> 1);
  const char* gb = my + (2 * t0 + 3 - kq) * 16;
  f32x4 acc[8];
#pragma unroll
  for (int b = 0; b < 8; ++b) { acc[b] = (f32x4){0.f, 0.f, 0.f, 0.f}; asm volatile("" : "+v"(acc[b])); }
  WAIT_LGKM0();
  BAR();
  STAMP(1, 1); CSTAMP(9, 1); STAMP(9, 0);
  bf16x8 F[2][8], G[2][8];
  const LAS unsigned* fbk[4];
#pragma unroll
  for (int i = 0; i < 4; ++i) { fbk[i] = (const LAS unsigned*)fb + 256 * i; asm volatile("" : "+v"(fbk[i])); }
  u32x2 fh[8];
  auto rdn = [&](int buf, int g0, int n) {
    if (n == 0 || n > 16) { const int g1 = n ? n - 16 : 0; G[buf][g1] = *(const bf16x8*)(gb + (32 * g1 + 4 * (7 - g0)) * 16); }
    else {
      const int e1 = (n - 1) >> 1;
      const LAS unsigned* p = fbk[e1 >> 1] + 128 * (e1 & 1) + 16 * g0;
      if ((n - 1) & 1) F[buf][e1] = as_frag((u32x4){fh[e1][0], fh[e1][1], p[2], p[3]});
      else fh[e1] = (u32x2){p[0], p[1]};
    }
  };
  auto cstep = [&](int cb, int g0n) {
    int n = 0;
#pragma unroll
    for (int g1 = 0; g1 < 8; ++g1)
#pragma unroll
      for (int e1 = 0; e1 + g1 < 8; ++e1) {
        acc[g1 + e1] = MFMA16(F[cb][e1], G[cb][g1], acc[g1 + e1]);
        SB();
        if (n < 24) { rdn(cb ^ 1, g0n, n); SB(); }
        ++n;
      }
  };
#pragma unroll
  for (int n = 0; n < 24; ++n) rdn(0, 0, n);
  for (int gp = 0; gp < 4; ++gp) {
    cstep(0, 1);
    cstep(1, 2);
    gb -= 128;
#pragma unroll
    for (int i = 0; i < 4; ++i) fbk[i] += 32;
  }
  asm volatile("" ::"v"(acc[7]), "v"(acc[0]));
  STAMP(1, 2); CSTAMP(9, 2); STAMP(9, 3);
#pragma unroll
  for (int b = 0; b < 8; ++b) {
    const int s = 256 * b + 16 * t0 + 4 * kq;
    const u32x2 ur = *(const u32x2*)(my + (30 + (s >> 3)) * 16 + ((s & 4) ? 0 : 8));
    *(u32x2*)(my + CV_HA + s * 2) = (u32x2){pk2(acc[b][0] + bfhi(ur[1]) * Dc, acc[b][1] + bflo(ur[1]) * Dc),
                                            pk2(acc[b][2] + bfhi(ur[0]) * Dc, acc[b][3] + bflo(ur[0]) * Dc)};
  }
  WAIT_LGKM0();
  BAR();
#pragma unroll
  for (int i = 0; i < 4; ++i) {
    const int p = tid + 256 * i;
    unsigned yy[4];
#pragma unroll
    for (int c = 0; c < 4; ++c) yy[c] = *(const unsigned*)(smem + c * CV_CH + CV_HA + p * 4);
    const u32x4 yo = {__builtin_amdgcn_perm(yy[1], yy[0], 0x05040100u), __builtin_amdgcn_perm(yy[3], yy[2], 0x05040100u),
                      __builtin_amdgcn_perm(yy[1], yy[0], 0x07060302u), __builtin_amdgcn_perm(yy[3], yy[2], 0x07060302u)};
    u32x4 xo;
#pragma unroll
    for (int c = 0; c < 4; ++c) {
      const float m = stv[i][c & 2], rs = stv[i][(c & 2) + 1];
      const float xa = bflo(x0v[i][c]), xb = bfhi(x0v[i][c]);
      xo[c] = pk2(xa + (xa - m) * rs * bflo(yo[c]), xb + (xb - m) * rs * bfhi(yo[c]));
    }
    st16(rw, OFF_X1 + (j * 1024 + p) * 16, xo);
    st16(rw, OFF_Y + (j * 1024 + p) * 16, yo);
  }
  STAMP(1, 6); DRAIN(); STAMP(1, 7);
}

__global__ __launch_bounds__(256) void row_kernel(char* __restrict__ ws) {
  __shared__ float red[4][16], red2[4][16];
  STAMP(4, 0);
  const int tid = threadIdx.x, lane = tid & 63, w = tid >> 6, row = tid & 7, og = tid >> 3, s = blockIdx.x * 8 + row;
  const u32x4* xin = (const u32x4*)(ws + OFF_X2);
  const u32x2* y4 = (const u32x2*)(ws + OFF_Y);
  const rsrc_t rw = mk_rsrc(ws, WS_USED);
  u32x4 x[4];
  u32x2 ya[4], yb[4];
#pragma unroll
  for (int i = 0; i < 4; ++i) x[i] = xin[(size_t)(og + 32 * i) * S + s];
#pragma unroll
  for (int i = 0; i < 4; ++i) { const int o = og + 32 * i; ya[i] = y4[(size_t)(2 * o) * S + s]; yb[i] = y4[(size_t)(2 * o + 1) * S + s]; }
  float sm = 0.f, sq = 0.f;
#pragma unroll
  for (int i = 0; i < 4; ++i)
#pragma unroll
    for (int c = 0; c < 4; ++c) { sm = dot2(x[i][c], 0x3f803f80u, sm); sq = dot2(x[i][c], x[i][c], sq); }
#pragma unroll
  for (int m = 8; m < 64; m <<= 1) { sm += __shfl_xor(sm, m); sq += __shfl_xor(sq, m); }
  if (lane < 8) { red[w][2 * lane] = sm; red[w][2 * lane + 1] = sq; }
  __syncthreads();
  sm = (red[0][2 * row] + red[1][2 * row]) + (red[2][2 * row] + red[3][2 * row]);
  sq = (red[0][2 * row + 1] + red[1][2 * row + 1]) + (red[2][2 * row + 1] + red[3][2 * row + 1]);
  const f32x2 st = ln_stats(sm, sq);
  sm = 0.f; sq = 0.f;
#pragma unroll
  for (int i = 0; i < 4; ++i) {
    u32x4 o;
#pragma unroll
    for (int c = 0; c < 4; ++c) {
      const unsigned yw = (c < 2) ? ya[i][c] : yb[i][c - 2];
      const float a = bflo(x[i][c]), b = bfhi(x[i][c]);
      o[c] = pk2(a + (a - st[0]) * st[1] * bflo(yw), b + (b - st[0]) * st[1] * bfhi(yw));
    }
    st16(rw, OFF_X3 + ((og + 32 * i) * S + s) * 16, o);
#pragma unroll
    for (int c = 0; c < 4; ++c) { sm = dot2(o[c], 0x3f803f80u, sm); sq = dot2(o[c], o[c], sq); }
  }
#pragma unroll
  for (int m = 8; m < 64; m <<= 1) { sm += __shfl_xor(sm, m); sq += __shfl_xor(sq, m); }
  if (lane < 8) { red2[w][2 * lane] = sm; red2[w][2 * lane + 1] = sq; }
  __syncthreads();
  if (tid < 8) {
    sm = (red2[0][2 * row] + red2[1][2 * row]) + (red2[2][2 * row] + red2[3][2 * row]);
    sq = (red2[0][2 * row + 1] + red2[1][2 * row + 1]) + (red2[2][2 * row + 1] + red2[3][2 * row + 1]);
    ((f32x2*)(ws + OFF_ST3))[s] = (f32x2){sm, sq};
  }
  STAMP(4, 6); DRAIN(); STAMP(4, 7);
}

constexpr int G1_STAGE = 40960, G1_NS = 4, G1_LDS = G1_STAGE * G1_NS;
constexpr float LOG2E = 1.44269504088896f;
DI f32x2 phi2(f32x2 t2) {
  const f32x2 a = t2 * LOG2E - 1e-6f * LOG2E;
  f32x2 e, m;
  e[0] = __builtin_amdgcn_fmed3f(__builtin_amdgcn_exp2f(a[0]), 0.0f, 1.0f);
  e[1] = __builtin_amdgcn_fmed3f(__builtin_amdgcn_exp2f(a[1]), 0.0f, 1.0f);
  m[0] = fmaxf(t2[0], 1e-6f);
  m[1] = fmaxf(t2[1], 1e-6f);
  return m + e;
}
DI f32x2 lo2(f32x4 v) { return (f32x2){v[0], v[1]}; }
DI f32x2 hi2(f32x4 v) { return (f32x2){v[2], v[3]}; }
template <int C4>
__global__ __launch_bounds__(512, 2) void g1_kernel(char* __restrict__ ws, unsigned off_x, float* __restrict__ kvacc) {
  extern __shared__ __attribute__((aligned(16))) char smem[];
  constexpr int kid = C4 ? 2 : 5;
  STAMP(kid, 0);
  const int tid = threadIdx.x, lane = tid & 63, w = __builtin_amdgcn_readfirstlane(tid >> 6);
  const int xcd = blockIdx.x & 7, idx = blockIdx.x >> 3;
  const int band = (xcd & 1) * 8 + (idx & 7), hp = (xcd >> 1) * 4 + (idx >> 3);
  float* Rs = (float*)smem;
  float* MRs = Rs + 128;
  float* CSs = Rs + 256;
  if (w >= 4) {
    const int lw = w - 4;
    const u32x4* X = (const u32x4*)(ws + off_x);
    const u32x4* ga0 = C4 ? X + (size_t)(4 * lw) * 1024 + band * 64 + lane : X + (size_t)(2 * lw) * S + band * 128 + lane;
    const u32x4* ga1 = C4 ? X + (size_t)(4 * lw) * 1024 + band * 64 + (lane ^ 8) : ga0;
    const u32x4* gb = (const u32x4*)(ws + OFF_W8) + (size_t)hp * 128 * 192 + lw * 384 + lane;
    auto issue = [&](int st) {
      char* base = smem + (st % G1_NS) * G1_STAGE;
#pragma unroll
      for (int p = 0; p < 4; ++p) {
        if (C4) glds16(((p & 2) ? ga1 : ga0) + (size_t)(st * 16 + p) * 1024, base + (4 * lw + p) * 1024);
        else glds16(ga0 + (size_t)(st * 8 + (p >> 1)) * S + (p & 1) * 64, base + (4 * lw + p) * 1024);
      }
#pragma unroll
      for (int p = 0; p < 6; ++p) glds16(gb + st * 1536 + p * 64, base + 16384 + (6 * lw + p) * 1024);
    };
    issue(0); issue(1);
    WAIT_VM(10);
    BAR();
    issue(2); issue(3);
#pragma unroll
    for (int i = 0; i < 13; ++i) {
      WAIT_VM(20);
      BAR();
      if (i + G1_NS < 16) issue(i + G1_NS);
    }
    const int t = tid - 256;
    if (lw < 2) {
      const char* gp = ws + (C4 ? OFF_GX + (band * 128 + t) * 32 : OFF_ST3 + (band * 128 + t) * 8);
      u32x2 g0, g1, g2, g3;
      if (C4) {
        asm volatile("global_load_dwordx2 %0, %4, off sc1\n\tglobal_load_dwordx2 %1, %4, off offset:8 sc1\n\t"
                     "global_load_dwordx2 %2, %4, off offset:16 sc1\n\tglobal_load_dwordx2 %3, %4, off offset:24 sc1"
                     : "=&v"(g0), "=&v"(g1), "=&v"(g2), "=&v"(g3) : "v"(gp) : "memory");
        WAIT_VM(14);
      } else {
        asm volatile("global_load_dwordx2 %0, %1, off" : "=&v"(g0) : "v"(gp) : "memory");
        WAIT_VM(11);
      }
      BAR();
      if (C4) asm volatile("s_waitcnt vmcnt(0)" : "+v"(g0), "+v"(g1), "+v"(g2), "+v"(g3)::"memory");
      else asm volatile("s_waitcnt vmcnt(0)" : "+v"(g0)::"memory");
      BAR();
      float sm, sq;
      if (C4) {
#pragma unroll 1
        for (int spin = 0; spin < (1 << 20); ++spin) {
          if (!__any(g0[1] == ~0u || g1[1] == ~0u || g2[1] == ~0u || g3[1] == ~0u)) break;
          __builtin_amdgcn_s_sleep(4);
          const unsigned long long* q = (const unsigned long long*)gp;
          g0 = __builtin_bit_cast(u32x2, __hip_atomic_load(q, __ATOMIC_RELAXED, __HIP_MEMORY_SCOPE_AGENT));
          g1 = __builtin_bit_cast(u32x2, __hip_atomic_load(q + 1, __ATOMIC_RELAXED, __HIP_MEMORY_SCOPE_AGENT));
          g2 = __builtin_bit_cast(u32x2, __hip_atomic_load(q + 2, __ATOMIC_RELAXED, __HIP_MEMORY_SCOPE_AGENT));
          g3 = __builtin_bit_cast(u32x2, __hip_atomic_load(q + 3, __ATOMIC_RELAXED, __HIP_MEMORY_SCOPE_AGENT));
        }
        sm = (__uint_as_float(g0[0]) + __uint_as_float(g1[0])) + (__uint_as_float(g2[0]) + __uint_as_float(g3[0]));
        sq = (__uint_as_float(g0[1]) + __uint_as_float(g1[1])) + (__uint_as_float(g2[1]) + __uint_as_float(g3[1]));
      } else { sm = __uint_as_float(g0[0]); sq = __uint_as_float(g0[1]); }
      const f32x2 st = ln_stats(sm, sq);
      Rs[t] = st[1]; MRs[t] = -st[0] * st[1];
      WAIT_LGKM0();
    } else {
      WAIT_VM(10);
      BAR();
      WAIT_VM(0);
      BAR();
    }
    BAR();
    return;
  }
  const int rh = w >> 1, hh = w & 1, r = lane & 15, kq = lane >> 4;
  const char* ab = C4 ? smem + kq * 2048 + ((64 * rh + r) ^ (16 * (kq & 1))) * 8 : smem + kq * 2048 + (64 * rh + r) * 16;
  const char* bb = smem + 16384 + kq * 3072 + (32 * hh + r) * 16;
  f32x4 acc[4][6];
#pragma unroll
  for (int m = 0; m < 4; ++m)
#pragma unroll
    for (int p = 0; p < 6; ++p) { acc[m][p] = (f32x4){0.f, 0.f, 0.f, 0.f}; asm volatile("" : "+v"(acc[m][p])); }
  bf16x8 F[2][10];
  const int msw = C4 ? (kq & 1) * 256 : 0;
  auto rd1 = [&](int buf, const char* a, const char* a2, const char* b, int t, int i) {
    if (i < 4) {
      if (C4) {
        const char* p = ((i & 1) ? a2 : a) + t * 8192 + (i >> 1) * 256;
        const u32x2 lo = *(const u32x2*)p, hi = *(const u32x2*)(p + 1024);
        F[buf][i] = as_frag((u32x4){lo[0], lo[1], hi[0], hi[1]});
      } else F[buf][i] = *(const bf16x8*)(a + t * 8192 + i * 256);
    } else F[buf][i] = *(const bf16x8*)(b + t * 12288 + ((i - 4) >> 1) * 1024 + ((i - 4) & 1) * 256);
  };
  auto step = [&](int cb, const char* a, const char* a2, const char* b, int t, bool rd) {
    const int ord[10] = {0, 4, 5, 6, 7, 8, 9, 1, 2, 3};
#pragma unroll
    for (int i = 0; i < 24; ++i) {
      const int m = i / 6, p = i % 6;
      if (p < 2) acc[m][p] = MFMA16(F[cb][4 + p], F[cb][m], acc[m][p]);
      else acc[m][p] = MFMA16(F[cb][m], F[cb][4 + p], acc[m][p]);
      SB();
      if (rd && (i & 1) && (i >> 1) < 10) { rd1(cb ^ 1, a, a2, b, t, ord[i >> 1]); SB(); }
    }
  };
  float csum = 0.f;
  {
    u32x4 xv[4];
    float cp[16];
    if (C4) {
      const u32x4* X = (const u32x4*)(ws + off_x) + (size_t)(tid >> 2) * 1024 + band * 64 + hp * 4 + (tid & 3);
#pragma unroll
      for (int i = 0; i < 4; ++i) xv[i] = X[(size_t)i * 65536];
    }
    const float* csp = (const float*)(ws + OFF_CSP) + hp * 192 + (tid < 192 ? tid : 0);
#pragma unroll
    for (int g = 0; g < 16; ++g) cp[g] = csp[g * 3072];
    SB();
#pragma unroll
    for (int g = 0; g < 16; ++g) csum += cp[g];
    asm volatile("" : "+v"(csum));
    if (C4) {
      float v[4] = {0.f, 0.f, 0.f, 0.f};
#pragma unroll
      for (int i = 0; i < 4; ++i) {
        v[0] = dot2(xv[i][0], 0x3f803f80u, dot2(xv[i][1], 0x3f803f80u, v[0])); v[1] = dot2(xv[i][0], xv[i][0], dot2(xv[i][1], xv[i][1], v[1]));
        v[2] = dot2(xv[i][2], 0x3f803f80u, dot2(xv[i][3], 0x3f803f80u, v[2])); v[3] = dot2(xv[i][2], xv[i][2], dot2(xv[i][3], xv[i][3], v[3]));
      }
#pragma unroll
      for (int i = 0; i < 4; ++i) { v[i] += row_ror<4>(v[i]); v[i] += row_ror<8>(v[i]); v[i] = sum_x32(sum_x16(v[i])); }
      if (lane < 8) {
        const bool up = (lane & 4) != 0;
        unsigned qb = __float_as_uint(up ? v[3] : v[1]);
        if (qb == ~0u) qb = 0x7fc00000u;
        const int row = band * 128 + hp * 8 + 2 * (lane & 3) + (lane >> 2);
        __hip_atomic_store((unsigned long long*)(ws + OFF_GX) + row * 4 + w,
                           ((unsigned long long)qb << 32) | __float_as_uint(up ? v[2] : v[0]), __ATOMIC_RELAXED, __HIP_MEMORY_SCOPE_AGENT);
      }
    }
  }
  asm volatile("" : "+v"(csum));
  const char* ab2 = ab + 128 - msw;
  BAR();
  STAMP(kid, 1); CSTAMP(kid + 8, 1); STAMP(kid + 8, 0);
#pragma unroll
  for (int i = 0; i < 10; ++i) rd1(0, ab, ab2, bb, 0, i);
  int so = 0;
  for (int it = 0; it < 15; ++it) {
    step(0, ab + so, ab2 + so, bb + so, 1, true);
    WAIT_LGKM0();
    BAR();
    so = (so == 3 * G1_STAGE) ? 0 : so + G1_STAGE;
    step(1, ab + so, ab2 + so, bb + so, 0, true);
  }
  if (tid < 192) CSs[tid] = csum;
  step(0, ab + so, ab2 + so, bb + so, 1, true);
  WAIT_LGKM0();
  BAR();
  step(1, ab, ab2, bb, 0, false);
  STAMP(kid, 2); CSTAMP(kid + 8, 2); STAMP(kid + 8, 3);
  const int h = hp * 2 + hh;
  f32x4 Z[2][2], Zz[2];
#pragma unroll
  for (int i = 0; i < 2; ++i) { Z[i][0] = (f32x4){0.f, 0.f, 0.f, 0.f}; Z[i][1] = Z[i][0]; Zz[i] = Z[i][0]; }
  {
    float ck[4];
#pragma unroll
    for (int p = 0; p < 4; ++p) ck[p] = CSs[64 + (p >> 1) * 64 + 32 * hh + (p & 1) * 16 + r];
    const bf16x8 ones = as_frag((u32x4){0x3f803f80u, 0x3f803f80u, 0x3f803f80u, 0x3f803f80u});
#pragma unroll
    for (int jj = 0; jj < 2; ++jj) {
      u32x4 fr[4];
#pragma unroll
      for (int mm = 0; mm < 2; ++mm) {
        const int m = 2 * jj + mm;
        const f32x4 rs = *(const f32x4*)(Rs + 64 * rh + 16 * m + 4 * kq), mr = *(const f32x4*)(MRs + 64 * rh + 16 * m + 4 * kq);
#pragma unroll
        for (int p = 0; p < 2; ++p) {
          fr[p][2 * mm] = pk2v(phi2(lo2(acc[m][2 + p]) * lo2(rs) + (lo2(mr) * ck[p] + 1e-6f)));
          fr[p][2 * mm + 1] = pk2v(phi2(hi2(acc[m][2 + p]) * hi2(rs) + (hi2(mr) * ck[p] + 1e-6f)));
          fr[2 + p][2 * mm] = pk2v(lo2(acc[m][4 + p]) * lo2(rs) + lo2(mr) * ck[2 + p]);
          fr[2 + p][2 * mm + 1] = pk2v(hi2(acc[m][4 + p]) * hi2(rs) + hi2(mr) * ck[2 + p]);
        }
      }
#pragma unroll
      for (int dh = 0; dh < 2; ++dh) {
        Z[0][dh] = MFMA16(as_frag(fr[dh]), as_frag(fr[2]), Z[0][dh]);
        Z[1][dh] = MFMA16(as_frag(fr[dh]), as_frag(fr[3]), Z[1][dh]);
        Zz[dh] = MFMA16(as_frag(fr[dh]), ones, Zz[dh]);
      }
    }
  }
  float* P = (float*)(smem + G1_STAGE);
  {
    float* mine = P + (rh * 2 + hh) * 1056;
#pragma unroll
    for (int i = 0; i < 4; ++i) *(f32x4*)(mine + i * 256 + lane * 4) = Z[i >> 1][i & 1];
    if (r == 0) { *(f32x4*)(mine + 1024 + 4 * kq) = Zz[0]; *(f32x4*)(mine + 1040 + 4 * kq) = Zz[1]; }
  }
  WAIT_LGKM0();
  BAR();
  STAMP(kid, 3);
#pragma unroll
  for (int i = 0; i < 8; ++i) {
    const int e = tid + 256 * (i & 3), hd = i >> 2;
    atomicAdd(kvacc + (hp * 2 + hd) * 1024 + e, P[hd * 1056 + e] + P[(2 + hd) * 1056 + e]);
  }
  if (tid < 64) {
    const int hd = tid >> 5, d = tid & 31;
    atomicAdd(kvacc + 32 * 1024 + (hp * 2 + hd) * 32 + d, P[hd * 1056 + 1024 + d] + P[(2 + hd) * 1056 + 1024 + d]);
  }
  SB();
  {
    const rsrc_t rw = mk_rsrc(ws, WS_USED);
    const f32x4 c0 = *(const f32x4*)(CSs + 32 * hh + 4 * kq), c1 = *(const f32x4*)(CSs + 32 * hh + 16 + 4 * kq);
#pragma unroll
    for (int m = 0; m < 4; ++m) {
      const float rs = Rs[64 * rh + 16 * m + r], mr = MRs[64 * rh + 16 * m + r];
      const u32x4 o = {pk2v(phi2(lo2(acc[m][0]) * rs + (lo2(c0) * mr + 1e-6f))), pk2v(phi2(hi2(acc[m][0]) * rs + (hi2(c0) * mr + 1e-6f))),
                       pk2v(phi2(lo2(acc[m][1]) * rs + (lo2(c1) * mr + 1e-6f))), pk2v(phi2(hi2(acc[m][1]) * rs + (hi2(c1) * mr + 1e-6f)))};
      st16(rw, OFF_PQ + (((band * 8 + rh * 4 + m) * 32 + h) * 64 + lane) * 16, o);
    }
  }
  STAMP(kid, 6); DRAIN(); STAMP(kid, 7);
}

constexpr int G2_LDS = 131072;
template <int FINAL>
__global__ __launch_bounds__(512, 2) void g2_kernel(char* __restrict__ ws, const float* __restrict__ kvacc, char* __restrict__ out) {
  extern __shared__ __attribute__((aligned(16))) char smem[];
  constexpr int kid = FINAL ? 6 : 3;
  STAMP(kid, 0);
  const int tid = threadIdx.x, lane = tid & 63, w = __builtin_amdgcn_readfirstlane(tid >> 6);
  const int xcd = blockIdx.x & 7, idx = blockIdx.x >> 3;
  const int sb = (xcd >> 1) * 8 + (idx & 7), nb = (xcd & 1) * 4 + (idx >> 3);
  const int r = lane & 15, kq = lane >> 4;
  u32x4* O = (u32x4*)smem;
  if (w < 4) {
    constexpr int PD = 4;
    u32x4 pq[PD][4];
    f32x4 kv[PD][4], z[PD][2];
    const u32x4* pqf = (const u32x4*)(ws + OFF_PQ) + ((size_t)(sb * 4) * 32 + w) * 64 + lane;
    const f32x4* kvp = (const f32x4*)kvacc + w * 256 + lane;
    const f32x4* zp = (const f32x4*)(kvacc + 32 * 1024) + w * 8 + kq;
    auto load = [&](int i) {
      const int b = i % PD;
#pragma unroll
      for (int g = 0; g < 4; ++g) kv[b][g] = kvp[i * 1024 + g * 64];
#pragma unroll
      for (int g = 0; g < 2; ++g) z[b][g] = zp[i * 32 + g * 4];
#pragma unroll
      for (int st = 0; st < 4; ++st) pq[b][st] = pqf[(st * 32 + i * 4) * 64];
    };
    load(0); load(1);
    SB();
#pragma unroll
    for (int i = 0; i < 8; ++i) {
      const int b = i % PD;
      const bf16x8 k0 = as_frag((u32x4){pk2v(lo2(kv[b][0])), pk2v(hi2(kv[b][0])), pk2v(lo2(kv[b][1])), pk2v(hi2(kv[b][1]))});
      const bf16x8 k1 = as_frag((u32x4){pk2v(lo2(kv[b][2])), pk2v(hi2(kv[b][2])), pk2v(lo2(kv[b][3])), pk2v(hi2(kv[b][3]))});
      const bf16x8 zb = as_frag((u32x4){pk2v(lo2(z[b][0])), pk2v(hi2(z[b][0])), pk2v(lo2(z[b][1])), pk2v(hi2(z[b][1]))});
      const f32x4 zero = {0.f, 0.f, 0.f, 0.f};
      f32x4 x0[4], x1[4], dn[4];
#pragma unroll
      for (int st = 0; st < 4; ++st) {
        dn[st] = MFMA16(zb, as_frag(pq[b][st]), zero);
        x0[st] = MFMA16(k0, as_frag(pq[b][st]), zero);
        x1[st] = MFMA16(k1, as_frag(pq[b][st]), zero);
      }
      SB();
#pragma unroll
      for (int st = 0; st < 4; ++st) {
        const float inv = __builtin_amdgcn_rcpf(dn[st][0] + 1e-6f);
        O[((i * 4 + w) * 4 + st) * 64 + lane] =
            (u32x4){pk2v(lo2(x0[st]) * inv), pk2v(hi2(x0[st]) * inv), pk2v(lo2(x1[st]) * inv), pk2v(hi2(x1[st]) * inv)};
      }
      WAIT_LGKM0();
      BAR();
      SB();
      if (i == 0) { load(2); load(3); }
      if (i + PD < 8) load(i + PD);
      SB();
      if (i == 0) STAMP(kid, 1);
    }
    STAMP(kid, 2);
    return;
  }
  const int c = w - 4, n0 = nb * 128 + c * 32, s0 = sb * 64;
  constexpr int CD = 4;
  u32x4 wo[CD][8];
  const u32x4* wof = (const u32x4*)(ws + OFF_WOF) + (size_t)(nb * 4 + c) * 4096 + lane;
  auto loadw = [&](int i) {
#pragma unroll
    for (int g = 0; g < 8; ++g) wo[i % CD][g] = wof[(i * 8 + g) * 64];
  };
  loadw(0); loadw(1); loadw(2);
  f32x4 acc[2][4];
#pragma unroll
  for (int a = 0; a < 2; ++a)
#pragma unroll
    for (int st = 0; st < 4; ++st) { acc[a][st] = (f32x4){0.f, 0.f, 0.f, 0.f}; asm volatile("" : "+v"(acc[a][st])); }
  u32x4 res[4];
  SB();
#pragma unroll
  for (int i = 0; i < 8; ++i) {
    BAR();
    u32x4 of[4][4];
#pragma unroll
    for (int u = 0; u < 8; ++u) of[u >> 2][u & 3] = O[(i * 16 + u) * 64 + lane];
    SB();
#pragma unroll
    for (int n = 0; n < 32; ++n) {
      const int hh = n >> 3, st = (n >> 1) & 3, a = n & 1;
      acc[a][st] = MFMA16(as_frag(wo[i % CD][hh * 2 + a]), as_frag(of[hh][st]), acc[a][st]);
      SB();
      if (n < 16 && (n & 1)) { const int u = 8 + (n >> 1); of[u >> 2][u & 3] = O[(i * 16 + u) * 64 + lane]; SB(); }
      if ((n & 3) == 2 && i + 3 < 8) { wo[(i + 3) % CD][n >> 2] = wof[((i + 3) * 8 + (n >> 2)) * 64]; SB(); }
      if (i == 5 && (n & 7) == 4) {
        const int st2 = n >> 3, s = s0 + 16 * st2 + r;
        if (FINAL) res[st2] = ((const u32x4*)(ws + OFF_X3))[(size_t)((n0 >> 3) + kq) * S + s];
        else {
          const u32x2* x1 = (const u32x2*)(ws + OFF_X1) + (size_t)((n0 >> 2) + 2 * kq) * S + s;
          const u32x2 lo = x1[0], hi = x1[S];
          res[st2] = (u32x4){lo[0], lo[1], hi[0], hi[1]};
        }
        SB();
      }
    }
  }
  STAMPW(kid, 3, 4);
  const rsrc_t rw = FINAL ? mk_rsrc(out, S * D * 2) : mk_rsrc(ws, WS_USED);
#pragma unroll
  for (int st = 0; st < 4; ++st) {
    const int s = s0 + 16 * st + r;
    const u32x4 x = {pk2(bflo(res[st][0]) + acc[0][st][0], bfhi(res[st][0]) + acc[0][st][1]),
                     pk2(bflo(res[st][1]) + acc[0][st][2], bfhi(res[st][1]) + acc[0][st][3]),
                     pk2(bflo(res[st][2]) + acc[1][st][0], bfhi(res[st][2]) + acc[1][st][1]),
                     pk2(bflo(res[st][3]) + acc[1][st][2], bfhi(res[st][3]) + acc[1][st][3])};
    if (FINAL) *(u32x4*)(out + ((size_t)s * D + n0 + 8 * kq) * 2) = x;
    else st16(rw, OFF_X2 + (((n0 >> 3) + kq) * S + s) * 16, x);
  }
  STAMPW(kid, 6, 4); DRAIN(); STAMPW(kid, 7, 4);
}

extern "C" void kernel_launch(void* const* d_in, const int* in_sizes, int n_in, void* d_out, int out_size, void* d_ws,
                              size_t ws_size, hipStream_t stream) {
  const uint16_t* q = (const uint16_t*)d_in[0];
  const uint16_t* k = (const uint16_t*)d_in[1];
  const uint16_t* v = (const uint16_t*)d_in[2];
  const uint16_t* shift_k = (const uint16_t*)d_in[3];
  const uint16_t* ssm_k = (const uint16_t*)d_in[4];
  const uint16_t* ssm_D = (const uint16_t*)d_in[5];
  const uint16_t* wqkv = (const uint16_t*)d_in[7];
  const uint16_t* wo = (const uint16_t*)d_in[8];
  char* ws = (char*)d_ws;
  (void)hipFuncSetAttribute((const void*)conv_kernel, hipFuncAttributeMaxDynamicSharedMemorySize, CV_LDS);
  (void)hipFuncSetAttribute((const void*)g1_kernel<0>, hipFuncAttributeMaxDynamicSharedMemorySize, G1_LDS);
  (void)hipFuncSetAttribute((const void*)g1_kernel<1>, hipFuncAttributeMaxDynamicSharedMemorySize, G1_LDS);
  (void)hipFuncSetAttribute((const void*)g2_kernel<0>, hipFuncAttributeMaxDynamicSharedMemorySize, G2_LDS);
  (void)hipFuncSetAttribute((const void*)g2_kernel<1>, hipFuncAttributeMaxDynamicSharedMemorySize, G2_LDS);
  float* kv0 = (float*)(ws + OFF_KV);
  float* kv1 = kv0 + KV_LAYER;
  prep_kernel<<<256, 256, 0, stream>>>(q, k, v, shift_k, ws);
  conv_kernel<<<256, 512, CV_LDS, stream>>>(ssm_k, ssm_D, wqkv, wo, ws);
  g1_kernel<1><<<256, 512, G1_LDS, stream>>>(ws, OFF_X1, kv0);
  g2_kernel<0><<<256, 512, G2_LDS, stream>>>(ws, kv0, nullptr);
  row_kernel<<<256, 256, 0, stream>>>(ws);
  g1_kernel<0><<<256, 512, G1_LDS, stream>>>(ws, OFF_X3, kv1);
  g2_kernel<1><<<256, 512, G2_LDS, stream>>>(ws, kv1, (char*)d_out);
}
```

```cpp
#define WSPOS 2
#include <hip/hip_runtime.h>
#include <stdint.h>

typedef __attribute__((ext_vector_type(8))) short bf16x8;
typedef __attribute__((ext_vector_type(4))) short s16x4;
typedef __attribute__((ext_vector_type(4))) float f32x4;
typedef __attribute__((ext_vector_type(2))) float f32x2;
typedef __attribute__((ext_vector_type(16))) float f32x16;
typedef __attribute__((ext_vector_type(4))) unsigned u32x4;
typedef __attribute__((ext_vector_type(2))) unsigned u32x2;
typedef __attribute__((ext_vector_type(2))) __bf16 bf16x2_t;
#define DI __device__ __forceinline__
#define LAS __attribute__((address_space(3)))
#define STAMPW(kid, slot, wv) do {} while (0)
#define CSTAMP(kid, slot) do {} while (0)
#define DRAIN() do {} while (0)
#define STAMP(kid, slot) STAMPW(kid, slot, 0)

constexpr int S = 2048, D = 1024;
#define TALL 1
constexpr int G1_ROWS = TALL ? 256 : 128, G1_HP = TALL ? 1 : 2, G1_COLS = 96 * G1_HP;
constexpr unsigned MB = 1024 * 1024;
constexpr unsigned OFF_UB = 0, OFF_X0 = 4 * MB, OFF_W8 = 8 * MB, OFF_WOF = 14 * MB, OFF_Y = 16 * MB, OFF_X1 = 20 * MB,
                   OFF_PQ = 24 * MB, OFF_X2 = 28 * MB, OFF_X3 = 32 * MB, OFF_SM = 36 * MB, WS_USED = 37 * MB;
constexpr unsigned OFF_ST0 = OFF_SM, OFF_CSP = OFF_SM + 16384,
                   OFF_KV = OFF_CSP + 16 * 3072 * 4,
                   OFF_GX = OFF_KV + 2 * 33792 * 4,
                   OFF_GX2 = OFF_GX + 65536,
                   OFF_ST3 = OFF_GX2 + 131072;
constexpr int KV_LAYER = 32 * 1024 + 32 * 32;

DI float bflo(unsigned u) { return __uint_as_float(u << 16); }
DI float bfhi(unsigned u) { return __uint_as_float(u & 0xffff0000u); }
DI unsigned pk2(float a, float b) {
  f32x2 v = {a, b};
  bf16x2_t r = __builtin_convertvector(v, bf16x2_t);
  return __builtin_bit_cast(unsigned, r);
}
DI unsigned pk2v(f32x2 v) { return __builtin_bit_cast(unsigned, __builtin_convertvector(v, bf16x2_t)); }
DI float rbf(float a) { return bflo(pk2(a, a)); }
DI bf16x8 as_frag(u32x4 v) { return __builtin_bit_cast(bf16x8, v); }
DI float dot2(unsigned a, unsigned b, float c) {
  return __builtin_amdgcn_fdot2_f32_bf16(__builtin_bit_cast(bf16x2_t, a), __builtin_bit_cast(bf16x2_t, b), c, false);
}
#define MFMA16(a, b, c) __builtin_amdgcn_mfma_f32_16x16x32_bf16((a), (b), (c), 0, 0, 0)
#define MFMA32(a, b, c) __builtin_amdgcn_mfma_f32_32x32x16_bf16((a), (b), (c), 0, 0, 0)
#define WAIT_VM(n) asm volatile("s_waitcnt vmcnt(" #n ")" ::: "memory")
#define WAIT_LGKM0() asm volatile("s_waitcnt lgkmcnt(0)" ::: "memory")
#define BAR() do { __builtin_amdgcn_s_barrier(); asm volatile("" ::: "memory"); } while (0)
#define SB() __builtin_amdgcn_sched_barrier(0)
DI void glds16(const void* g, char* l) {
  __builtin_amdgcn_global_load_lds((const __attribute__((address_space(1))) void*)g, (LAS void*)l, 16, 0, 0);
}
typedef __amdgpu_buffer_rsrc_t rsrc_t;
DI rsrc_t mk_rsrc(void* p, unsigned bytes) { return __builtin_amdgcn_make_buffer_rsrc(p, 0, bytes, 0x00020000); }
DI void dma16(rsrc_t r, char* l, unsigned voff, unsigned soff) { __builtin_amdgcn_raw_ptr_buffer_load_lds(r, (LAS void*)l, 16, voff, soff, 0, 0); }
template <int N> DI void wait_vm() { asm volatile("s_waitcnt vmcnt(%0)" ::"i"(N) : "memory"); }
DI void publish8(char* p, float sm, float sq) {
  unsigned qb = __float_as_uint(sq);
  if (qb == ~0u) qb = 0x7fc00000u;
  __hip_atomic_store((unsigned long long*)p, ((unsigned long long)qb << 32) | __float_as_uint(sm), __ATOMIC_RELAXED, __HIP_MEMORY_SCOPE_AGENT);
}
DI u32x2 poll8(const char* p) { return __builtin_bit_cast(u32x2, __hip_atomic_load((const unsigned long long*)p, __ATOMIC_RELAXED, __HIP_MEMORY_SCOPE_AGENT)); }
DI u32x4 ld16(rsrc_t r, unsigned voff, unsigned soff) { return __builtin_amdgcn_raw_buffer_load_b128(r, voff, soff, 0); }
DI u32x2 ld8(rsrc_t r, unsigned voff, unsigned soff) { return __builtin_amdgcn_raw_buffer_load_b64(r, voff, soff, 0); }
DI void st16(rsrc_t r, unsigned off, u32x4 v) { __builtin_amdgcn_raw_buffer_store_b128(v, r, off, 0, 16); }

DI float opq(float m) { asm("" : "+v"(m)); return m; }
template <int N> DI float row_ror(float x) {
  return __int_as_float(__builtin_amdgcn_update_dpp(0, __float_as_int(x), 0x120 + N, 0xf, 0xf, false));
}
DI float sum_x16(float x) { const auto p = __builtin_amdgcn_permlane16_swap(__float_as_uint(x), __float_as_uint(x), false, false); return __uint_as_float(p[0]) + __uint_as_float(p[1]); }
DI float sum_x32(float x) { const auto p = __builtin_amdgcn_permlane32_swap(__float_as_uint(x), __float_as_uint(x), false, false); return __uint_as_float(p[0]) + __uint_as_float(p[1]); }

DI f32x2 ln_stats(float s, float q) {
  float m = s * (1.0f / 1024.0f);
  float v = fmaxf(q * (1.0f / 1024.0f) - m * m, 0.0f);
  float sd = rbf(sqrtf(rbf(rbf(v) + 1e-5f)));
  f32x2 r = {rbf(m), 1.0f / sd};
  return r;
}

__global__ __launch_bounds__(512) void prep_kernel(const uint16_t* __restrict__ q, const uint16_t* __restrict__ k,
                                                   const uint16_t* __restrict__ v, const uint16_t* __restrict__ shift_k,
                                                   char* __restrict__ ws) {
  __shared__ __attribute__((aligned(16))) u32x4 bounce[8][256];
  __shared__ float red[4][16];
  STAMP(0, 0);
  const int lane = threadIdx.x & 63, w8 = __builtin_amdgcn_readfirstlane(threadIdx.x >> 6), w = w8 & 3, tid = threadIdx.x & 255;
  const int bid = blockIdx.x, s0 = bid * 8;
  const rsrc_t rw = mk_rsrc(ws, WS_USED);
  const int sw = (lane >> 1) & 3;
  if (w8 < 4) {
    u32x2 kk[11], vv[8];
#pragma unroll
    for (int r = 0; r < 11; ++r) {
      const int s = (r < 3) ? max(s0 - 3 + r, 0) : s0 - 3 + r;
      kk[r] = *(const u32x2*)(k + (size_t)s * D + tid * 4);
    }
#pragma unroll
    for (int r = 0; r < 8; ++r) vv[r] = *(const u32x2*)(v + (size_t)(s0 + r) * D + tid * 4);
    const u32x4 wa = *(const u32x4*)(shift_k + tid * 16), wb = *(const u32x4*)(shift_k + tid * 16 + 8);
    asm volatile("" ::"v"(vv[7]), "v"(wb));
    STAMP(0, 1);
    const unsigned hm = bid ? ~0u : 0u;
#pragma unroll
    for (int r = 0; r < 3; ++r) { kk[r][0] &= hm; kk[r][1] &= hm; }
#pragma unroll
    for (int c = 0; c < 4; ++c) {
      const unsigned w01 = (c == 0) ? wa[0] : (c == 1) ? wa[2] : (c == 2) ? wb[0] : wb[2];
      const unsigned w23 = (c == 0) ? wa[1] : (c == 1) ? wa[3] : (c == 2) ? wb[1] : wb[3];
      const float w0 = bflo(w01), w1 = bfhi(w01), w2 = bflo(w23), w3 = bfhi(w23);
      float kf[11], u[8];
#pragma unroll
      for (int r = 0; r < 11; ++r) kf[r] = (c & 1) ? bfhi(kk[r][c >> 1]) : bflo(kk[r][c >> 1]);
#pragma unroll
      for (int r = 0; r < 8; ++r) {
        const float ks = kf[r] * w3 + kf[r + 1] * w2 + kf[r + 2] * w1 + kf[r + 3] * w0;
        u[r] = ks * ((c & 1) ? bfhi(vv[r][c >> 1]) : bflo(vv[r][c >> 1]));
      }
      bounce[w8][lane * 4 + (c ^ sw)] = (u32x4){pk2(u[7], u[6]), pk2(u[5], u[4]), pk2(u[3], u[2]), pk2(u[1], u[0])};
    }
#pragma unroll
    for (int i = 0; i < 4; ++i) {
      const int u = 64 * i + lane;
      st16(rw, OFF_UB + (bid * 1024 + w * 256 + u) * 16, bounce[w8][(u & ~3) | ((u & 3) ^ ((u >> 3) & 3))]);
    }
    STAMP(0, 6);
    BAR();
    DRAIN(); STAMP(0, 7);
    return;
  }
  u32x2 qq[8];
#pragma unroll
  for (int r = 0; r < 8; ++r) qq[r] = *(const u32x2*)(q + (size_t)(s0 + r) * D + tid * 4);
  asm volatile("" ::"v"(qq[7]));
  STAMPW(0, 2, 4);
#pragma unroll
  for (int r = 0; r < 4; ++r) bounce[w8][lane * 4 + (r ^ sw)] = (u32x4){qq[2 * r][0], qq[2 * r][1], qq[2 * r + 1][0], qq[2 * r + 1][1]};
#pragma unroll
  for (int i = 0; i < 4; ++i) {
    const int u = 64 * i + lane;
    st16(rw, OFF_X0 + (bid * 1024 + w * 256 + u) * 16, bounce[w8][(u & ~3) | ((u & 3) ^ ((u >> 3) & 3))]);
  }
  if (tid < 48) st16(rw, OFF_GX + bid * 768 + tid * 16, (u32x4){~0u, ~0u, ~0u, ~0u});
  float a[16];
#pragma unroll
  for (int r = 0; r < 8; ++r) {
    a[2 * r] = dot2(qq[r][0], 0x3f803f80u, dot2(qq[r][1], 0x3f803f80u, 0.f));
    a[2 * r + 1] = dot2(qq[r][0], qq[r][0], dot2(qq[r][1], qq[r][1], 0.f));
  }
#pragma unroll
  for (int i = 0; i < 8; ++i) { const auto p = __builtin_amdgcn_permlane32_swap(__float_as_uint(a[i]), __float_as_uint(a[i + 8]), false, false); a[i] = __uint_as_float(p[0]) + __uint_as_float(p[1]); }
#pragma unroll
  for (int i = 0; i < 4; ++i) { const auto p = __builtin_amdgcn_permlane16_swap(__float_as_uint(a[i]), __float_as_uint(a[i + 4]), false, false); a[i] = __uint_as_float(p[0]) + __uint_as_float(p[1]); }
#pragma unroll
  for (int i = 0; i < 4; ++i) { a[i] += row_ror<1>(a[i]); a[i] += row_ror<2>(a[i]); a[i] += row_ror<4>(a[i]); a[i] += row_ror<8>(a[i]); }
  if ((lane & 15) == 0) *(f32x4*)&red[w][(lane >> 4) * 4] = (f32x4){a[0], a[1], a[2], a[3]};
  WAIT_LGKM0();
  STAMPW(0, 3, 4);
  BAR();
  if (tid < 8) {
    const float sm = (red[0][2 * tid] + red[1][2 * tid]) + (red[2][2 * tid] + red[3][2 * tid]);
    const float sq = (red[0][2 * tid + 1] + red[1][2 * tid + 1]) + (red[2][2 * tid + 1] + red[3][2 * tid + 1]);
    const f32x2 st = ln_stats(sm, sq);
    __builtin_amdgcn_raw_buffer_store_b64(__builtin_bit_cast(u32x2, st), rw, OFF_ST0 + (s0 + tid) * 8, 0, 16);
  }
  STAMPW(0, 4, 4); DRAIN(); STAMPW(0, 5, 4);
}

constexpr int CV_U = 286 * 16;
constexpr int CV_HA = CV_U;
constexpr int CV_HB = CV_HA + 4160;
constexpr int CV_CH = CV_HB + 4128 + 32;
#define WEARLY 0
constexpr int CV_PAD = 64;
constexpr int CV_X0 = 4 * CV_CH + 4 * 8192, CV_ST = CV_X0 + 16384;
constexpr int CV_LDS = CV_PAD + CV_ST + 16384;
__global__ __launch_bounds__(512, 2) void conv_kernel(const uint16_t* __restrict__ ssm_k, const uint16_t* __restrict__ ssm_D,
                                                      const uint16_t* __restrict__ wqkv, const uint16_t* __restrict__ wo,
                                                      char* __restrict__ ws) {
  extern __shared__ __attribute__((aligned(16))) char smem_raw[];
  char* smem = smem_raw + CV_PAD;
  STAMP(1, 0);
  const int tid = threadIdx.x, lane = tid & 63, w = __builtin_amdgcn_readfirstlane(tid >> 6);
  const rsrc_t rw = mk_rsrc(ws, WS_USED);
  const int j = (blockIdx.x & 7) * 32 + (blockIdx.x >> 3);
  const unsigned cnt = (unsigned)(size_t)(LAS char*)smem_raw;
  auto arrive = [&]() { if (lane == 0) asm volatile("ds_add_u32 %0, %1" ::"v"(cnt), "v"(1u) : "memory"); };
  if (w >= 4) {
    const int gw = blockIdx.x * 4 + (w - 4);
    char* buf = smem + 4 * CV_CH + (w - 4) * 8192;
    const int g = lane >> 4, qq = (lane >> 2) & 3, p = lane & 3;
    const bool isw = gw < 768;
    const int cg = isw ? gw % 48 : (gw - 768) & 15, og = isw ? gw / 48 : (gw - 768) >> 4;
    const int rr = lane >> 3;
    auto w_loads = [&]() {
      if (isw) {
        const uint16_t* src = wqkv + (size_t)(og * 64 + rr) * 3072 + cg * 64 + (lane & 7) * 8;
#pragma unroll
        for (int i = 0; i < 8; ++i) glds16(src + (size_t)i * 8 * 3072, buf + i * 1024);
      } else {
        const uint16_t* src = wo + (size_t)(og * 64 + (rr & 3) + 16 * (rr >> 2)) * 1024 + cg * 64 + (lane & 7) * 8;
#pragma unroll
        for (int i = 0; i < 8; ++i) glds16(src + (size_t)((i >> 2) * 32 + (i & 3) * 4) * 1024, buf + i * 1024);
      }
    };
    if (WEARLY) w_loads();
    BAR();
#pragma unroll
    for (int i = 0; i < 4; ++i) {
      const int pp = (w - 4) * 256 + i * 64 + lane;
      glds16((const u32x4*)(ws + OFF_X0) + ((size_t)(pp >> 2) * 256 + j) * 4 + (pp & 3), smem + CV_X0 + ((w - 4) * 4 + i) * 1024);
      glds16((const u32x4*)(ws + OFF_ST0) + pp, smem + CV_ST + ((w - 4) * 4 + i) * 1024);
    }
    if (!WEARLY) { w_loads(); WAIT_VM(8); arrive(); }
    WAIT_VM(0);
    if (WEARLY) arrive();
    STAMPW(1, 3, 4);
    const int col = TALL ? ((cg & 15) * 2 + (lane >> 5)) * 96 + (cg >> 4) * 32 + (lane & 31) : (cg & 15) * 192 + (cg >> 4) * 64 + lane;
    const unsigned dst = isw ? OFF_W8 + (((col / G1_COLS) * 128 + og * 8) * G1_COLS + col % G1_COLS) * 16
                             : OFF_WOF + ((((og >> 1) * 32 + cg * 2 + (g >> 1)) * 8 + (og & 1) * 4) * 64 + (lane & 31)) * 16;
    const unsigned dstep = isw ? G1_COLS * 16 : 512;
    const unsigned la = (unsigned)(size_t)(LAS char*)buf + qq * 128 + (isw ? g * 32 + p * 8 : (g >> 1) * 64 + (p & 1) * 32 + ((g & 1) * 2 + (p >> 1)) * 8);
    u32x2 lo[8], hi[8];
#pragma unroll
    for (int i = 0; i < 8; ++i) {
      lo[i] = __builtin_bit_cast(u32x2, __builtin_amdgcn_ds_read_tr16_b64_v4i16((LAS s16x4*)(size_t)(la + i * 1024)));
      hi[i] = __builtin_bit_cast(u32x2, __builtin_amdgcn_ds_read_tr16_b64_v4i16((LAS s16x4*)(size_t)(la + i * 1024 + 512)));
    }
#pragma unroll
    for (int i = 0; i < 8; ++i) st16(rw, dst + i * dstep, (u32x4){lo[i][0], lo[i][1], hi[i][0], hi[i][1]});
    if (lane < 16 + 1 - (gw & 1)) st16(rw, OFF_KV + (gw * 33 / 2 + (gw & 1) + lane) * 16, (u32x4){0u, 0u, 0u, 0u});
    if (isw) {
      float c0 = 0.f, c1 = 0.f;
#pragma unroll
      for (int i = 0; i < 8; ++i)
#pragma unroll
        for (int e = 0; e < 2; ++e) {
          c0 = opq(opq(c0 + bflo(lo[i][e])) + bflo(hi[i][e]));
          c1 = opq(opq(c1 + bfhi(lo[i][e])) + bfhi(hi[i][e]));
        }
      __builtin_amdgcn_raw_buffer_store_b32(__float_as_uint(c0 + c1), rw, OFF_CSP + (col * 16 + og) * 4, 0, 16);
    }
    STAMPW(1, 4, 4); DRAIN(); STAMPW(1, 5, 4);
    return;
  }
  u32x4 uu[4], hh[4];
  unsigned hn[4];
  const uint16_t* hrow = ssm_k + (size_t)(4 * j + w) * S;
#pragma unroll
  for (int i = 0; i < 4; ++i) {
    const int c = i * 64 + lane;
    hh[i] = *(const u32x4*)(hrow + c * 8);
    hn[i] = *(const unsigned*)(hrow + min(c * 8 + 8, S - 2));
  }
  SB();
  const u32x4* ub = (const u32x4*)(ws + OFF_UB);
#pragma unroll
  for (int i = 0; i < 4; ++i) uu[i] = ub[(size_t)(w * 64 + i * 16 + (lane >> 2)) * 1024 + 4 * j + (lane & 3)];
  SB();
  const float Dc = bflo((unsigned)ssm_D[4 * j + w]);
  SB();
  char* my = smem + w * CV_CH;
  if (lane < 30) *(u32x4*)(my + lane * 16) = (u32x4){0u, 0u, 0u, 0u};
  if (lane >= 32 && lane < 34) *(u32x4*)(my + CV_HA + (lane - 32) * 16) = (u32x4){0u, 0u, 0u, 0u};
  if (lane == 34) *(u32x4*)(my + CV_HB) = (u32x4){0u, 0u, 0u, 0u};
  if (tid == 35) *(unsigned*)smem_raw = 0u;
  if (lane == 0) *(u32x4*)(my + CV_HB + 16) = (u32x4){0u, 0u, 0u, hh[0][0] << 16};
#pragma unroll
  for (int i = 0; i < 4; ++i) {
    const int c = i * 64 + lane;
    *(u32x4*)(my + CV_HA + 32 + c * 16) = hh[i];
    u32x4 o = {__builtin_amdgcn_alignbyte(hh[i][1], hh[i][0], 2), __builtin_amdgcn_alignbyte(hh[i][2], hh[i][1], 2),
               __builtin_amdgcn_alignbyte(hh[i][3], hh[i][2], 2), __builtin_amdgcn_alignbyte(hn[i], hh[i][3], 2)};
    *(u32x4*)(my + CV_HB + 32 + c * 16) = o;
  }
#pragma unroll
  for (int i = 0; i < 4; ++i) *(u32x4*)(smem + (lane & 3) * CV_CH + (30 + w * 64 + i * 16 + (lane >> 2)) * 16) = uu[i];
  const int t0 = lane & 15, kq = lane >> 4;
  const unsigned* fb = (const unsigned*)(my + ((t0 & 1) ? CV_HA : CV_HB)) + 4 * kq + ((1 + t0) >> 1);
  const char* gb = my + (2 * t0 + 3 - kq) * 16;
  f32x4 acc[8];
#pragma unroll
  for (int b = 0; b < 8; ++b) { acc[b] = (f32x4){0.f, 0.f, 0.f, 0.f}; asm volatile("" : "+v"(acc[b])); }
  WAIT_LGKM0();
  BAR();
  STAMP(1, 1); CSTAMP(9, 1); STAMP(9, 0);
  bf16x8 F[2][8], G[2][8];
  const LAS unsigned* fbk[4];
#pragma unroll
  for (int i = 0; i < 4; ++i) { fbk[i] = (const LAS unsigned*)fb + 256 * i; asm volatile("" : "+v"(fbk[i])); }
  u32x2 fh[8];
  auto rdn = [&](int buf, int g0, int n) {
    if (n == 0 || n > 16) { const int g1 = n ? n - 16 : 0; G[buf][g1] = *(const bf16x8*)(gb + (32 * g1 + 4 * (7 - g0)) * 16); }
    else {
      const int e1 = (n - 1) >> 1;
      const LAS unsigned* p = fbk[e1 >> 1] + 128 * (e1 & 1) + 16 * g0;
      if ((n - 1) & 1) F[buf][e1] = as_frag((u32x4){fh[e1][0], fh[e1][1], p[2], p[3]});
      else fh[e1] = (u32x2){p[0], p[1]};
    }
  };
  auto cstep = [&](int cb, int g0n) {
    int n = 0;
#pragma unroll
    for (int g1 = 0; g1 < 8; ++g1)
#pragma unroll
      for (int e1 = 0; e1 + g1 < 8; ++e1) {
        acc[g1 + e1] = MFMA16(F[cb][e1], G[cb][g1], acc[g1 + e1]);
        SB();
        if (n < 24) { rdn(cb ^ 1, g0n, n); SB(); }
        ++n;
      }
  };
#pragma unroll
  for (int n = 0; n < 24; ++n) rdn(0, 0, n);
  for (int gp = 0; gp < 4; ++gp) {
    cstep(0, 1);
    cstep(1, 2);
    gb -= 128;
#pragma unroll
    for (int i = 0; i < 4; ++i) fbk[i] += 32;
  }
  asm volatile("" ::"v"(acc[7]), "v"(acc[0]));
  STAMP(1, 2); CSTAMP(9, 2); STAMP(9, 3);
#pragma unroll
  for (int b = 0; b < 8; ++b) {
    const int s = 256 * b + 16 * t0 + 4 * kq;
    const u32x2 ur = *(const u32x2*)(my + (30 + (s >> 3)) * 16 + ((s & 4) ? 0 : 8));
    *(u32x2*)(my + CV_HA + s * 2) = (u32x2){pk2(acc[b][0] + bfhi(ur[1]) * Dc, acc[b][1] + bflo(ur[1]) * Dc),
                                            pk2(acc[b][2] + bfhi(ur[0]) * Dc, acc[b][3] + bflo(ur[0]) * Dc)};
  }
  WAIT_LGKM0();
  arrive();
  while (*(volatile LAS unsigned*)(size_t)cnt < 8u) __builtin_amdgcn_s_sleep(1);
  asm volatile("" ::: "memory");
#pragma unroll
  for (int i = 0; i < 4; ++i) {
    const int p = tid + 256 * i;
    unsigned yy[4];
#pragma unroll
    for (int c = 0; c < 4; ++c) yy[c] = *(const unsigned*)(smem + c * CV_CH + CV_HA + p * 4);
    const u32x4 yo = {__builtin_amdgcn_perm(yy[1], yy[0], 0x05040100u), __builtin_amdgcn_perm(yy[3], yy[2], 0x05040100u),
                      __builtin_amdgcn_perm(yy[1], yy[0], 0x07060302u), __builtin_amdgcn_perm(yy[3], yy[2], 0x07060302u)};
    u32x4 xo;
    const u32x4 x0v = *(const u32x4*)(smem + CV_X0 + p * 16);
    const f32x4 stv = *(const f32x4*)(smem + CV_ST + p * 16);
#pragma unroll
    for (int c = 0; c < 4; ++c) {
      const float m = stv[c & 2], rs = stv[(c & 2) + 1];
      const float xa = bflo(x0v[c]), xb = bfhi(x0v[c]);
      xo[c] = pk2(xa + (xa - m) * rs * bflo(yo[c]), xb + (xb - m) * rs * bfhi(yo[c]));
    }
    st16(rw, OFF_X1 + (j * 1024 + p) * 16, xo);
    st16(rw, OFF_Y + (j * 1024 + p) * 16, yo);
  }
  STAMP(1, 6); DRAIN(); STAMP(1, 7);
}

constexpr int G1_AB = G1_ROWS * 128, G1_STAGE = G1_AB + G1_COLS * 128, G1_NS = 3, G1_SCR = G1_STAGE * G1_NS, G1_LDS = G1_SCR + 2816 + 4 * 4224;
constexpr int G1_NA = G1_ROWS / 32, G1_NB = G1_COLS / 32, G1_NP = G1_NA + G1_NB;
constexpr float LOG2E = 1.44269504088896f;
DI f32x2 phi2(f32x2 t2) {
  const f32x2 a = t2 * LOG2E - 1e-6f * LOG2E;
  f32x2 e, m;
  e[0] = __builtin_amdgcn_fmed3f(__builtin_amdgcn_exp2f(a[0]), 0.0f, 1.0f);
  e[1] = __builtin_amdgcn_fmed3f(__builtin_amdgcn_exp2f(a[1]), 0.0f, 1.0f);
  m[0] = fmaxf(t2[0], 1e-6f);
  m[1] = fmaxf(t2[1], 1e-6f);
  return m + e;
}
DI f32x2 lo2(f32x4 v) { return (f32x2){v[0], v[1]}; }
DI f32x2 hi2(f32x4 v) { return (f32x2){v[2], v[3]}; }
template <int C4>
__global__ __launch_bounds__(512, 2) void g1_kernel(char* __restrict__ ws) {
  extern __shared__ __attribute__((aligned(16))) char smem[];
  constexpr int kid = C4 ? 2 : 4;
  constexpr unsigned OFF_X = C4 ? OFF_X1 : OFF_X3, KVO = OFF_KV + (C4 ? 0 : KV_LAYER * 4);
  STAMP(kid, 0);
  const int tid = threadIdx.x, lane = tid & 63, w = __builtin_amdgcn_readfirstlane(tid >> 6);
  const int xcd = blockIdx.x & 7, idx = blockIdx.x >> 3;
  const int band = TALL ? (xcd & 1) * 4 + (idx & 3) : (xcd & 1) * 8 + (idx & 7), hg = TALL ? (xcd >> 1) * 8 + (idx >> 2) : (xcd >> 1) * 4 + (idx >> 3);
  const rsrc_t rw = mk_rsrc(ws, WS_USED);
  float* Rs = (float*)(smem + G1_SCR);
  float* MRs = Rs + 256;
  float* CSs = Rs + 512;
  float* P = Rs + 704;
  if (w >= 4) {
    const int lw = w - 4, t = tid - 256, row0 = band * G1_ROWS, tr = t & (G1_ROWS - 1);
    const unsigned l0 = lane * 16, l1 = (lane ^ 8) * 16;
    auto issue = [&](int st) {
      char* base = smem + (st % G1_NS) * G1_STAGE;
#pragma unroll
      for (int p = 0; p < G1_NA; ++p) {
        constexpr int PQ = G1_NA / 4, PO = G1_NA / 2;
        if (C4) dma16(rw, base + (G1_NA * lw + p) * 1024, ((p / PQ) & 2) ? l1 : l0, OFF_X + ((st * 16 + lw * 4 + p / PQ) * 1024 + band * (G1_ROWS / 2) + (p % PQ) * 64) * 16);
        else dma16(rw, base + (G1_NA * lw + p) * 1024, l0, OFF_X + ((st * 8 + lw * 2 + p / PO) * S + band * G1_ROWS + (p % PO) * 64) * 16);
      }
#pragma unroll
      for (int p = 0; p < G1_NB; ++p) dma16(rw, base + G1_AB + (G1_NB * lw + p) * 1024, l0, OFF_W8 + (hg * 128 + st * 8) * G1_COLS * 16 + (G1_NB * lw + p) * 1024);
    };
    issue(0); issue(1);
    wait_vm<G1_NP>();
    BAR();
    issue(2);
    u32x4 xv[4];
    float v[4] = {0.f, 0.f, 0.f, 0.f};
    if (C4) {
      const u32x4* xp = (const u32x4*)(ws + OFF_X) + (size_t)(t >> 2) * 1024 + band * (G1_ROWS / 2) + hg * 4 + (t & 3);
      asm volatile("global_load_dwordx4 %0, %4, off\n\tglobal_load_dwordx4 %1, %5, off\n\tglobal_load_dwordx4 %2, %6, off\n\tglobal_load_dwordx4 %3, %7, off"
                   : "=&v"(xv[0]), "=&v"(xv[1]), "=&v"(xv[2]), "=&v"(xv[3]) : "v"(xp), "v"(xp + 65536), "v"(xp + 131072), "v"(xp + 196608) : "memory");
    }
#pragma unroll
    for (int i = 0; i < 13; ++i) {
      if (C4 && i < 2) wait_vm<G1_NP + 4>();
      else if (C4 && i == 2) asm volatile("s_waitcnt vmcnt(%4)" : "+v"(xv[0]), "+v"(xv[1]), "+v"(xv[2]), "+v"(xv[3]) : "i"(G1_NP) : "memory");
      else wait_vm<G1_NP>();
      BAR();
      issue(i + G1_NS);
      if (C4 && i >= 2 && i < 6) {
        const u32x4 x = xv[i - 2];
#pragma unroll
        for (int c = 0; c < 4; ++c) {
          const float lo = bflo(x[c]), hi = bfhi(x[c]);
          v[c & 2] = opq(v[c & 2] + lo); v[(c & 2) + 1] = opq(fmaf(lo, lo, v[(c & 2) + 1]));
          v[c & 2] = opq(v[c & 2] + hi); v[(c & 2) + 1] = opq(fmaf(hi, hi, v[(c & 2) + 1]));
        }
        asm volatile("" : "+v"(v[0]), "+v"(v[1]), "+v"(v[2]), "+v"(v[3]));
      }
      if (C4 && i == 6) {
#pragma unroll
        for (int c = 0; c < 4; ++c) { v[c] += row_ror<4>(v[c]); v[c] += row_ror<8>(v[c]); v[c] = sum_x32(sum_x16(v[c])); }
        const bool up = (lane & 4) != 0;
        if (lane < 8) publish8(ws + OFF_GX + ((row0 >> 8) * 1024 + lw * 256 + (row0 & 255) + hg * 8 + 2 * (lane & 3) + (lane >> 2)) * 8, up ? v[2] : v[0], up ? v[3] : v[1]);
      }
    }
    const char* gp = ws + (C4 ? OFF_GX + ((row0 >> 8) * 1024 + (row0 & 255) + tr) * 8 + 4096 : OFF_ST3 + (((row0 + tr) >> 6) * 512 + lane) * 8);
    const char* cp = ws + OFF_CSP + (hg * G1_COLS + (t < G1_COLS ? t : 0)) * 64;
    u32x2 g[8];
    f32x4 c0, c1, c2, c3;
    asm volatile("global_load_dwordx4 %0, %4, off\n\tglobal_load_dwordx4 %1, %4, off offset:16\n\t"
                 "global_load_dwordx4 %2, %4, off offset:32\n\tglobal_load_dwordx4 %3, %4, off offset:48"
                 : "=&v"(c0), "=&v"(c1), "=&v"(c2), "=&v"(c3) : "v"(cp) : "memory");
    if (C4) {
      asm volatile("global_load_dwordx2 %0, %4, off offset:-4096 sc1\n\tglobal_load_dwordx2 %1, %4, off offset:-2048 sc1\n\t"
                   "global_load_dwordx2 %2, %4, off sc1\n\tglobal_load_dwordx2 %3, %4, off offset:2048 sc1"
                   : "=&v"(g[0]), "=&v"(g[1]), "=&v"(g[2]), "=&v"(g[3]) : "v"(gp) : "memory");
      wait_vm<G1_NP + 8>();
    } else {
      asm volatile("global_load_dwordx2 %0, %8, off\n\tglobal_load_dwordx2 %1, %8, off offset:512\n\tglobal_load_dwordx2 %2, %8, off offset:1024\n\t"
                   "global_load_dwordx2 %3, %8, off offset:1536\n\tglobal_load_dwordx2 %4, %8, off offset:2048\n\tglobal_load_dwordx2 %5, %8, off offset:2560\n\t"
                   "global_load_dwordx2 %6, %8, off offset:3072\n\tglobal_load_dwordx2 %7, %8, off offset:3584"
                   : "=&v"(g[0]), "=&v"(g[1]), "=&v"(g[2]), "=&v"(g[3]), "=&v"(g[4]), "=&v"(g[5]), "=&v"(g[6]), "=&v"(g[7]) : "v"(gp) : "memory");
      wait_vm<G1_NP + 12>();
    }
    BAR();
    if (C4) asm volatile("s_waitcnt vmcnt(0)" : "+v"(g[0]), "+v"(g[1]), "+v"(g[2]), "+v"(g[3]), "+v"(c0), "+v"(c1), "+v"(c2), "+v"(c3)::"memory");
    else asm volatile("s_waitcnt vmcnt(0)" : "+v"(g[0]), "+v"(g[1]), "+v"(g[2]), "+v"(g[3]), "+v"(g[4]), "+v"(g[5]), "+v"(g[6]), "+v"(g[7]),
                      "+v"(c0), "+v"(c1), "+v"(c2), "+v"(c3)::"memory");
    BAR();
    if (C4) {
#pragma unroll 1
      for (int spin = 0; spin < (1 << 20); ++spin) {
        if (!__any(g[0][1] == ~0u || g[1][1] == ~0u || g[2][1] == ~0u || g[3][1] == ~0u)) break;
        __builtin_amdgcn_s_sleep(4);
#pragma unroll
        for (int k = 0; k < 4; ++k) g[k] = poll8(gp + (k - 2) * 2048);
      }
    }
    float sm = 0.f, sq = 0.f, cs = 0.f;
#pragma unroll
    for (int k = 0; k < (C4 ? 4 : 8); ++k) { sm = opq(sm + __uint_as_float(g[k][0])); sq = opq(sq + __uint_as_float(g[k][1])); }
#pragma unroll
    for (int k = 0; k < 4; ++k) cs = opq(opq(opq(opq(cs + c0[k]) + c1[k]) + c2[k]) + c3[k]);
    const f32x2 st = ln_stats(sm, sq);
    Rs[t] = st[1]; MRs[t] = -st[0] * st[1];
    if (t < G1_COLS) CSs[t] = cs;
    WAIT_LGKM0();
    BAR();
    BAR();
    STAMPW(kid, 4, 4);
    float* kvacc = (float*)(ws + KVO);
    if (TALL) {
#pragma unroll
      for (int i = 0; i < 4; ++i) {
        const int e = t + 256 * i;
        atomicAdd(kvacc + hg * 1024 + e, (P[e] + P[1056 + e]) + (P[2112 + e] + P[3168 + e]));
      }
      if (t < 32) atomicAdd(kvacc + 32768 + hg * 32 + t, (P[1024 + t] + P[2080 + t]) + (P[3136 + t] + P[4192 + t]));
    } else {
#pragma unroll
      for (int i = 0; i < 8; ++i) {
        const int e = t + 256 * (i & 3), hd = i >> 2;
        atomicAdd(kvacc + (hg * 2 + hd) * 1024 + e, P[hd * 1056 + e] + P[(2 + hd) * 1056 + e]);
      }
      if (t < 64) { const int hd = t >> 5, d = t & 31; atomicAdd(kvacc + 32768 + hg * 64 + t, P[hd * 1056 + 1024 + d] + P[(2 + hd) * 1056 + 1024 + d]); }
    }
    DRAIN(); STAMPW(kid, 5, 4);
    return;
  }
  const int r = lane & 15, kq = lane >> 4, rh = TALL ? w : w >> 1, hh = TALL ? 0 : w & 1, h = hg * G1_HP + hh;
  constexpr int AO = G1_ROWS * 16, BO = G1_COLS * 16, PS = 32 * G1_HP;
  const char* ab = C4 ? smem + kq * AO + ((64 * rh + r) ^ (16 * (kq & 1))) * 8 : smem + kq * AO + (64 * rh + r) * 16;
  const char* ab2 = ab + 128 - (kq & 1) * 256;
  const char* bb = smem + G1_AB + kq * BO + (32 * hh + r) * 16;
  f32x4 acc[4][6];
#pragma unroll
  for (int m = 0; m < 4; ++m)
#pragma unroll
    for (int p = 0; p < 6; ++p) { acc[m][p] = (f32x4){0.f, 0.f, 0.f, 0.f}; asm volatile("" : "+v"(acc[m][p])); }
  bf16x8 F[2][10];
  auto rd1 = [&](int buf, const char* a, const char* a2, const char* b, int t, int i) {
    if (i < 4) {
      if (C4) {
        const char* p = ((i & 1) ? a2 : a) + t * 4 * AO + (i >> 1) * 256;
        const u32x2 lo = *(const u32x2*)p, hi = *(const u32x2*)(p + AO / 2);
        F[buf][i] = as_frag((u32x4){lo[0], lo[1], hi[0], hi[1]});
      } else F[buf][i] = *(const bf16x8*)(a + t * 4 * AO + i * 256);
    } else F[buf][i] = *(const bf16x8*)(b + t * 4 * BO + ((i - 4) >> 1) * PS * 16 + ((i - 4) & 1) * 256);
  };
  auto step = [&](int cb, const char* a, const char* a2, const char* b, int t, bool rd) {
    const int ord[10] = {0, 4, 5, 6, 7, 8, 9, 1, 2, 3};
#pragma unroll
    for (int i = 0; i < 24; ++i) {
      const int m = i / 6, p = i % 6;
      if (p < 2) acc[m][p] = MFMA16(F[cb][4 + p], F[cb][m], acc[m][p]);
      else acc[m][p] = MFMA16(F[cb][m], F[cb][4 + p], acc[m][p]);
      SB();
      if (rd && (i & 1) && (i >> 1) < 10) { rd1(cb ^ 1, a, a2, b, t, ord[i >> 1]); SB(); }
    }
  };
  BAR();
  STAMP(kid, 1); CSTAMP(kid + 8, 1); STAMP(kid + 8, 0);
#pragma unroll
  for (int i = 0; i < 10; ++i) rd1(0, ab, ab2, bb, 0, i);
  int so = 0;
  for (int it = 0; it < 15; ++it) {
    step(0, ab + so, ab2 + so, bb + so, 1, true);
    WAIT_LGKM0();
    BAR();
    so = (so == (G1_NS - 1) * G1_STAGE) ? 0 : so + G1_STAGE;
    step(1, ab + so, ab2 + so, bb + so, 0, true);
  }
  step(0, ab + so, ab2 + so, bb + so, 1, true);
  BAR();
  step(1, ab, ab2, bb, 0, false);
  STAMP(kid, 2); CSTAMP(kid + 8, 2); STAMP(kid + 8, 3);
  f32x4 Z[2][2], Zz[2];
#pragma unroll
  for (int i = 0; i < 2; ++i) { Z[i][0] = (f32x4){0.f, 0.f, 0.f, 0.f}; Z[i][1] = Z[i][0]; Zz[i] = Z[i][0]; }
  {
    float ck[4];
#pragma unroll
    for (int p = 0; p < 4; ++p) ck[p] = CSs[PS + (p >> 1) * PS + 32 * hh + (p & 1) * 16 + r];
    const bf16x8 ones = as_frag((u32x4){0x3f803f80u, 0x3f803f80u, 0x3f803f80u, 0x3f803f80u});
#pragma unroll
    for (int jj = 0; jj < 2; ++jj) {
      u32x4 fr[4];
#pragma unroll
      for (int mm = 0; mm < 2; ++mm) {
        const int m = 2 * jj + mm;
        const f32x4 rs = *(const f32x4*)(Rs + 64 * rh + 16 * m + 4 * kq), mr = *(const f32x4*)(MRs + 64 * rh + 16 * m + 4 * kq);
#pragma unroll
        for (int p = 0; p < 2; ++p) {
          fr[p][2 * mm] = pk2v(phi2(lo2(acc[m][2 + p]) * lo2(rs) + (lo2(mr) * ck[p] + 1e-6f)));
          fr[p][2 * mm + 1] = pk2v(phi2(hi2(acc[m][2 + p]) * hi2(rs) + (hi2(mr) * ck[p] + 1e-6f)));
          fr[2 + p][2 * mm] = pk2v(lo2(acc[m][4 + p]) * lo2(rs) + lo2(mr) * ck[2 + p]);
          fr[2 + p][2 * mm + 1] = pk2v(hi2(acc[m][4 + p]) * hi2(rs) + hi2(mr) * ck[2 + p]);
        }
      }
#pragma unroll
      for (int dh = 0; dh < 2; ++dh) {
        Z[0][dh] = MFMA16(as_frag(fr[dh]), as_frag(fr[2]), Z[0][dh]);
        Z[1][dh] = MFMA16(as_frag(fr[dh]), as_frag(fr[3]), Z[1][dh]);
        Zz[dh] = MFMA16(as_frag(fr[dh]), ones, Zz[dh]);
      }
    }
  }
  {
    float* mine = P + w * 1056;
#pragma unroll
    for (int i = 0; i < 4; ++i) *(f32x4*)(mine + i * 256 + lane * 4) = Z[i >> 1][i & 1];
    if (r == 0) { *(f32x4*)(mine + 1024 + 4 * kq) = Zz[0]; *(f32x4*)(mine + 1040 + 4 * kq) = Zz[1]; }
  }
  WAIT_LGKM0();
  BAR();
  STAMP(kid, 3);
  {
    const f32x4 c0 = *(const f32x4*)(CSs + 32 * hh + 4 * kq), c1 = *(const f32x4*)(CSs + 32 * hh + 16 + 4 * kq);
#pragma unroll
    for (int m = 0; m < 4; ++m) {
      const float rs = Rs[64 * rh + 16 * m + r], mr = MRs[64 * rh + 16 * m + r];
      const u32x4 o = {pk2v(phi2(lo2(acc[m][0]) * rs + (lo2(c0) * mr + 1e-6f))), pk2v(phi2(hi2(acc[m][0]) * rs + (hi2(c0) * mr + 1e-6f))),
                       pk2v(phi2(lo2(acc[m][1]) * rs + (lo2(c1) * mr + 1e-6f))), pk2v(phi2(hi2(acc[m][1]) * rs + (hi2(c1) * mr + 1e-6f)))};
      st16(rw, OFF_PQ + ((((h >> 2) * 128 + band * (G1_ROWS / 16) + rh * 4 + m) * 4 + (h & 3)) * 64 + lane) * 16, o);
    }
  }
  STAMP(kid, 6); DRAIN(); STAMP(kid, 7);
}

constexpr int G2_LDS = 131072 + 2048 + 4096;
template <int FINAL>
__global__ __launch_bounds__(512, 2) void g2_kernel(char* __restrict__ ws, char* __restrict__ out) {
  extern __shared__ __attribute__((aligned(16))) char smem[];
  constexpr int kid = FINAL ? 5 : 3;
  constexpr unsigned KVO = OFF_KV + (FINAL ? KV_LAYER * 4 : 0);
  STAMP(kid, 0);
  const int tid = threadIdx.x, lane = tid & 63, w = __builtin_amdgcn_readfirstlane(tid >> 6);
  const int xcd = blockIdx.x & 7, idx = blockIdx.x >> 3;
  const int sb = (xcd >> 1) * 8 + (idx & 7), nb = (xcd & 1) * 4 + (idx >> 3);
  const rsrc_t rw = mk_rsrc(ws, WS_USED);
  const unsigned l16 = lane * 16;
  u32x4* O = (u32x4*)smem;
  if (w < 4) {
    const int kq = lane >> 4;
    constexpr int PL = 2, PD = PL + 1;
    u32x4 pq[PD][4];
    f32x4 kv[PD][4];
    const unsigned spq = OFF_PQ + (sb * 16 + w) * 1024, skv = KVO + w * 4096;
    auto ld = [&](int i, int n) {
      if (n < 4) kv[i % PD][n] = __builtin_bit_cast(f32x4, ld16(rw, l16 + n * 1024, skv + i * 16384));
      else pq[i % PD][n - 4] = ld16(rw, l16, spq + (i * 512 + (n - 4) * 4) * 1024);
      SB();
    };
    const f32x4 zall = __builtin_bit_cast(f32x4, ld16(rw, (lane >> 3) * 512 + (lane & 7) * 16, KVO + 131072 + w * 128));
    SB();
#pragma unroll
    for (int n = 0; n < 8 * PL; ++n) ld(n >> 3, n & 7);
    u32x2* Zs = (u32x2*)(smem + 131072) + w * 64;
    Zs[lane] = (u32x2){pk2v(lo2(zall)), pk2v(hi2(zall))};
    const f32x4 zero = {0.f, 0.f, 0.f, 0.f}, eps = {1e-6f, 1e-6f, 1e-6f, 1e-6f};
    bf16x8 k0, k1, zb;
    auto prep = [&](int i) {
      const int b = i % PD;
      const u32x2 za = Zs[i * 8 + kq], zc = Zs[i * 8 + 4 + kq];
      k0 = as_frag((u32x4){pk2v(lo2(kv[b][0])), pk2v(hi2(kv[b][0])), pk2v(lo2(kv[b][1])), pk2v(hi2(kv[b][1]))});
      k1 = as_frag((u32x4){pk2v(lo2(kv[b][2])), pk2v(hi2(kv[b][2])), pk2v(lo2(kv[b][3])), pk2v(hi2(kv[b][3]))});
      zb = as_frag((u32x4){za[0], za[1], zc[0], zc[1]});
      asm volatile("" : "+v"(k0), "+v"(k1), "+v"(zb));
    };
    prep(0);
#pragma unroll
    for (int i = 0; i < 8; ++i) {
      const int b = i % PD;
      f32x4 x0[4], x1[4], dn[4];
      __builtin_amdgcn_s_setprio(2);
#pragma unroll
      for (int st = 0; st < 4; ++st) {
        dn[st] = MFMA16(zb, as_frag(pq[b][st]), eps);
        x0[st] = MFMA16(k0, as_frag(pq[b][st]), zero);
        x1[st] = MFMA16(k1, as_frag(pq[b][st]), zero);
      }
      __builtin_amdgcn_s_setprio(0);
      SB();
#pragma unroll
      for (int st = 0; st < 4; ++st) {
        const float inv = __builtin_amdgcn_rcpf(dn[st][0]);
        O[((i * 4 + w) * 4 + st) * 64 + lane] =
            (u32x4){pk2(opq(x0[st][0] * inv), opq(x0[st][1] * inv)), pk2(opq(x0[st][2] * inv), opq(x0[st][3] * inv)),
                    pk2(opq(x1[st][0] * inv), opq(x1[st][1] * inv)), pk2(opq(x1[st][2] * inv), opq(x1[st][3] * inv))};
        SB();
        if (i + PL < 8) { ld(i + PL, 2 * st); ld(i + PL, 2 * st + 1); }
      }
      if (i < 7) prep(i + 1);
      WAIT_LGKM0();
      BAR();
      if (i == 0) STAMP(kid, 1);
    }
    return;
  }
  const int c = w - 4, n0 = nb * 128 + c * 32, s0 = sb * 64, r = lane & 31, hf = lane >> 5;
  constexpr int CL = 2, CD = CL + 1, LA = 6;
  u32x4 wo[CD][8], of[8];
  const unsigned swo = OFF_WOF + (nb * 4 + c) * 8192;
  auto ldw = [&](int i, int n) { wo[i % CD][n] = ld16(rw, l16 + (n & 3) * 1024, swo + i * 262144 + (n >> 2) * 4096); SB(); };
#pragma unroll
  for (int n = 0; n < 8 * CL; ++n) ldw(n >> 3, n & 7);
  f32x16 acc[2];
#pragma unroll
  for (int i = 0; i < 16; ++i) { acc[0][i] = 0.f; acc[1][i] = 0.f; }
  asm volatile("" : "+v"(acc[0]), "+v"(acc[1]));
  u32x4 res[2][2], yv[2][2];
  auto ldres = [&](int k) {
    const int s2 = (k >> 1) & 1, g = k & 1;
    if (FINAL) { if (k < 4) res[s2][g] = ld16(rw, (2 * hf * S + r) * 16 + s2 * 512, OFF_X3 + (((n0 >> 3) + g) * S + s0) * 16); }
    else {
      const unsigned so = (k < 4 ? OFF_X1 : OFF_Y) + (((n0 >> 2) + 2 * g) * S + s0) * 8, vo = (4 * hf * S + r) * 8 + s2 * 256;
      const u32x2 lo = ld8(rw, vo, so), hi = ld8(rw, vo, so + S * 8);
      if (k < 4) res[s2][g] = (u32x4){lo[0], lo[1], hi[0], hi[1]}; else yv[s2][g] = (u32x4){lo[0], lo[1], hi[0], hi[1]};
    }
    SB();
  };
  const u32x4* Ob = O + (r >> 4) * 64 + hf * 16 + (r & 15);
  auto rdo = [&](int f) { const int u = f & 15; of[f & 7] = Ob[((f >> 4) * 16 + (u >> 2) * 4 + (u & 1) * 2) * 64 + ((u >> 1) & 1) * 32]; SB(); };
  __builtin_amdgcn_s_setprio(1);
  BAR();
#pragma unroll
  for (int f = 0; f < LA; ++f) rdo(f);
#pragma unroll
  for (int i = 0; i < 8; ++i)
#pragma unroll
    for (int n = 0; n < 16; ++n) {
      const int f = i * 16 + n;
      acc[n & 1] = MFMA32(as_frag(wo[i % CD][n >> 1]), as_frag(of[f & 7]), acc[n & 1]);
      SB();
      if (f + LA < 128) { if (((f + LA) & 15) == 0) BAR(); rdo(f + LA); }
      if ((n & 1) && i + CL < 8) ldw(i + CL, n >> 1);
      if (i == 6 && (n & 1)) ldres(n >> 1);
    }
  STAMPW(kid, 2, 4);
  u32x4 x[2][2];
#pragma unroll
  for (int s2 = 0; s2 < 2; ++s2)
#pragma unroll
    for (int g = 0; g < 2; ++g)
#pragma unroll
      for (int e = 0; e < 4; ++e) x[s2][g][e] = pk2(bflo(res[s2][g][e]) + acc[s2][8 * g + 2 * e], bfhi(res[s2][g][e]) + acc[s2][8 * g + 2 * e + 1]);
  if (FINAL) {
#pragma unroll
    for (int s2 = 0; s2 < 2; ++s2)
#pragma unroll
      for (int g = 0; g < 2; ++g) *(u32x4*)(out + ((size_t)(s0 + 32 * s2 + r) * D + n0 + 16 * hf + 8 * g) * 2) = x[s2][g];
  } else {
    f32x2* Red = (f32x2*)(smem + 131072 + 2048);
    auto rowpart = [&]() {
      float v[4] = {0.f, 0.f, 0.f, 0.f};
#pragma unroll
      for (int e = 0; e < 8; ++e)
#pragma unroll
        for (int s2 = 0; s2 < 2; ++s2) { const unsigned u = x[s2][e >> 2][e & 3]; v[2 * s2] = dot2(u, 0x3f803f80u, v[2 * s2]); v[2 * s2 + 1] = dot2(u, u, v[2 * s2 + 1]); }
      const auto a = __builtin_amdgcn_permlane32_swap(__float_as_uint(v[0]), __float_as_uint(v[2]), false, false);
      const auto b = __builtin_amdgcn_permlane32_swap(__float_as_uint(v[1]), __float_as_uint(v[3]), false, false);
      return (f32x2){__uint_as_float(a[0]) + __uint_as_float(a[1]), __uint_as_float(b[0]) + __uint_as_float(b[1])};
    };
    Red[c * 64 + lane] = rowpart();
    WAIT_LGKM0();
    BAR();
    char* gx = ws + OFF_GX2 + (sb * 512 + lane) * 8;
    if (c == 0) {
      const f32x2 t = (Red[lane] + Red[64 + lane]) + (Red[128 + lane] + Red[192 + lane]);
      publish8(gx + nb * 512, t[0], t[1]);
    }
    STAMPW(kid, 4, 4);
    float sm, sq;
#pragma unroll 1
    for (int spin = 0; spin < (1 << 20); ++spin) {
      u32x2 g[8];
#pragma unroll
      for (int k = 0; k < 8; ++k) g[k] = poll8(gx + k * 512);
      bool empty = false;
      sm = 0.f; sq = 0.f;
#pragma unroll
      for (int k = 0; k < 8; ++k) { empty |= g[k][1] == ~0u; sm += __uint_as_float(g[k][0]); sq += __uint_as_float(g[k][1]); }
      if (!__any(empty)) break;
      __builtin_amdgcn_s_sleep(1);
    }
    STAMPW(kid, 3, 4);
    const f32x2 st = ln_stats(sm, sq);
    const auto mm = __builtin_amdgcn_permlane32_swap(__float_as_uint(st[0]), __float_as_uint(st[0]), false, false);
    const auto rr = __builtin_amdgcn_permlane32_swap(__float_as_uint(st[1]), __float_as_uint(st[1]), false, false);
#pragma unroll
    for (int s2 = 0; s2 < 2; ++s2) {
      const float mu = __uint_as_float(mm[s2]), rs = __uint_as_float(rr[s2]);
#pragma unroll
      for (int g = 0; g < 2; ++g) {
#pragma unroll
        for (int e = 0; e < 4; ++e) {
          const float a = bflo(x[s2][g][e]), b = bfhi(x[s2][g][e]);
          x[s2][g][e] = pk2(a + (a - mu) * rs * bflo(yv[s2][g][e]), b + (b - mu) * rs * bfhi(yv[s2][g][e]));
        }
        st16(rw, OFF_X3 + (((n0 >> 3) + 2 * hf + g) * S + s0 + 32 * s2 + r) * 16, x[s2][g]);
      }
    }
    Red[256 + c * 64 + lane] = rowpart();
    WAIT_LGKM0();
    BAR();
    if (c == 0) {
      const f32x2 t = (Red[256 + lane] + Red[320 + lane]) + (Red[384 + lane] + Red[448 + lane]);
      __builtin_amdgcn_raw_buffer_store_b64(__builtin_bit_cast(u32x2, t), rw, OFF_ST3 + ((sb * 8 + nb) * 64 + lane) * 8, 0, 16);
    }
  }
  STAMPW(kid, 6, 4); DRAIN(); STAMPW(kid, 7, 4);
}

extern "C" void kernel_launch(void* const* d_in, const int* in_sizes, int n_in, void* d_out, int out_size, void* d_ws,
                              size_t ws_size, hipStream_t stream) {
  const uint16_t* q = (const uint16_t*)d_in[0];
  const uint16_t* k = (const uint16_t*)d_in[1];
  const uint16_t* v = (const uint16_t*)d_in[2];
  const uint16_t* shift_k = (const uint16_t*)d_in[3];
  const uint16_t* ssm_k = (const uint16_t*)d_in[4];
  const uint16_t* ssm_D = (const uint16_t*)d_in[5];
  const uint16_t* wqkv = (const uint16_t*)d_in[7];
  const uint16_t* wo = (const uint16_t*)d_in[8];
  char* ws = (char*)d_ws + (WSPOS == 1 ? ((ws_size - WS_USED) & ~(size_t)(2 * MB - 1)) : WSPOS == 2 ? ((ws_size / 2) & ~(size_t)(2 * MB - 1)) : 0);
  (void)hipFuncSetAttribute((const void*)conv_kernel, hipFuncAttributeMaxDynamicSharedMemorySize, CV_LDS);
  (void)hipFuncSetAttribute((const void*)g1_kernel<0>, hipFuncAttributeMaxDynamicSharedMemorySize, G1_LDS);
  (void)hipFuncSetAttribute((const void*)g1_kernel<1>, hipFuncAttributeMaxDynamicSharedMemorySize, G1_LDS);
  (void)hipFuncSetAttribute((const void*)g2_kernel<0>, hipFuncAttributeMaxDynamicSharedMemorySize, G2_LDS);
  (void)hipFuncSetAttribute((const void*)g2_kernel<1>, hipFuncAttributeMaxDynamicSharedMemorySize, G2_LDS);
  prep_kernel<<<256, 512, 0, stream>>>(q, k, v, shift_k, ws);
  conv_kernel<<<256, 512, CV_LDS, stream>>>(ssm_k, ssm_D, wqkv, wo, ws);
  g1_kernel<1><<<256, 512, G1_LDS, stream>>>(ws);
  g2_kernel<0><<<256, 512, G2_LDS, stream>>>(ws, nullptr);
  g1_kernel<0><<<256, 512, G1_LDS, stream>>>(ws);
  g2_kernel<1><<<256, 512, G2_LDS, stream>>>(ws, (char*)d_out);
}
```

```cpp
#include <hip/hip_runtime.h>
#include <stdint.h>

typedef __attribute__((ext_vector_type(8))) short bf16x8;
typedef __attribute__((ext_vector_type(4))) short s16x4;
typedef __attribute__((ext_vector_type(4))) float f32x4;
typedef __attribute__((ext_vector_type(2))) float f32x2;
typedef __attribute__((ext_vector_type(16))) float f32x16;
typedef __attribute__((ext_vector_type(4))) unsigned u32x4;
typedef __attribute__((ext_vector_type(2))) unsigned u32x2;
typedef __attribute__((ext_vector_type(2))) __bf16 bf16x2_t;
#define DI __device__ __forceinline__
#define LAS __attribute__((address_space(3)))
#define STAMPW(kid, slot, wv) do {} while (0)
#define CSTAMP(kid, slot) do {} while (0)
#define DRAIN() do {} while (0)
#define STAMP(kid, slot) STAMPW(kid, slot, 0)

constexpr int S = 2048, D = 1024;
#define TALL 1
constexpr int G1_ROWS = TALL ? 256 : 128, G1_HP = TALL ? 1 : 2, G1_COLS = 96 * G1_HP;
constexpr unsigned MB = 1024 * 1024;
constexpr unsigned OFF_UB = 0, OFF_X0 = 4 * MB, OFF_W8 = 8 * MB, OFF_WOF = 14 * MB, OFF_Y = 16 * MB, OFF_X1 = 20 * MB,
                   OFF_PQ = 24 * MB, OFF_X2 = 28 * MB, OFF_X3 = 32 * MB, OFF_SM = 36 * MB, WS_USED = 37 * MB;
constexpr unsigned OFF_ST0 = OFF_SM, OFF_CSP = OFF_SM + 16384,
                   OFF_KV = OFF_CSP + 16 * 3072 * 4,
                   OFF_GX = OFF_KV + 2 * 33792 * 4,
                   OFF_GX2 = OFF_GX + 65536,
                   OFF_ST3 = OFF_GX2 + 131072;
constexpr unsigned CODE_ALL = 58 * 1024, CODE_CONV = 7168, CODE_G1 = 10496, CODE_G2 = 12544, CODE_G2F = 10240;
constexpr int KV_LAYER = 32 * 1024 + 32 * 32;

DI float bflo(unsigned u) { return __uint_as_float(u << 16); }
DI float bfhi(unsigned u) { return __uint_as_float(u & 0xffff0000u); }
DI unsigned pk2(float a, float b) {
  f32x2 v = {a, b};
  bf16x2_t r = __builtin_convertvector(v, bf16x2_t);
  return __builtin_bit_cast(unsigned, r);
}
DI unsigned pk2v(f32x2 v) { return __builtin_bit_cast(unsigned, __builtin_convertvector(v, bf16x2_t)); }
DI float rbf(float a) { return bflo(pk2(a, a)); }
DI bf16x8 as_frag(u32x4 v) { return __builtin_bit_cast(bf16x8, v); }
DI float dot2(unsigned a, unsigned b, float c) {
  return __builtin_amdgcn_fdot2_f32_bf16(__builtin_bit_cast(bf16x2_t, a), __builtin_bit_cast(bf16x2_t, b), c, false);
}
#define MFMA16(a, b, c) __builtin_amdgcn_mfma_f32_16x16x32_bf16((a), (b), (c), 0, 0, 0)
#define MFMA32(a, b, c) __builtin_amdgcn_mfma_f32_32x32x16_bf16((a), (b), (c), 0, 0, 0)
#define WAIT_VM(n) asm volatile("s_waitcnt vmcnt(" #n ")" ::: "memory")
#define WAIT_LGKM0() asm volatile("s_waitcnt lgkmcnt(0)" ::: "memory")
#define BAR() do { __builtin_amdgcn_s_barrier(); asm volatile("" ::: "memory"); } while (0)
#define SB() __builtin_amdgcn_sched_barrier(0)
DI void glds16(const void* g, char* l) {
  __builtin_amdgcn_global_load_lds((const __attribute__((address_space(1))) void*)g, (LAS void*)l, 16, 0, 0);
}
typedef __amdgpu_buffer_rsrc_t rsrc_t;
DI rsrc_t mk_rsrc(void* p, unsigned bytes) { return __builtin_amdgcn_make_buffer_rsrc(p, 0, bytes, 0x00020000); }
DI void dma16(rsrc_t r, char* l, unsigned voff, unsigned soff) { __builtin_amdgcn_raw_ptr_buffer_load_lds(r, (LAS void*)l, 16, voff, soff, 0, 0); }
template <int N> DI void wait_vm() { asm volatile("s_waitcnt vmcnt(%0)" ::"i"(N) : "memory"); }
DI void publish8(char* p, float sm, float sq) {
  unsigned qb = __float_as_uint(sq);
  if (qb == ~0u) qb = 0x7fc00000u;
  __hip_atomic_store((unsigned long long*)p, ((unsigned long long)qb << 32) | __float_as_uint(sm), __ATOMIC_RELAXED, __HIP_MEMORY_SCOPE_AGENT);
}
DI u32x2 poll8(const char* p) { return __builtin_bit_cast(u32x2, __hip_atomic_load((const unsigned long long*)p, __ATOMIC_RELAXED, __HIP_MEMORY_SCOPE_AGENT)); }
DI u32x4 ld16(rsrc_t r, unsigned voff, unsigned soff) { return __builtin_amdgcn_raw_buffer_load_b128(r, voff, soff, 0); }
DI u32x2 ld8(rsrc_t r, unsigned voff, unsigned soff) { return __builtin_amdgcn_raw_buffer_load_b64(r, voff, soff, 0); }
DI void st16(rsrc_t r, unsigned off, u32x4 v) { __builtin_amdgcn_raw_buffer_store_b128(v, r, off, 0, 16); }

DI float opq(float m) { asm("" : "+v"(m)); return m; }
#define CODEPF 1
DI rsrc_t code_rsrc(unsigned bytes) {
  unsigned long long pc;
  asm volatile("s_getpc_b64 %0" : "=s"(pc));
  return mk_rsrc((void*)(pc & ~255ull), bytes);
}
DI unsigned code_touch(rsrc_t r, int lane) {
  if (!CODEPF) return 0u;
  return __builtin_amdgcn_raw_buffer_load_b32(r, lane * 128, 0, 0) ^ __builtin_amdgcn_raw_buffer_load_b32(r, lane * 128, 8192, 0);
}
#define KEEP(x) asm volatile("" ::"v"(x))
template <int N> DI float row_ror(float x) {
  return __int_as_float(__builtin_amdgcn_update_dpp(0, __float_as_int(x), 0x120 + N, 0xf, 0xf, false));
}
DI float sum_x16(float x) { const auto p = __builtin_amdgcn_permlane16_swap(__float_as_uint(x), __float_as_uint(x), false, false); return __uint_as_float(p[0]) + __uint_as_float(p[1]); }
DI float sum_x32(float x) { const auto p = __builtin_amdgcn_permlane32_swap(__float_as_uint(x), __float_as_uint(x), false, false); return __uint_as_float(p[0]) + __uint_as_float(p[1]); }

DI f32x2 ln_stats(float s, float q) {
  float m = s * (1.0f / 1024.0f);
  float v = fmaxf(q * (1.0f / 1024.0f) - m * m, 0.0f);
  float sd = rbf(sqrtf(rbf(rbf(v) + 1e-5f)));
  f32x2 r = {rbf(m), 1.0f / sd};
  return r;
}

__global__ __launch_bounds__(512) void prep_kernel(const uint16_t* __restrict__ q, const uint16_t* __restrict__ k,
                                                   const uint16_t* __restrict__ v, const uint16_t* __restrict__ shift_k,
                                                   char* __restrict__ ws) {
  __shared__ __attribute__((aligned(16))) u32x4 bounce[8][256];
  __shared__ float red[4][16];
  const rsrc_t rc = code_rsrc(CODE_ALL);
  STAMP(0, 0);
  const int lane = threadIdx.x & 63, w8 = __builtin_amdgcn_readfirstlane(threadIdx.x >> 6), w = w8 & 3, tid = threadIdx.x & 255;
  const int bid = blockIdx.x, s0 = bid * 8;
  const rsrc_t rw = mk_rsrc(ws, WS_USED);
  const int sw = (lane >> 1) & 3;
  if (w8 < 4) {
    u32x2 kk[11], vv[8];
#pragma unroll
    for (int r = 0; r < 11; ++r) {
      const int s = (r < 3) ? max(s0 - 3 + r, 0) : s0 - 3 + r;
      kk[r] = *(const u32x2*)(k + (size_t)s * D + tid * 4);
    }
#pragma unroll
    for (int r = 0; r < 8; ++r) vv[r] = *(const u32x2*)(v + (size_t)(s0 + r) * D + tid * 4);
    const u32x4 wa = *(const u32x4*)(shift_k + tid * 16), wb = *(const u32x4*)(shift_k + tid * 16 + 8);
    asm volatile("" ::"v"(vv[7]), "v"(wb));
    STAMP(0, 1);
    const unsigned hm = bid ? ~0u : 0u;
#pragma unroll
    for (int r = 0; r < 3; ++r) { kk[r][0] &= hm; kk[r][1] &= hm; }
#pragma unroll
    for (int c = 0; c < 4; ++c) {
      const unsigned w01 = (c == 0) ? wa[0] : (c == 1) ? wa[2] : (c == 2) ? wb[0] : wb[2];
      const unsigned w23 = (c == 0) ? wa[1] : (c == 1) ? wa[3] : (c == 2) ? wb[1] : wb[3];
      const float w0 = bflo(w01), w1 = bfhi(w01), w2 = bflo(w23), w3 = bfhi(w23);
      float kf[11], u[8];
#pragma unroll
      for (int r = 0; r < 11; ++r) kf[r] = (c & 1) ? bfhi(kk[r][c >> 1]) : bflo(kk[r][c >> 1]);
#pragma unroll
      for (int r = 0; r < 8; ++r) {
        const float ks = kf[r] * w3 + kf[r + 1] * w2 + kf[r + 2] * w1 + kf[r + 3] * w0;
        u[r] = ks * ((c & 1) ? bfhi(vv[r][c >> 1]) : bflo(vv[r][c >> 1]));
      }
      bounce[w8][lane * 4 + (c ^ sw)] = (u32x4){pk2(u[7], u[6]), pk2(u[5], u[4]), pk2(u[3], u[2]), pk2(u[1], u[0])};
    }
#pragma unroll
    for (int i = 0; i < 4; ++i) {
      const int u = 64 * i + lane;
      st16(rw, OFF_UB + (bid * 1024 + w * 256 + u) * 16, bounce[w8][(u & ~3) | ((u & 3) ^ ((u >> 3) & 3))]);
    }
    STAMP(0, 6);
    BAR();
    DRAIN(); STAMP(0, 7);
    return;
  }
  u32x2 qq[8];
#pragma unroll
  for (int r = 0; r < 8; ++r) qq[r] = *(const u32x2*)(q + (size_t)(s0 + r) * D + tid * 4);
  SB();
  unsigned pf = 0u;
  if (CODEPF && w8 == 4) pf = __builtin_amdgcn_raw_buffer_load_b32(rc, lane < 34 ? lane * 128 : (bid * 2 + (lane & 1)) * 128, 0, 0);
  SB();
  STAMPW(0, 2, 4);
#pragma unroll
  for (int r = 0; r < 4; ++r) bounce[w8][lane * 4 + (r ^ sw)] = (u32x4){qq[2 * r][0], qq[2 * r][1], qq[2 * r + 1][0], qq[2 * r + 1][1]};
#pragma unroll
  for (int i = 0; i < 4; ++i) {
    const int u = 64 * i + lane;
    st16(rw, OFF_X0 + (bid * 1024 + w * 256 + u) * 16, bounce[w8][(u & ~3) | ((u & 3) ^ ((u >> 3) & 3))]);
  }
  if (tid < 48) st16(rw, OFF_GX + bid * 768 + tid * 16, (u32x4){~0u, ~0u, ~0u, ~0u});
  float a[16];
#pragma unroll
  for (int r = 0; r < 8; ++r) {
    a[2 * r] = dot2(qq[r][0], 0x3f803f80u, dot2(qq[r][1], 0x3f803f80u, 0.f));
    a[2 * r + 1] = dot2(qq[r][0], qq[r][0], dot2(qq[r][1], qq[r][1], 0.f));
  }
#pragma unroll
  for (int i = 0; i < 8; ++i) { const auto p = __builtin_amdgcn_permlane32_swap(__float_as_uint(a[i]), __float_as_uint(a[i + 8]), false, false); a[i] = __uint_as_float(p[0]) + __uint_as_float(p[1]); }
#pragma unroll
  for (int i = 0; i < 4; ++i) { const auto p = __builtin_amdgcn_permlane16_swap(__float_as_uint(a[i]), __float_as_uint(a[i + 4]), false, false); a[i] = __uint_as_float(p[0]) + __uint_as_float(p[1]); }
#pragma unroll
  for (int i = 0; i < 4; ++i) { a[i] += row_ror<1>(a[i]); a[i] += row_ror<2>(a[i]); a[i] += row_ror<4>(a[i]); a[i] += row_ror<8>(a[i]); }
  if ((lane & 15) == 0) *(f32x4*)&red[w][(lane >> 4) * 4] = (f32x4){a[0], a[1], a[2], a[3]};
  WAIT_LGKM0();
  STAMPW(0, 3, 4);
  BAR();
  if (tid < 8) {
    const float sm = (red[0][2 * tid] + red[1][2 * tid]) + (red[2][2 * tid] + red[3][2 * tid]);
    const float sq = (red[0][2 * tid + 1] + red[1][2 * tid + 1]) + (red[2][2 * tid + 1] + red[3][2 * tid + 1]);
    const f32x2 st = ln_stats(sm, sq);
    __builtin_amdgcn_raw_buffer_store_b64(__builtin_bit_cast(u32x2, st), rw, OFF_ST0 + (s0 + tid) * 8, 0, 16);
  }
  KEEP(pf);
  STAMPW(0, 4, 4); DRAIN(); STAMPW(0, 5, 4);
}

constexpr int CV_U = 286 * 16;
constexpr int CV_HA = CV_U;
constexpr int CV_HB = CV_HA + 4160;
constexpr int CV_CH = CV_HB + 4128 + 32;
#define WEARLY 0
constexpr int CV_PAD = 64;
constexpr int CV_X0 = 4 * CV_CH + 4 * 8192, CV_ST = CV_X0 + 16384;
constexpr int CV_LDS = CV_PAD + CV_ST + 16384;
__global__ __launch_bounds__(512, 2) void conv_kernel(const uint16_t* __restrict__ ssm_k, const uint16_t* __restrict__ ssm_D,
                                                      const uint16_t* __restrict__ wqkv, const uint16_t* __restrict__ wo,
                                                      char* __restrict__ ws) {
  extern __shared__ __attribute__((aligned(16))) char smem_raw[];
  char* smem = smem_raw + CV_PAD;
  const rsrc_t rc = code_rsrc(CODE_CONV);
  STAMP(1, 0);
  const int tid = threadIdx.x, lane = tid & 63, w = __builtin_amdgcn_readfirstlane(tid >> 6);
  const rsrc_t rw = mk_rsrc(ws, WS_USED);
  const int j = (blockIdx.x & 7) * 32 + (blockIdx.x >> 3);
  const unsigned cnt = (unsigned)(size_t)(LAS char*)smem_raw;
  auto arrive = [&]() { if (lane == 0) asm volatile("ds_add_u32 %0, %1" ::"v"(cnt), "v"(1u) : "memory"); };
  if (w >= 4) {
    const int gw = blockIdx.x * 4 + (w - 4);
    char* buf = smem + 4 * CV_CH + (w - 4) * 8192;
    const int g = lane >> 4, qq = (lane >> 2) & 3, p = lane & 3;
    const bool isw = gw < 768;
    const int cg = isw ? gw % 48 : (gw - 768) & 15, og = isw ? gw / 48 : (gw - 768) >> 4;
    const int rr = lane >> 3;
    auto w_loads = [&]() {
      if (isw) {
        const uint16_t* src = wqkv + (size_t)(og * 64 + rr) * 3072 + cg * 64 + (lane & 7) * 8;
#pragma unroll
        for (int i = 0; i < 8; ++i) glds16(src + (size_t)i * 8 * 3072, buf + i * 1024);
      } else {
        const uint16_t* src = wo + (size_t)(og * 64 + (rr & 3) + 16 * (rr >> 2)) * 1024 + cg * 64 + (lane & 7) * 8;
#pragma unroll
        for (int i = 0; i < 8; ++i) glds16(src + (size_t)((i >> 2) * 32 + (i & 3) * 4) * 1024, buf + i * 1024);
      }
    };
    if (w == 4) KEEP(code_touch(rc, lane));
    if (WEARLY) w_loads();
    BAR();
#pragma unroll
    for (int i = 0; i < 4; ++i) {
      const int pp = (w - 4) * 256 + i * 64 + lane;
      glds16((const u32x4*)(ws + OFF_X0) + ((size_t)(pp >> 2) * 256 + j) * 4 + (pp & 3), smem + CV_X0 + ((w - 4) * 4 + i) * 1024);
      glds16((const u32x4*)(ws + OFF_ST0) + pp, smem + CV_ST + ((w - 4) * 4 + i) * 1024);
    }
    if (!WEARLY) { w_loads(); WAIT_VM(8); arrive(); }
    WAIT_VM(0);
    if (WEARLY) arrive();
    STAMPW(1, 3, 4);
    const int col = TALL ? ((cg & 15) * 2 + (lane >> 5)) * 96 + (cg >> 4) * 32 + (lane & 31) : (cg & 15) * 192 + (cg >> 4) * 64 + lane;
    const unsigned dst = isw ? OFF_W8 + (((col / G1_COLS) * 128 + og * 8) * G1_COLS + col % G1_COLS) * 16
                             : OFF_WOF + ((((og >> 1) * 32 + cg * 2 + (g >> 1)) * 8 + (og & 1) * 4) * 64 + (lane & 31)) * 16;
    const unsigned dstep = isw ? G1_COLS * 16 : 512;
    const unsigned la = (unsigned)(size_t)(LAS char*)buf + qq * 128 + (isw ? g * 32 + p * 8 : (g >> 1) * 64 + (p & 1) * 32 + ((g & 1) * 2 + (p >> 1)) * 8);
    u32x2 lo[8], hi[8];
#pragma unroll
    for (int i = 0; i < 8; ++i) {
      lo[i] = __builtin_bit_cast(u32x2, __builtin_amdgcn_ds_read_tr16_b64_v4i16((LAS s16x4*)(size_t)(la + i * 1024)));
      hi[i] = __builtin_bit_cast(u32x2, __builtin_amdgcn_ds_read_tr16_b64_v4i16((LAS s16x4*)(size_t)(la + i * 1024 + 512)));
    }
#pragma unroll
    for (int i = 0; i < 8; ++i) st16(rw, dst + i * dstep, (u32x4){lo[i][0], lo[i][1], hi[i][0], hi[i][1]});
    if (lane < 16 + 1 - (gw & 1)) st16(rw, OFF_KV + (gw * 33 / 2 + (gw & 1) + lane) * 16, (u32x4){0u, 0u, 0u, 0u});
    if (isw) {
      float c0 = 0.f, c1 = 0.f;
#pragma unroll
      for (int i = 0; i < 8; ++i)
#pragma unroll
        for (int e = 0; e < 2; ++e) {
          c0 = opq(opq(c0 + bflo(lo[i][e])) + bflo(hi[i][e]));
          c1 = opq(opq(c1 + bfhi(lo[i][e])) + bfhi(hi[i][e]));
        }
      __builtin_amdgcn_raw_buffer_store_b32(__float_as_uint(c0 + c1), rw, OFF_CSP + (col * 16 + og) * 4, 0, 16);
    }
    STAMPW(1, 4, 4); DRAIN(); STAMPW(1, 5, 4);
    return;
  }
  u32x4 uu[4], hh[4];
  unsigned hn[4];
  const uint16_t* hrow = ssm_k + (size_t)(4 * j + w) * S;
#pragma unroll
  for (int i = 0; i < 4; ++i) {
    const int c = i * 64 + lane;
    hh[i] = *(const u32x4*)(hrow + c * 8);
    hn[i] = *(const unsigned*)(hrow + min(c * 8 + 8, S - 2));
  }
  SB();
  const u32x4* ub = (const u32x4*)(ws + OFF_UB);
#pragma unroll
  for (int i = 0; i < 4; ++i) uu[i] = ub[(size_t)(w * 64 + i * 16 + (lane >> 2)) * 1024 + 4 * j + (lane & 3)];
  SB();
  const float Dc = bflo((unsigned)ssm_D[4 * j + w]);
  SB();
  char* my = smem + w * CV_CH;
  if (lane < 30) *(u32x4*)(my + lane * 16) = (u32x4){0u, 0u, 0u, 0u};
  if (lane >= 32 && lane < 34) *(u32x4*)(my + CV_HA + (lane - 32) * 16) = (u32x4){0u, 0u, 0u, 0u};
  if (lane == 34) *(u32x4*)(my + CV_HB) = (u32x4){0u, 0u, 0u, 0u};
  if (tid == 35) *(unsigned*)smem_raw = 0u;
  if (lane == 0) *(u32x4*)(my + CV_HB + 16) = (u32x4){0u, 0u, 0u, hh[0][0] << 16};
#pragma unroll
  for (int i = 0; i < 4; ++i) {
    const int c = i * 64 + lane;
    *(u32x4*)(my + CV_HA + 32 + c * 16) = hh[i];
    u32x4 o = {__builtin_amdgcn_alignbyte(hh[i][1], hh[i][0], 2), __builtin_amdgcn_alignbyte(hh[i][2], hh[i][1], 2),
               __builtin_amdgcn_alignbyte(hh[i][3], hh[i][2], 2), __builtin_amdgcn_alignbyte(hn[i], hh[i][3], 2)};
    *(u32x4*)(my + CV_HB + 32 + c * 16) = o;
  }
#pragma unroll
  for (int i = 0; i < 4; ++i) *(u32x4*)(smem + (lane & 3) * CV_CH + (30 + w * 64 + i * 16 + (lane >> 2)) * 16) = uu[i];
  const int t0 = lane & 15, kq = lane >> 4;
  const unsigned* fb = (const unsigned*)(my + ((t0 & 1) ? CV_HA : CV_HB)) + 4 * kq + ((1 + t0) >> 1);
  const char* gb = my + (2 * t0 + 3 - kq) * 16;
  f32x4 acc[8];
#pragma unroll
  for (int b = 0; b < 8; ++b) { acc[b] = (f32x4){0.f, 0.f, 0.f, 0.f}; asm volatile("" : "+v"(acc[b])); }
  WAIT_LGKM0();
  BAR();
  STAMP(1, 1); CSTAMP(9, 1); STAMP(9, 0);
  bf16x8 F[2][8], G[2][8];
  const LAS unsigned* fbk[4];
#pragma unroll
  for (int i = 0; i < 4; ++i) { fbk[i] = (const LAS unsigned*)fb + 256 * i; asm volatile("" : "+v"(fbk[i])); }
  u32x2 fh[8];
  auto rdn = [&](int buf, int g0, int n) {
    if (n == 0 || n > 16) { const int g1 = n ? n - 16 : 0; G[buf][g1] = *(const bf16x8*)(gb + (32 * g1 + 4 * (7 - g0)) * 16); }
    else {
      const int e1 = (n - 1) >> 1;
      const LAS unsigned* p = fbk[e1 >> 1] + 128 * (e1 & 1) + 16 * g0;
      if ((n - 1) & 1) F[buf][e1] = as_frag((u32x4){fh[e1][0], fh[e1][1], p[2], p[3]});
      else fh[e1] = (u32x2){p[0], p[1]};
    }
  };
  auto cstep = [&](int cb, int g0n) {
    int n = 0;
#pragma unroll
    for (int g1 = 0; g1 < 8; ++g1)
#pragma unroll
      for (int e1 = 0; e1 + g1 < 8; ++e1) {
        acc[g1 + e1] = MFMA16(F[cb][e1], G[cb][g1], acc[g1 + e1]);
        SB();
        if (n < 24) { rdn(cb ^ 1, g0n, n); SB(); }
        ++n;
      }
  };
#pragma unroll
  for (int n = 0; n < 24; ++n) rdn(0, 0, n);
  for (int gp = 0; gp < 4; ++gp) {
    cstep(0, 1);
    cstep(1, 2);
    gb -= 128;
#pragma unroll
    for (int i = 0; i < 4; ++i) fbk[i] += 32;
  }
  asm volatile("" ::"v"(acc[7]), "v"(acc[0]));
  STAMP(1, 2); CSTAMP(9, 2); STAMP(9, 3);
#pragma unroll
  for (int b = 0; b < 8; ++b) {
    const int s = 256 * b + 16 * t0 + 4 * kq;
    const u32x2 ur = *(const u32x2*)(my + (30 + (s >> 3)) * 16 + ((s & 4) ? 0 : 8));
    *(u32x2*)(my + CV_HA + s * 2) = (u32x2){pk2(acc[b][0] + bfhi(ur[1]) * Dc, acc[b][1] + bflo(ur[1]) * Dc),
                                            pk2(acc[b][2] + bfhi(ur[0]) * Dc, acc[b][3] + bflo(ur[0]) * Dc)};
  }
  WAIT_LGKM0();
  arrive();
  while (*(volatile LAS unsigned*)(size_t)cnt < 8u) __builtin_amdgcn_s_sleep(1);
  asm volatile("" ::: "memory");
#pragma unroll
  for (int i = 0; i < 4; ++i) {
    const int p = tid + 256 * i;
    unsigned yy[4];
#pragma unroll
    for (int c = 0; c < 4; ++c) yy[c] = *(const unsigned*)(smem + c * CV_CH + CV_HA + p * 4);
    const u32x4 yo = {__builtin_amdgcn_perm(yy[1], yy[0], 0x05040100u), __builtin_amdgcn_perm(yy[3], yy[2], 0x05040100u),
                      __builtin_amdgcn_perm(yy[1], yy[0], 0x07060302u), __builtin_amdgcn_perm(yy[3], yy[2], 0x07060302u)};
    u32x4 xo;
    const u32x4 x0v = *(const u32x4*)(smem + CV_X0 + p * 16);
    const f32x4 stv = *(const f32x4*)(smem + CV_ST + p * 16);
#pragma unroll
    for (int c = 0; c < 4; ++c) {
      const float m = stv[c & 2], rs = stv[(c & 2) + 1];
      const float xa = bflo(x0v[c]), xb = bfhi(x0v[c]);
      xo[c] = pk2(xa + (xa - m) * rs * bflo(yo[c]), xb + (xb - m) * rs * bfhi(yo[c]));
    }
    st16(rw, OFF_X1 + (j * 1024 + p) * 16, xo);
    st16(rw, OFF_Y + (j * 1024 + p) * 16, yo);
  }
  STAMP(1, 6); DRAIN(); STAMP(1, 7);
}

constexpr int G1_AB = G1_ROWS * 128, G1_STAGE = G1_AB + G1_COLS * 128, G1_NS = 3, G1_SCR = G1_STAGE * G1_NS, G1_LDS = G1_SCR + 2816 + 4 * 4224;
constexpr int G1_NA = G1_ROWS / 32, G1_NB = G1_COLS / 32, G1_NP = G1_NA + G1_NB;
constexpr float LOG2E = 1.44269504088896f;
DI f32x2 phi2(f32x2 t2) {
  const f32x2 a = t2 * LOG2E - 1e-6f * LOG2E;
  f32x2 e, m;
  e[0] = __builtin_amdgcn_fmed3f(__builtin_amdgcn_exp2f(a[0]), 0.0f, 1.0f);
  e[1] = __builtin_amdgcn_fmed3f(__builtin_amdgcn_exp2f(a[1]), 0.0f, 1.0f);
  m[0] = fmaxf(t2[0], 1e-6f);
  m[1] = fmaxf(t2[1], 1e-6f);
  return m + e;
}
DI f32x2 lo2(f32x4 v) { return (f32x2){v[0], v[1]}; }
DI f32x2 hi2(f32x4 v) { return (f32x2){v[2], v[3]}; }
template <int C4>
__global__ __launch_bounds__(512, 2) void g1_kernel(char* __restrict__ ws) {
  extern __shared__ __attribute__((aligned(16))) char smem[];
  const rsrc_t rc = code_rsrc(CODE_G1);
  constexpr int kid = C4 ? 2 : 4;
  constexpr unsigned OFF_X = C4 ? OFF_X1 : OFF_X3, KVO = OFF_KV + (C4 ? 0 : KV_LAYER * 4);
  STAMP(kid, 0);
  const int tid = threadIdx.x, lane = tid & 63, w = __builtin_amdgcn_readfirstlane(tid >> 6);
  const int xcd = blockIdx.x & 7, idx = blockIdx.x >> 3;
  const int band = TALL ? (xcd & 1) * 4 + (idx & 3) : (xcd & 1) * 8 + (idx & 7), hg = TALL ? (xcd >> 1) * 8 + (idx >> 2) : (xcd >> 1) * 4 + (idx >> 3);
  const rsrc_t rw = mk_rsrc(ws, WS_USED);
  float* Rs = (float*)(smem + G1_SCR);
  float* MRs = Rs + 256;
  float* CSs = Rs + 512;
  float* P = Rs + 704;
  if (w >= 4) {
    const int lw = w - 4, t = tid - 256, row0 = band * G1_ROWS, tr = t & (G1_ROWS - 1);
    const unsigned l0 = lane * 16, l1 = (lane ^ 8) * 16;
    auto issue = [&](int st) {
      char* base = smem + (st % G1_NS) * G1_STAGE;
#pragma unroll
      for (int p = 0; p < G1_NA; ++p) {
        constexpr int PQ = G1_NA / 4, PO = G1_NA / 2;
        if (C4) dma16(rw, base + (G1_NA * lw + p) * 1024, ((p / PQ) & 2) ? l1 : l0, OFF_X + ((st * 16 + lw * 4 + p / PQ) * 1024 + band * (G1_ROWS / 2) + (p % PQ) * 64) * 16);
        else dma16(rw, base + (G1_NA * lw + p) * 1024, l0, OFF_X + ((st * 8 + lw * 2 + p / PO) * S + band * G1_ROWS + (p % PO) * 64) * 16);
      }
#pragma unroll
      for (int p = 0; p < G1_NB; ++p) dma16(rw, base + G1_AB + (G1_NB * lw + p) * 1024, l0, OFF_W8 + (hg * 128 + st * 8) * G1_COLS * 16 + (G1_NB * lw + p) * 1024);
    };
    issue(0); issue(1);
    wait_vm<G1_NP>();
    BAR();
    issue(2);
    u32x4 xv[4];
    float v[4] = {0.f, 0.f, 0.f, 0.f};
    if (C4) {
      const u32x4* xp = (const u32x4*)(ws + OFF_X) + (size_t)(t >> 2) * 1024 + band * (G1_ROWS / 2) + hg * 4 + (t & 3);
      asm volatile("global_load_dwordx4 %0, %4, off\n\tglobal_load_dwordx4 %1, %5, off\n\tglobal_load_dwordx4 %2, %6, off\n\tglobal_load_dwordx4 %3, %7, off"
                   : "=&v"(xv[0]), "=&v"(xv[1]), "=&v"(xv[2]), "=&v"(xv[3]) : "v"(xp), "v"(xp + 65536), "v"(xp + 131072), "v"(xp + 196608) : "memory");
    }
#pragma unroll
    for (int i = 0; i < 13; ++i) {
      if (C4 && i < 2) wait_vm<G1_NP + 4>();
      else if (C4 && i == 2) asm volatile("s_waitcnt vmcnt(%4)" : "+v"(xv[0]), "+v"(xv[1]), "+v"(xv[2]), "+v"(xv[3]) : "i"(G1_NP) : "memory");
      else wait_vm<G1_NP>();
      BAR();
      issue(i + G1_NS);
      if (C4 && i >= 2 && i < 6) {
        const u32x4 x = xv[i - 2];
#pragma unroll
        for (int c = 0; c < 4; ++c) {
          const float lo = bflo(x[c]), hi = bfhi(x[c]);
          v[c & 2] = opq(v[c & 2] + lo); v[(c & 2) + 1] = opq(fmaf(lo, lo, v[(c & 2) + 1]));
          v[c & 2] = opq(v[c & 2] + hi); v[(c & 2) + 1] = opq(fmaf(hi, hi, v[(c & 2) + 1]));
        }
        asm volatile("" : "+v"(v[0]), "+v"(v[1]), "+v"(v[2]), "+v"(v[3]));
      }
      if (C4 && i == 6) {
#pragma unroll
        for (int c = 0; c < 4; ++c) { v[c] += row_ror<4>(v[c]); v[c] += row_ror<8>(v[c]); v[c] = sum_x32(sum_x16(v[c])); }
        const bool up = (lane & 4) != 0;
        if (lane < 8) publish8(ws + OFF_GX + ((row0 >> 8) * 1024 + lw * 256 + (row0 & 255) + hg * 8 + 2 * (lane & 3) + (lane >> 2)) * 8, up ? v[2] : v[0], up ? v[3] : v[1]);
      }
    }
    const char* gp = ws + (C4 ? OFF_GX + ((row0 >> 8) * 1024 + (row0 & 255) + tr) * 8 + 4096 : OFF_ST3 + (((row0 + tr) >> 6) * 512 + lane) * 8);
    const char* cp = ws + OFF_CSP + (hg * G1_COLS + (t < G1_COLS ? t : 0)) * 64;
    u32x2 g[8];
    f32x4 c0, c1, c2, c3;
    asm volatile("global_load_dwordx4 %0, %4, off\n\tglobal_load_dwordx4 %1, %4, off offset:16\n\t"
                 "global_load_dwordx4 %2, %4, off offset:32\n\tglobal_load_dwordx4 %3, %4, off offset:48"
                 : "=&v"(c0), "=&v"(c1), "=&v"(c2), "=&v"(c3) : "v"(cp) : "memory");
    if (C4) {
      asm volatile("global_load_dwordx2 %0, %4, off offset:-4096 sc1\n\tglobal_load_dwordx2 %1, %4, off offset:-2048 sc1\n\t"
                   "global_load_dwordx2 %2, %4, off sc1\n\tglobal_load_dwordx2 %3, %4, off offset:2048 sc1"
                   : "=&v"(g[0]), "=&v"(g[1]), "=&v"(g[2]), "=&v"(g[3]) : "v"(gp) : "memory");
      wait_vm<G1_NP + 8>();
    } else {
      asm volatile("global_load_dwordx2 %0, %8, off\n\tglobal_load_dwordx2 %1, %8, off offset:512\n\tglobal_load_dwordx2 %2, %8, off offset:1024\n\t"
                   "global_load_dwordx2 %3, %8, off offset:1536\n\tglobal_load_dwordx2 %4, %8, off offset:2048\n\tglobal_load_dwordx2 %5, %8, off offset:2560\n\t"
                   "global_load_dwordx2 %6, %8, off offset:3072\n\tglobal_load_dwordx2 %7, %8, off offset:3584"
                   : "=&v"(g[0]), "=&v"(g[1]), "=&v"(g[2]), "=&v"(g[3]), "=&v"(g[4]), "=&v"(g[5]), "=&v"(g[6]), "=&v"(g[7]) : "v"(gp) : "memory");
      wait_vm<G1_NP + 12>();
    }
    BAR();
    if (C4) asm volatile("s_waitcnt vmcnt(0)" : "+v"(g[0]), "+v"(g[1]), "+v"(g[2]), "+v"(g[3]), "+v"(c0), "+v"(c1), "+v"(c2), "+v"(c3)::"memory");
    else asm volatile("s_waitcnt vmcnt(0)" : "+v"(g[0]), "+v"(g[1]), "+v"(g[2]), "+v"(g[3]), "+v"(g[4]), "+v"(g[5]), "+v"(g[6]), "+v"(g[7]),
                      "+v"(c0), "+v"(c1), "+v"(c2), "+v"(c3)::"memory");
    BAR();
    if (C4) {
#pragma unroll 1
      for (int spin = 0; spin < (1 << 20); ++spin) {
        if (!__any(g[0][1] == ~0u || g[1][1] == ~0u || g[2][1] == ~0u || g[3][1] == ~0u)) break;
        __builtin_amdgcn_s_sleep(4);
#pragma unroll
        for (int k = 0; k < 4; ++k) g[k] = poll8(gp + (k - 2) * 2048);
      }
    }
    float sm = 0.f, sq = 0.f, cs = 0.f;
#pragma unroll
    for (int k = 0; k < (C4 ? 4 : 8); ++k) { sm = opq(sm + __uint_as_float(g[k][0])); sq = opq(sq + __uint_as_float(g[k][1])); }
#pragma unroll
    for (int k = 0; k < 4; ++k) cs = opq(opq(opq(opq(cs + c0[k]) + c1[k]) + c2[k]) + c3[k]);
    const f32x2 st = ln_stats(sm, sq);
    Rs[t] = st[1]; MRs[t] = -st[0] * st[1];
    if (t < G1_COLS) CSs[t] = cs;
    WAIT_LGKM0();
    BAR();
    BAR();
    STAMPW(kid, 4, 4);
    float* kvacc = (float*)(ws + KVO);
    if (TALL) {
#pragma unroll
      for (int i = 0; i < 4; ++i) {
        const int e = t + 256 * i;
        atomicAdd(kvacc + hg * 1024 + e, (P[e] + P[1056 + e]) + (P[2112 + e] + P[3168 + e]));
      }
      if (t < 32) atomicAdd(kvacc + 32768 + hg * 32 + t, (P[1024 + t] + P[2080 + t]) + (P[3136 + t] + P[4192 + t]));
    } else {
#pragma unroll
      for (int i = 0; i < 8; ++i) {
        const int e = t + 256 * (i & 3), hd = i >> 2;
        atomicAdd(kvacc + (hg * 2 + hd) * 1024 + e, P[hd * 1056 + e] + P[(2 + hd) * 1056 + e]);
      }
      if (t < 64) { const int hd = t >> 5, d = t & 31; atomicAdd(kvacc + 32768 + hg * 64 + t, P[hd * 1056 + 1024 + d] + P[(2 + hd) * 1056 + 1024 + d]); }
    }
    DRAIN(); STAMPW(kid, 5, 4);
    return;
  }
  const int r = lane & 15, kq = lane >> 4, rh = TALL ? w : w >> 1, hh = TALL ? 0 : w & 1, h = hg * G1_HP + hh;
  constexpr int AO = G1_ROWS * 16, BO = G1_COLS * 16, PS = 32 * G1_HP;
  const char* ab = C4 ? smem + kq * AO + ((64 * rh + r) ^ (16 * (kq & 1))) * 8 : smem + kq * AO + (64 * rh + r) * 16;
  const char* ab2 = ab + 128 - (kq & 1) * 256;
  const char* bb = smem + G1_AB + kq * BO + (32 * hh + r) * 16;
  unsigned pf0 = 0u, pf1 = 0u;
  if (CODEPF && w == 0) { pf0 = __builtin_amdgcn_raw_buffer_load_b32(rc, lane * 128, 0, 0); pf1 = __builtin_amdgcn_raw_buffer_load_b32(rc, lane * 128, 8192, 0); }
  SB();
  f32x4 acc[4][6];
#pragma unroll
  for (int m = 0; m < 4; ++m)
#pragma unroll
    for (int p = 0; p < 6; ++p) { acc[m][p] = (f32x4){0.f, 0.f, 0.f, 0.f}; asm volatile("" : "+v"(acc[m][p])); }
  bf16x8 F[2][10];
  auto rd1 = [&](int buf, const char* a, const char* a2, const char* b, int t, int i) {
    if (i < 4) {
      if (C4) {
        const char* p = ((i & 1) ? a2 : a) + t * 4 * AO + (i >> 1) * 256;
        const u32x2 lo = *(const u32x2*)p, hi = *(const u32x2*)(p + AO / 2);
        F[buf][i] = as_frag((u32x4){lo[0], lo[1], hi[0], hi[1]});
      } else F[buf][i] = *(const bf16x8*)(a + t * 4 * AO + i * 256);
    } else F[buf][i] = *(const bf16x8*)(b + t * 4 * BO + ((i - 4) >> 1) * PS * 16 + ((i - 4) & 1) * 256);
  };
  auto step = [&](int cb, const char* a, const char* a2, const char* b, int t, bool rd) {
    const int ord[10] = {0, 4, 5, 6, 7, 8, 9, 1, 2, 3};
#pragma unroll
    for (int i = 0; i < 24; ++i) {
      const int m = i / 6, p = i % 6;
      if (p < 2) acc[m][p] = MFMA16(F[cb][4 + p], F[cb][m], acc[m][p]);
      else acc[m][p] = MFMA16(F[cb][m], F[cb][4 + p], acc[m][p]);
      SB();
      if (rd && (i & 1) && (i >> 1) < 10) { rd1(cb ^ 1, a, a2, b, t, ord[i >> 1]); SB(); }
    }
  };
  BAR();
  STAMP(kid, 1); CSTAMP(kid + 8, 1); STAMP(kid + 8, 0);
#pragma unroll
  for (int i = 0; i < 10; ++i) rd1(0, ab, ab2, bb, 0, i);
  int so = 0;
  for (int it = 0; it < 15; ++it) {
    step(0, ab + so, ab2 + so, bb + so, 1, true);
    WAIT_LGKM0();
    BAR();
    so = (so == (G1_NS - 1) * G1_STAGE) ? 0 : so + G1_STAGE;
    step(1, ab + so, ab2 + so, bb + so, 0, true);
  }
  step(0, ab + so, ab2 + so, bb + so, 1, true);
  KEEP(pf0); KEEP(pf1);
  BAR();
  step(1, ab, ab2, bb, 0, false);
  STAMP(kid, 2); CSTAMP(kid + 8, 2); STAMP(kid + 8, 3);
  f32x4 Z[2][2], Zz[2];
#pragma unroll
  for (int i = 0; i < 2; ++i) { Z[i][0] = (f32x4){0.f, 0.f, 0.f, 0.f}; Z[i][1] = Z[i][0]; Zz[i] = Z[i][0]; }
  {
    float ck[4];
#pragma unroll
    for (int p = 0; p < 4; ++p) ck[p] = CSs[PS + (p >> 1) * PS + 32 * hh + (p & 1) * 16 + r];
    const bf16x8 ones = as_frag((u32x4){0x3f803f80u, 0x3f803f80u, 0x3f803f80u, 0x3f803f80u});
#pragma unroll
    for (int jj = 0; jj < 2; ++jj) {
      u32x4 fr[4];
#pragma unroll
      for (int mm = 0; mm < 2; ++mm) {
        const int m = 2 * jj + mm;
        const f32x4 rs = *(const f32x4*)(Rs + 64 * rh + 16 * m + 4 * kq), mr = *(const f32x4*)(MRs + 64 * rh + 16 * m + 4 * kq);
#pragma unroll
        for (int p = 0; p < 2; ++p) {
          fr[p][2 * mm] = pk2v(phi2(lo2(acc[m][2 + p]) * lo2(rs) + (lo2(mr) * ck[p] + 1e-6f)));
          fr[p][2 * mm + 1] = pk2v(phi2(hi2(acc[m][2 + p]) * hi2(rs) + (hi2(mr) * ck[p] + 1e-6f)));
          fr[2 + p][2 * mm] = pk2v(lo2(acc[m][4 + p]) * lo2(rs) + lo2(mr) * ck[2 + p]);
          fr[2 + p][2 * mm + 1] = pk2v(hi2(acc[m][4 + p]) * hi2(rs) + hi2(mr) * ck[2 + p]);
        }
      }
#pragma unroll
      for (int dh = 0; dh < 2; ++dh) {
        Z[0][dh] = MFMA16(as_frag(fr[dh]), as_frag(fr[2]), Z[0][dh]);
        Z[1][dh] = MFMA16(as_frag(fr[dh]), as_frag(fr[3]), Z[1][dh]);
        Zz[dh] = MFMA16(as_frag(fr[dh]), ones, Zz[dh]);
      }
    }
  }
  {
    float* mine = P + w * 1056;
#pragma unroll
    for (int i = 0; i < 4; ++i) *(f32x4*)(mine + i * 256 + lane * 4) = Z[i >> 1][i & 1];
    if (r == 0) { *(f32x4*)(mine + 1024 + 4 * kq) = Zz[0]; *(f32x4*)(mine + 1040 + 4 * kq) = Zz[1]; }
  }
  WAIT_LGKM0();
  BAR();
  STAMP(kid, 3);
  {
    const f32x4 c0 = *(const f32x4*)(CSs + 32 * hh + 4 * kq), c1 = *(const f32x4*)(CSs + 32 * hh + 16 + 4 * kq);
#pragma unroll
    for (int m = 0; m < 4; ++m) {
      const float rs = Rs[64 * rh + 16 * m + r], mr = MRs[64 * rh + 16 * m + r];
      const u32x4 o = {pk2v(phi2(lo2(acc[m][0]) * rs + (lo2(c0) * mr + 1e-6f))), pk2v(phi2(hi2(acc[m][0]) * rs + (hi2(c0) * mr + 1e-6f))),
                       pk2v(phi2(lo2(acc[m][1]) * rs + (lo2(c1) * mr + 1e-6f))), pk2v(phi2(hi2(acc[m][1]) * rs + (hi2(c1) * mr + 1e-6f)))};
      st16(rw, OFF_PQ + ((((h >> 2) * 128 + band * (G1_ROWS / 16) + rh * 4 + m) * 4 + (h & 3)) * 64 + lane) * 16, o);
    }
  }
  STAMP(kid, 6); DRAIN(); STAMP(kid, 7);
}

constexpr int G2_LDS = 131072 + 2048 + 4096;
template <int FINAL>
__global__ __launch_bounds__(512, 2) void g2_kernel(char* __restrict__ ws, char* __restrict__ out) {
  extern __shared__ __attribute__((aligned(16))) char smem[];
  const rsrc_t rc = code_rsrc(FINAL ? CODE_G2F : CODE_G2);
  constexpr int kid = FINAL ? 5 : 3;
  constexpr unsigned KVO = OFF_KV + (FINAL ? KV_LAYER * 4 : 0);
  STAMP(kid, 0);
  const int tid = threadIdx.x, lane = tid & 63, w = __builtin_amdgcn_readfirstlane(tid >> 6);
  const int xcd = blockIdx.x & 7, idx = blockIdx.x >> 3;
  const int sb = (xcd >> 1) * 8 + (idx & 7), nb = (xcd & 1) * 4 + (idx >> 3);
  const rsrc_t rw = mk_rsrc(ws, WS_USED);
  const unsigned l16 = lane * 16;
  u32x4* O = (u32x4*)smem;
  if (w < 4) {
    const int kq = lane >> 4;
    constexpr int PL = 2, PD = PL + 1;
    u32x4 pq[PD][4];
    f32x4 kv[PD][4];
    const unsigned spq = OFF_PQ + (sb * 16 + w) * 1024, skv = KVO + w * 4096;
    auto ld = [&](int i, int n) {
      if (n < 4) kv[i % PD][n] = __builtin_bit_cast(f32x4, ld16(rw, l16 + n * 1024, skv + i * 16384));
      else pq[i % PD][n - 4] = ld16(rw, l16, spq + (i * 512 + (n - 4) * 4) * 1024);
      SB();
    };
    const f32x4 zall = __builtin_bit_cast(f32x4, ld16(rw, (lane >> 3) * 512 + (lane & 7) * 16, KVO + 131072 + w * 128));
    SB();
#pragma unroll
    for (int n = 0; n < 8 * PL; ++n) ld(n >> 3, n & 7);
    u32x2* Zs = (u32x2*)(smem + 131072) + w * 64;
    Zs[lane] = (u32x2){pk2v(lo2(zall)), pk2v(hi2(zall))};
    const f32x4 zero = {0.f, 0.f, 0.f, 0.f}, eps = {1e-6f, 1e-6f, 1e-6f, 1e-6f};
    bf16x8 k0, k1, zb;
    auto prep = [&](int i) {
      const int b = i % PD;
      const u32x2 za = Zs[i * 8 + kq], zc = Zs[i * 8 + 4 + kq];
      k0 = as_frag((u32x4){pk2v(lo2(kv[b][0])), pk2v(hi2(kv[b][0])), pk2v(lo2(kv[b][1])), pk2v(hi2(kv[b][1]))});
      k1 = as_frag((u32x4){pk2v(lo2(kv[b][2])), pk2v(hi2(kv[b][2])), pk2v(lo2(kv[b][3])), pk2v(hi2(kv[b][3]))});
      zb = as_frag((u32x4){za[0], za[1], zc[0], zc[1]});
      asm volatile("" : "+v"(k0), "+v"(k1), "+v"(zb));
    };
    prep(0);
#pragma unroll
    for (int i = 0; i < 8; ++i) {
      const int b = i % PD;
      f32x4 x0[4], x1[4], dn[4];
      __builtin_amdgcn_s_setprio(2);
#pragma unroll
      for (int st = 0; st < 4; ++st) {
        dn[st] = MFMA16(zb, as_frag(pq[b][st]), eps);
        x0[st] = MFMA16(k0, as_frag(pq[b][st]), zero);
        x1[st] = MFMA16(k1, as_frag(pq[b][st]), zero);
      }
      __builtin_amdgcn_s_setprio(0);
      SB();
#pragma unroll
      for (int st = 0; st < 4; ++st) {
        const float inv = __builtin_amdgcn_rcpf(dn[st][0]);
        O[((i * 4 + w) * 4 + st) * 64 + lane] =
            (u32x4){pk2(opq(x0[st][0] * inv), opq(x0[st][1] * inv)), pk2(opq(x0[st][2] * inv), opq(x0[st][3] * inv)),
                    pk2(opq(x1[st][0] * inv), opq(x1[st][1] * inv)), pk2(opq(x1[st][2] * inv), opq(x1[st][3] * inv))};
        SB();
        if (i + PL < 8) { ld(i + PL, 2 * st); ld(i + PL, 2 * st + 1); }
      }
      if (i < 7) prep(i + 1);
      WAIT_LGKM0();
      BAR();
      if (i == 0) STAMP(kid, 1);
    }
    return;
  }
  const int c = w - 4, n0 = nb * 128 + c * 32, s0 = sb * 64, r = lane & 31, hf = lane >> 5;
  constexpr int CL = 2, CD = CL + 1, LA = 6;
  u32x4 wo[CD][8], of[8];
  const unsigned swo = OFF_WOF + (nb * 4 + c) * 8192;
  auto ldw = [&](int i, int n) { wo[i % CD][n] = ld16(rw, l16 + (n & 3) * 1024, swo + i * 262144 + (n >> 2) * 4096); SB(); };
  unsigned pf0 = 0u, pf1 = 0u;
  if (CODEPF && c == 0) { pf0 = __builtin_amdgcn_raw_buffer_load_b32(rc, lane * 128, 0, 0); pf1 = __builtin_amdgcn_raw_buffer_load_b32(rc, lane * 128, 8192, 0); }
  SB();
#pragma unroll
  for (int n = 0; n < 8 * CL; ++n) ldw(n >> 3, n & 7);
  f32x16 acc[2];
#pragma unroll
  for (int i = 0; i < 16; ++i) { acc[0][i] = 0.f; acc[1][i] = 0.f; }
  asm volatile("" : "+v"(acc[0]), "+v"(acc[1]));
  u32x4 res[2][2], yv[2][2];
  auto ldres = [&](int k) {
    const int s2 = (k >> 1) & 1, g = k & 1;
    if (FINAL) { if (k < 4) res[s2][g] = ld16(rw, (2 * hf * S + r) * 16 + s2 * 512, OFF_X3 + (((n0 >> 3) + g) * S + s0) * 16); }
    else {
      const unsigned so = (k < 4 ? OFF_X1 : OFF_Y) + (((n0 >> 2) + 2 * g) * S + s0) * 8, vo = (4 * hf * S + r) * 8 + s2 * 256;
      const u32x2 lo = ld8(rw, vo, so), hi = ld8(rw, vo, so + S * 8);
      if (k < 4) res[s2][g] = (u32x4){lo[0], lo[1], hi[0], hi[1]}; else yv[s2][g] = (u32x4){lo[0], lo[1], hi[0], hi[1]};
    }
    SB();
  };
  const u32x4* Ob = O + (r >> 4) * 64 + hf * 16 + (r & 15);
  auto rdo = [&](int f) { const int u = f & 15; of[f & 7] = Ob[((f >> 4) * 16 + (u >> 2) * 4 + (u & 1) * 2) * 64 + ((u >> 1) & 1) * 32]; SB(); };
  __builtin_amdgcn_s_setprio(1);
  BAR();
#pragma unroll
  for (int f = 0; f < LA; ++f) rdo(f);
#pragma unroll
  for (int i = 0; i < 8; ++i)
#pragma unroll
    for (int n = 0; n < 16; ++n) {
      const int f = i * 16 + n;
      acc[n & 1] = MFMA32(as_frag(wo[i % CD][n >> 1]), as_frag(of[f & 7]), acc[n & 1]);
      SB();
      if (f + LA < 128) { if (((f + LA) & 15) == 0) BAR(); rdo(f + LA); }
      if ((n & 1) && i + CL < 8) ldw(i + CL, n >> 1);
      if (i == 6 && (n & 1)) ldres(n >> 1);
    }
  KEEP(pf0); KEEP(pf1);
  STAMPW(kid, 2, 4);
  u32x4 x[2][2];
#pragma unroll
  for (int s2 = 0; s2 < 2; ++s2)
#pragma unroll
    for (int g = 0; g < 2; ++g)
#pragma unroll
      for (int e = 0; e < 4; ++e) x[s2][g][e] = pk2(bflo(res[s2][g][e]) + acc[s2][8 * g + 2 * e], bfhi(res[s2][g][e]) + acc[s2][8 * g + 2 * e + 1]);
  if (FINAL) {
#pragma unroll
    for (int s2 = 0; s2 < 2; ++s2)
#pragma unroll
      for (int g = 0; g < 2; ++g) *(u32x4*)(out + ((size_t)(s0 + 32 * s2 + r) * D + n0 + 16 * hf + 8 * g) * 2) = x[s2][g];
  } else {
    f32x2* Red = (f32x2*)(smem + 131072 + 2048);
    auto rowpart = [&]() {
      float v[4] = {0.f, 0.f, 0.f, 0.f};
#pragma unroll
      for (int e = 0; e < 8; ++e)
#pragma unroll
        for (int s2 = 0; s2 < 2; ++s2) { const unsigned u = x[s2][e >> 2][e & 3]; v[2 * s2] = dot2(u, 0x3f803f80u, v[2 * s2]); v[2 * s2 + 1] = dot2(u, u, v[2 * s2 + 1]); }
      const auto a = __builtin_amdgcn_permlane32_swap(__float_as_uint(v[0]), __float_as_uint(v[2]), false, false);
      const auto b = __builtin_amdgcn_permlane32_swap(__float_as_uint(v[1]), __float_as_uint(v[3]), false, false);
      return (f32x2){__uint_as_float(a[0]) + __uint_as_float(a[1]), __uint_as_float(b[0]) + __uint_as_float(b[1])};
    };
    Red[c * 64 + lane] = rowpart();
    WAIT_LGKM0();
    BAR();
    char* gx = ws + OFF_GX2 + (sb * 512 + lane) * 8;
    if (c == 0) {
      const f32x2 t = (Red[lane] + Red[64 + lane]) + (Red[128 + lane] + Red[192 + lane]);
      publish8(gx + nb * 512, t[0], t[1]);
    }
    STAMPW(kid, 4, 4);
    float sm, sq;
#pragma unroll 1
    for (int spin = 0; spin < (1 << 20); ++spin) {
      u32x2 g[8];
#pragma unroll
      for (int k = 0; k < 8; ++k) g[k] = poll8(gx + k * 512);
      bool empty = false;
      sm = 0.f; sq = 0.f;
#pragma unroll
      for (int k = 0; k < 8; ++k) { empty |= g[k][1] == ~0u; sm += __uint_as_float(g[k][0]); sq += __uint_as_float(g[k][1]); }
      if (!__any(empty)) break;
      __builtin_amdgcn_s_sleep(1);
    }
    STAMPW(kid, 3, 4);
    const f32x2 st = ln_stats(sm, sq);
    const auto mm = __builtin_amdgcn_permlane32_swap(__float_as_uint(st[0]), __float_as_uint(st[0]), false, false);
    const auto rr = __builtin_amdgcn_permlane32_swap(__float_as_uint(st[1]), __float_as_uint(st[1]), false, false);
#pragma unroll
    for (int s2 = 0; s2 < 2; ++s2) {
      const float mu = __uint_as_float(mm[s2]), rs = __uint_as_float(rr[s2]);
#pragma unroll
      for (int g = 0; g < 2; ++g) {
#pragma unroll
        for (int e = 0; e < 4; ++e) {
          const float a = bflo(x[s2][g][e]), b = bfhi(x[s2][g][e]);
          x[s2][g][e] = pk2(a + (a - mu) * rs * bflo(yv[s2][g][e]), b + (b - mu) * rs * bfhi(yv[s2][g][e]));
        }
        st16(rw, OFF_X3 + (((n0 >> 3) + 2 * hf + g) * S + s0 + 32 * s2 + r) * 16, x[s2][g]);
      }
    }
    Red[256 + c * 64 + lane] = rowpart();
    WAIT_LGKM0();
    BAR();
    if (c == 0) {
      const f32x2 t = (Red[256 + lane] + Red[320 + lane]) + (Red[384 + lane] + Red[448 + lane]);
      __builtin_amdgcn_raw_buffer_store_b64(__builtin_bit_cast(u32x2, t), rw, OFF_ST3 + ((sb * 8 + nb) * 64 + lane) * 8, 0, 16);
    }
  }
  STAMPW(kid, 6, 4); DRAIN(); STAMPW(kid, 7, 4);
}

extern "C" void kernel_launch(void* const* d_in, const int* in_sizes, int n_in, void* d_out, int out_size, void* d_ws,
                              size_t ws_size, hipStream_t stream) {
  const uint16_t* q = (const uint16_t*)d_in[0];
  const uint16_t* k = (const uint16_t*)d_in[1];
  const uint16_t* v = (const uint16_t*)d_in[2];
  const uint16_t* shift_k = (const uint16_t*)d_in[3];
  const uint16_t* ssm_k = (const uint16_t*)d_in[4];
  const uint16_t* ssm_D = (const uint16_t*)d_in[5];
  const uint16_t* wqkv = (const uint16_t*)d_in[7];
  const uint16_t* wo = (const uint16_t*)d_in[8];
#define WSPOS 0
  char* ws = (char*)d_ws + (WSPOS == 1 ? ((ws_size - WS_USED) & ~(size_t)(2 * MB - 1)) : WSPOS == 2 ? ((ws_size / 2) & ~(size_t)(2 * MB - 1)) : 0);
  (void)hipFuncSetAttribute((const void*)conv_kernel, hipFuncAttributeMaxDynamicSharedMemorySize, CV_LDS);
  (void)hipFuncSetAttribute((const void*)g1_kernel<0>, hipFuncAttributeMaxDynamicSharedMemorySize, G1_LDS);
  (void)hipFuncSetAttribute((const void*)g1_kernel<1>, hipFuncAttributeMaxDynamicSharedMemorySize, G1_LDS);
  (void)hipFuncSetAttribute((const void*)g2_kernel<0>, hipFuncAttributeMaxDynamicSharedMemorySize, G2_LDS);
  (void)hipFuncSetAttribute((const void*)g2_kernel<1>, hipFuncAttributeMaxDynamicSharedMemorySize, G2_LDS);
  prep_kernel<<<256, 512, 0, stream>>>(q, k, v, shift_k, ws);
  conv_kernel<<<256, 512, CV_LDS, stream>>>(ssm_k, ssm_D, wqkv, wo, ws);
  g1_kernel<1><<<256, 512, G1_LDS, stream>>>(ws);
  g2_kernel<0><<<256, 512, G2_LDS, stream>>>(ws, nullptr);
  g1_kernel<0><<<256, 512, G1_LDS, stream>>>(ws);
  g2_kernel<1><<<256, 512, G2_LDS, stream>>>(ws, (char*)d_out);
}
```

```cpp
#include <hip/hip_runtime.h>
#include <stdint.h>

typedef __attribute__((ext_vector_type(8))) short bf16x8;
typedef __attribute__((ext_vector_type(4))) short s16x4;
typedef __attribute__((ext_vector_type(4))) float f32x4;
typedef __attribute__((ext_vector_type(2))) float f32x2;
typedef __attribute__((ext_vector_type(16))) float f32x16;
typedef __attribute__((ext_vector_type(4))) unsigned u32x4;
typedef __attribute__((ext_vector_type(2))) unsigned u32x2;
typedef __attribute__((ext_vector_type(2))) __bf16 bf16x2_t;
typedef __amdgpu_buffer_rsrc_t rsrc_t;
#define DI __device__ __forceinline__
#define LAS __attribute__((address_space(3)))
#define STAMPW(kid, slot, wv) do {} while (0)
#define DRAIN() do {} while (0)
#define STAMP(kid, slot) STAMPW(kid, slot, 0)

constexpr int S = 2048, D = 1024;
constexpr unsigned MB = 1024 * 1024;
constexpr unsigned OFF_UB = 0,
                   OFF_X0 = 4 * MB,
                   OFF_W8 = 8 * MB,
                   OFF_WOF = 14 * MB,
                   OFF_Y = 16 * MB,
                   OFF_X1 = 20 * MB,
                   OFF_PQ = 24 * MB,
                   OFF_X3 = 32 * MB,
                   OFF_SM = 36 * MB, WS_USED = 37 * MB;
constexpr int KV_LAYER = 32 * 1024 + 32 * 32;
constexpr unsigned OFF_ST0 = OFF_SM,
                   OFF_CSP = OFF_ST0 + 16384,
                   OFF_KV = OFF_CSP + 16 * 3072 * 4,
                   OFF_GX = OFF_KV + 2 * KV_LAYER * 4,
                   OFF_GX2 = OFF_GX + 65536,
                   OFF_ST3 = OFF_GX2 + 131072,
                   OFF_FLG = OFF_ST3 + 131072,
                   OFF_EPO = OFF_FLG + 2304 * 32;
constexpr int FL_U = 0, FL_Q = 1024, FL_X1 = 1280, FL_G1 = 1536  , FL_X3 = 2048;
constexpr unsigned CODE_BYTES = 56 * 1024;
constexpr int SPIN_MAX = 1 << 17;

DI float bflo(unsigned u) { return __uint_as_float(u << 16); }
DI float bfhi(unsigned u) { return __uint_as_float(u & 0xffff0000u); }
DI unsigned pk2v(f32x2 v) { return __builtin_bit_cast(unsigned, __builtin_convertvector(v, bf16x2_t)); }
DI unsigned pk2(float a, float b) { return pk2v((f32x2){a, b}); }
DI float rbf(float a) { return bflo(pk2(a, a)); }
DI bf16x8 as_frag(u32x4 v) { return __builtin_bit_cast(bf16x8, v); }
DI f32x2 lo2(f32x4 v) { return (f32x2){v[0], v[1]}; }
DI f32x2 hi2(f32x4 v) { return (f32x2){v[2], v[3]}; }
DI float dot2(unsigned a, unsigned b, float c) {
  return __builtin_amdgcn_fdot2_f32_bf16(__builtin_bit_cast(bf16x2_t, a), __builtin_bit_cast(bf16x2_t, b), c, false);
}
#define MFMA16(a, b, c) __builtin_amdgcn_mfma_f32_16x16x32_bf16((a), (b), (c), 0, 0, 0)
#define MFMA32(a, b, c) __builtin_amdgcn_mfma_f32_32x32x16_bf16((a), (b), (c), 0, 0, 0)
#define WAIT_LGKM0() asm volatile("s_waitcnt lgkmcnt(0)" ::: "memory")
#define BAR() do { __builtin_amdgcn_s_barrier(); asm volatile("" ::: "memory"); } while (0)
#define SB() __builtin_amdgcn_sched_barrier(0)
#define KEEP(x) asm volatile("" ::"v"(x))
template <int N> DI void wait_vm() { asm volatile("s_waitcnt vmcnt(%0)" ::"i"(N) : "memory"); }
DI rsrc_t mk_rsrc(void* p, unsigned bytes) { return __builtin_amdgcn_make_buffer_rsrc(p, 0, bytes, 0x00020000); }
DI void glds16(const void* g, char* l) {
  __builtin_amdgcn_global_load_lds((const __attribute__((address_space(1))) void*)g, (LAS void*)l, 16, 0, 0);
}
DI void dma16(rsrc_t r, char* l, unsigned voff, unsigned soff) { __builtin_amdgcn_raw_ptr_buffer_load_lds(r, (LAS void*)l, 16, voff, soff, 0, 16); }
DI u32x4 ld16(rsrc_t r, unsigned voff, unsigned soff) { return __builtin_amdgcn_raw_buffer_load_b128(r, voff, soff, 16); }
DI u32x2 ld8(rsrc_t r, unsigned voff, unsigned soff) { return __builtin_amdgcn_raw_buffer_load_b64(r, voff, soff, 16); }
DI void st16(rsrc_t r, unsigned off, u32x4 v) { __builtin_amdgcn_raw_buffer_store_b128(v, r, off, 0, 16); }
DI void st8(rsrc_t r, unsigned off, u32x2 v) { __builtin_amdgcn_raw_buffer_store_b64(v, r, off, 0, 16); }
DI void put8(char* p, u32x2 v) { __hip_atomic_store((unsigned long long*)p, __builtin_bit_cast(unsigned long long, v), __ATOMIC_RELAXED, __HIP_MEMORY_SCOPE_AGENT); }
DI u32x2 poll8(const char* p) { return __builtin_bit_cast(u32x2, __hip_atomic_load((const unsigned long long*)p, __ATOMIC_RELAXED, __HIP_MEMORY_SCOPE_AGENT)); }
DI void publish8(char* p, float sm, float sq) {
  unsigned qb = __float_as_uint(sq);
  if (qb == ~0u) qb = 0x7fc00000u;
  put8(p, (u32x2){__float_as_uint(sm), qb});
}
DI float opq(float m) { asm("" : "+v"(m)); return m; }
template <int N> DI float row_ror(float x) {
  return __int_as_float(__builtin_amdgcn_update_dpp(0, __float_as_int(x), 0x120 + N, 0xf, 0xf, false));
}
DI float swap16_sum(float a, float b) { const auto p = __builtin_amdgcn_permlane16_swap(__float_as_uint(a), __float_as_uint(b), false, false); return __uint_as_float(p[0]) + __uint_as_float(p[1]); }
DI float swap32_sum(float a, float b) { const auto p = __builtin_amdgcn_permlane32_swap(__float_as_uint(a), __float_as_uint(b), false, false); return __uint_as_float(p[0]) + __uint_as_float(p[1]); }

DI f32x2 ln_stats(float s, float q) {
  const float m = s * (1.0f / 1024.0f);
  const float v = fmaxf(q * (1.0f / 1024.0f) - m * m, 0.0f);
  const float sd = rbf(sqrtf(rbf(rbf(v) + 1e-5f)));
  return (f32x2){rbf(m), 1.0f / sd};
}
constexpr float LOG2E = 1.44269504088896f;
DI f32x2 phi2(f32x2 t2) {
  const f32x2 a = t2 * LOG2E - 1e-6f * LOG2E;
  f32x2 e, m;
  e[0] = __builtin_amdgcn_fmed3f(__builtin_amdgcn_exp2f(a[0]), 0.0f, 1.0f);
  e[1] = __builtin_amdgcn_fmed3f(__builtin_amdgcn_exp2f(a[1]), 0.0f, 1.0f);
  m[0] = fmaxf(t2[0], 1e-6f);
  m[1] = fmaxf(t2[1], 1e-6f);
  return m + e;
}

constexpr int LDS_BYTES = 163840, LDS_CNT = LDS_BYTES - 64;
constexpr int CV_PAD = 512, CV_HA = 286 * 16, CV_HB = CV_HA + 4160, CV_CH = CV_HB + 4128 + 32;
constexpr int CV_WB = CV_PAD + 4 * CV_CH, CV_X0 = CV_WB + 4 * 8192, CV_ST = CV_X0 + 16384;
constexpr int G1_AB = 32768, G1_STAGE = G1_AB + 12288, G1_SCR = 3 * G1_STAGE, G1_NP = 11;
constexpr int G2_Z = 131072, G2_RED = G2_Z + 2048;

struct Cx {
  char* ws; char* smem; rsrc_t rw, rc; int lane, w, tid, bid; u32x2 tag;
  DI void half_sync(int n) const {
    const unsigned c = (unsigned)(size_t)(LAS char*)(smem + LDS_CNT + (w >> 2) * 16);
    asm volatile("s_waitcnt lgkmcnt(0)" ::: "memory");
    if (lane == 0) asm volatile("ds_add_u32 %0, %1" ::"v"(c), "v"(1u) : "memory");
    unsigned x;
    do { asm volatile("ds_read_b32 %0, %1\n\ts_waitcnt lgkmcnt(0)" : "=v"(x) : "v"(c) : "memory"); } while (__builtin_amdgcn_readfirstlane(x) < 4u * n);
  }
  DI void signal(int idx) const { if (lane == 0) put8(ws + OFF_FLG + idx * 32, tag); }
  DI void await(int idx, bool pend) const {
    const char* fp = ws + OFF_FLG + idx * 32;
#pragma unroll 1
    for (int spin = 0; spin < SPIN_MAX; ++spin) {
      if (pend) { const u32x2 f = poll8(fp); pend = f[0] != tag[0] || f[1] != tag[1]; }
      if (!__any(pend)) break;
      __builtin_amdgcn_s_sleep(1);
    }
    asm volatile("" ::: "memory");
  }
  DI unsigned touch_code() const {
    unsigned x;
    asm volatile("buffer_load_dword %0, %1, %2, 0 offen" : "=v"(x) : "v"(((bid >> 3) * 16 + (lane & 15)) * 128), "s"(rc) : "memory");
    return x;
  }
  DI void fresh() {
    asm volatile("" : "+v"(tid), "+s"(w), "+s"(bid));
    lane = tid & 63;
  }
  DI void set_tag(u32x2 e) {
    tag = (u32x2){__builtin_amdgcn_readfirstlane(e[0]) * 1664525u + 1013904223u, __builtin_amdgcn_readfirstlane(e[1]) * 22695477u + 1u};
  }
};

DI void conv_store(const Cx& c) {
  const int j = (c.bid & 7) * 32 + (c.bid >> 3), t = c.tid + (c.w >> 2) * 256;
  const char* smem = c.smem;
  u32x4 yo[2];
#pragma unroll
  for (int i = 0; i < 2; ++i) {
    const int p = t + 512 * i;
    unsigned yy[4];
#pragma unroll
    for (int ch = 0; ch < 4; ++ch) yy[ch] = *(const unsigned*)(smem + CV_PAD + ch * CV_CH + CV_HA + p * 4);
    yo[i] = (u32x4){__builtin_amdgcn_perm(yy[1], yy[0], 0x05040100u), __builtin_amdgcn_perm(yy[3], yy[2], 0x05040100u),
                    __builtin_amdgcn_perm(yy[1], yy[0], 0x07060302u), __builtin_amdgcn_perm(yy[3], yy[2], 0x07060302u)};
    const u32x4 x0v = *(const u32x4*)(smem + CV_X0 + p * 16);
    const f32x4 stv = *(const f32x4*)(smem + CV_ST + p * 16);
    u32x4 xo;
#pragma unroll
    for (int e = 0; e < 4; ++e) {
      const float m = stv[e & 2], rs = stv[(e & 2) + 1], xa = bflo(x0v[e]), xb = bfhi(x0v[e]);
      xo[e] = pk2(xa + (xa - m) * rs * bflo(yo[i][e]), xb + (xb - m) * rs * bfhi(yo[i][e]));
    }
    st16(c.rw, OFF_X1 + (j * 1024 + p) * 16, xo);
  }
  SB();
#pragma unroll
  for (int i = 0; i < 2; ++i) st16(c.rw, OFF_Y + (j * 1024 + t + 512 * i) * 16, yo[i]);
  wait_vm<2>();
  WAIT_LGKM0();
  BAR();
}

DI void front_m(Cx& c, const uint16_t* k, const uint16_t* v, const uint16_t* shift_k, const uint16_t* ssm_k, const uint16_t* ssm_D) {
  const int lane = c.lane, w = c.w, tid = c.tid, bid = c.bid, s0 = bid * 8, j = (bid & 7) * 32 + (bid >> 3);
  char* smem = c.smem;
  u32x4* bounce = (u32x4*)(smem + CV_X0) + w * 256;
  u32x4 uu[4], hh[4];
  unsigned hn[4];
  float Dc;
  u32x2 e0;
  {
    u32x2 kk[11], vv[8];
#pragma unroll
    for (int r = 0; r < 11; ++r) kk[r] = *(const u32x2*)(k + (size_t)((r < 3) ? max(s0 - 3 + r, 0) : s0 - 3 + r) * D + tid * 4);
#pragma unroll
    for (int r = 0; r < 8; ++r) vv[r] = *(const u32x2*)(v + (size_t)(s0 + r) * D + tid * 4);
    const u32x4 wa = *(const u32x4*)(shift_k + tid * 16), wb = *(const u32x4*)(shift_k + tid * 16 + 8);
    SB();
    const uint16_t* hrow = ssm_k + (size_t)(4 * j + w) * S;
#pragma unroll
    for (int i = 0; i < 4; ++i) {
      const int ch = i * 64 + lane;
      hh[i] = *(const u32x4*)(hrow + ch * 8);
      hn[i] = *(const unsigned*)(hrow + min(ch * 8 + 8, S - 2));
    }
    Dc = bflo((unsigned)ssm_D[4 * j + w]);
    e0 = poll8(c.ws + OFF_EPO);
    SB();
    if (tid < 2) *(unsigned*)(smem + LDS_CNT + tid * 16) = 0u;
    WAIT_LGKM0();
    BAR();
    SB();
    const unsigned hm = bid ? ~0u : 0u;
#pragma unroll
    for (int r = 0; r < 3; ++r) { kk[r][0] &= hm; kk[r][1] &= hm; }
    const int sw = (lane >> 1) & 3;
#pragma unroll
    for (int ch = 0; ch < 4; ++ch) {
      const unsigned w01 = (ch == 0) ? wa[0] : (ch == 1) ? wa[2] : (ch == 2) ? wb[0] : wb[2];
      const unsigned w23 = (ch == 0) ? wa[1] : (ch == 1) ? wa[3] : (ch == 2) ? wb[1] : wb[3];
      const float w0 = bflo(w01), w1 = bfhi(w01), w2 = bflo(w23), w3 = bfhi(w23);
      float kf[11], u[8];
#pragma unroll
      for (int r = 0; r < 11; ++r) kf[r] = (ch & 1) ? bfhi(kk[r][ch >> 1]) : bflo(kk[r][ch >> 1]);
#pragma unroll
      for (int r = 0; r < 8; ++r) {
        const float ks = kf[r] * w3 + kf[r + 1] * w2 + kf[r + 2] * w1 + kf[r + 3] * w0;
        u[r] = ks * ((ch & 1) ? bfhi(vv[r][ch >> 1]) : bflo(vv[r][ch >> 1]));
      }
      bounce[lane * 4 + (ch ^ sw)] = (u32x4){pk2(u[7], u[6]), pk2(u[5], u[4]), pk2(u[3], u[2]), pk2(u[1], u[0])};
    }
#pragma unroll
    for (int i = 0; i < 4; ++i) {
      const int u = 64 * i + lane;
      st16(c.rw, OFF_UB + (bid * 1024 + w * 256 + u) * 16, bounce[(u & ~3) | ((u & 3) ^ ((u >> 3) & 3))]);
    }
    STAMP(0, 2);
    SB();
  }
  char* my = smem + CV_PAD + w * CV_CH;
  if (lane < 30) *(u32x4*)(my + lane * 16) = (u32x4){0u, 0u, 0u, 0u};
  if (lane >= 32 && lane < 34) *(u32x4*)(my + CV_HA + (lane - 32) * 16) = (u32x4){0u, 0u, 0u, 0u};
  if (lane == 34) *(u32x4*)(my + CV_HB) = (u32x4){0u, 0u, 0u, 0u};
  if (lane == 0) *(u32x4*)(my + CV_HB + 16) = (u32x4){0u, 0u, 0u, hh[0][0] << 16};
#pragma unroll
  for (int i = 0; i < 4; ++i) {
    const int ch = i * 64 + lane;
    *(u32x4*)(my + CV_HA + 32 + ch * 16) = hh[i];
    *(u32x4*)(my + CV_HB + 32 + ch * 16) =
        (u32x4){__builtin_amdgcn_alignbyte(hh[i][1], hh[i][0], 2), __builtin_amdgcn_alignbyte(hh[i][2], hh[i][1], 2),
                __builtin_amdgcn_alignbyte(hh[i][3], hh[i][2], 2), __builtin_amdgcn_alignbyte(hn[i], hh[i][3], 2)};
  }
  asm volatile("" : "+v"(Dc), "+v"(e0));
  c.set_tag(e0);
  wait_vm<0>();
  c.signal(FL_U + w * 256 + bid);
  STAMP(0, 3);
  c.await(FL_U + (j >> 6) * 256 + w * 64 + lane, true);
  STAMP(0, 4);
#pragma unroll
  for (int i = 0; i < 4; ++i) uu[i] = ld16(c.rw, ((i * 16 + (lane >> 2)) * 1024 + (lane & 3)) * 16, OFF_UB + (w * 65536 + 4 * j) * 16);
#pragma unroll
  for (int i = 0; i < 4; ++i) *(u32x4*)(smem + CV_PAD + (lane & 3) * CV_CH + (30 + w * 64 + i * 16 + (lane >> 2)) * 16) = uu[i];
  const int t0 = lane & 15, kq = lane >> 4;
  const LAS unsigned* fbk[4];
#pragma unroll
  for (int i = 0; i < 4; ++i) {
    fbk[i] = (const LAS unsigned*)(my + ((t0 & 1) ? CV_HA : CV_HB)) + 4 * kq + ((1 + t0) >> 1) + 256 * i;
    asm volatile("" : "+v"(fbk[i]));
  }
  const char* gb = my + (2 * t0 + 3 - kq) * 16;
  f32x4 acc[8];
#pragma unroll
  for (int b = 0; b < 8; ++b) { acc[b] = (f32x4){0.f, 0.f, 0.f, 0.f}; asm volatile("" : "+v"(acc[b])); }
  WAIT_LGKM0();
  BAR();
  STAMP(1, 1);
  bf16x8 F[2][8], G[2][8];
  u32x2 fh[8];
  auto rdn = [&](int buf, int g0, int n) {
    if (n == 0 || n > 16) { const int g1 = n ? n - 16 : 0; G[buf][g1] = *(const bf16x8*)(gb + (32 * g1 + 4 * (7 - g0)) * 16); }
    else {
      const int e1 = (n - 1) >> 1;
      const LAS unsigned* p = fbk[e1 >> 1] + 128 * (e1 & 1) + 16 * g0;
      if ((n - 1) & 1) F[buf][e1] = as_frag((u32x4){fh[e1][0], fh[e1][1], p[2], p[3]});
      else fh[e1] = (u32x2){p[0], p[1]};
    }
  };
  auto cstep = [&](int cb, int g0n) {
    int n = 0;
#pragma unroll
    for (int g1 = 0; g1 < 8; ++g1)
#pragma unroll
      for (int e1 = 0; e1 + g1 < 8; ++e1) {
        acc[g1 + e1] = MFMA16(F[cb][e1], G[cb][g1], acc[g1 + e1]);
        SB();
        if (n < 24) { rdn(cb ^ 1, g0n, n); SB(); }
        ++n;
      }
  };
#pragma unroll
  for (int n = 0; n < 24; ++n) rdn(0, 0, n);
  for (int gp = 0; gp < 4; ++gp) {
    cstep(0, 1);
    cstep(1, 2);
    gb -= 128;
#pragma unroll
    for (int i = 0; i < 4; ++i) fbk[i] += 32;
  }
  asm volatile("" ::"v"(acc[7]), "v"(acc[0]));
  STAMP(1, 2);
#pragma unroll
  for (int b = 0; b < 8; ++b) {
    const int s = 256 * b + 16 * t0 + 4 * kq;
    const u32x2 ur = *(const u32x2*)(my + (30 + (s >> 3)) * 16 + ((s & 4) ? 0 : 8));
    *(u32x2*)(my + CV_HA + s * 2) = (u32x2){pk2(acc[b][0] + bfhi(ur[1]) * Dc, acc[b][1] + bflo(ur[1]) * Dc),
                                            pk2(acc[b][2] + bfhi(ur[0]) * Dc, acc[b][3] + bflo(ur[0]) * Dc)};
  }
  WAIT_LGKM0();
  BAR();
  STAMP(1, 3);
  conv_store(c);
  STAMP(1, 6);
  if (w == 0) c.signal(FL_X1 + bid);
  const unsigned pf = c.touch_code();
  c.await(FL_X1 + w * 64 + lane, true);
  KEEP(pf);
  STAMP(1, 7);
  BAR();
  if (bid == 0 && tid == 0) put8(c.ws + OFF_EPO, c.tag);
}

DI void front_h(Cx& c, const uint16_t* q, const uint16_t* wqkv, const uint16_t* wo) {
  const int lane = c.lane, w4 = c.w - 4, tid = c.tid, bid = c.bid, s0 = bid * 8, j = (bid & 7) * 32 + (bid >> 3);
  char* smem = c.smem;
  const rsrc_t rw = c.rw;
  u32x4* bounce = (u32x4*)(smem + CV_X0) + c.w * 256;
  float* red = (float*)smem;
  u32x2 qq[8];
#pragma unroll
  for (int r = 0; r < 8; ++r) qq[r] = *(const u32x2*)(q + (size_t)(s0 + r) * D + tid * 4);
  SB();
  u32x2 e0 = poll8(c.ws + OFF_EPO);
  const unsigned pf = c.touch_code();
  SB();
  BAR();
  const int sw = (lane >> 1) & 3;
#pragma unroll
  for (int r = 0; r < 4; ++r) bounce[lane * 4 + (r ^ sw)] = (u32x4){qq[2 * r][0], qq[2 * r][1], qq[2 * r + 1][0], qq[2 * r + 1][1]};
#pragma unroll
  for (int i = 0; i < 4; ++i) {
    const int u = 64 * i + lane;
    st16(rw, OFF_X0 + (bid * 1024 + w4 * 256 + u) * 16, bounce[(u & ~3) | ((u & 3) ^ ((u >> 3) & 3))]);
  }
  if (tid < 48) st16(rw, OFF_GX + bid * 768 + tid * 16, (u32x4){~0u, ~0u, ~0u, ~0u});
  float a[16];
#pragma unroll
  for (int r = 0; r < 8; ++r) {
    a[2 * r] = dot2(qq[r][0], 0x3f803f80u, dot2(qq[r][1], 0x3f803f80u, 0.f));
    a[2 * r + 1] = dot2(qq[r][0], qq[r][0], dot2(qq[r][1], qq[r][1], 0.f));
  }
#pragma unroll
  for (int i = 0; i < 8; ++i) a[i] = swap32_sum(a[i], a[i + 8]);
#pragma unroll
  for (int i = 0; i < 4; ++i) a[i] = swap16_sum(a[i], a[i + 4]);
#pragma unroll
  for (int i = 0; i < 4; ++i) { a[i] += row_ror<1>(a[i]); a[i] += row_ror<2>(a[i]); a[i] += row_ror<4>(a[i]); a[i] += row_ror<8>(a[i]); }
  if ((lane & 15) == 0) *(f32x4*)&red[w4 * 16 + (lane >> 4) * 4] = (f32x4){a[0], a[1], a[2], a[3]};
  asm volatile("" : "+v"(e0));
  c.set_tag(e0);
  wait_vm<0>();
  KEEP(pf);
  c.half_sync(1);
  if (w4 == 0) {
    if (lane < 8) {
      const float sm = (red[2 * lane] + red[16 + 2 * lane]) + (red[32 + 2 * lane] + red[48 + 2 * lane]);
      const float sq = (red[2 * lane + 1] + red[17 + 2 * lane]) + (red[33 + 2 * lane] + red[49 + 2 * lane]);
      st8(rw, OFF_ST0 + (s0 + lane) * 8, __builtin_bit_cast(u32x2, ln_stats(sm, sq)));
    }
    wait_vm<0>();
    c.signal(FL_Q + bid);
  }
  STAMPW(0, 5, 4);
  const int gw = bid * 4 + w4;
  char* buf = smem + CV_WB + w4 * 8192;
  const int g = lane >> 4, qr = (lane >> 2) & 3, p = lane & 3, rr = lane >> 3;
  const bool isw = gw < 768;
  const int cg = isw ? gw % 48 : (gw - 768) & 15, og = isw ? gw / 48 : (gw - 768) >> 4;
  if (isw) {
    const uint16_t* src = wqkv + (size_t)(og * 64 + rr) * 3072 + cg * 64 + (lane & 7) * 8;
#pragma unroll
    for (int i = 0; i < 8; ++i) glds16(src + (size_t)i * 8 * 3072, buf + i * 1024);
  } else {
    const uint16_t* src = wo + (size_t)(og * 64 + (rr & 3) + 16 * (rr >> 2)) * 1024 + cg * 64 + (lane & 7) * 8;
#pragma unroll
    for (int i = 0; i < 8; ++i) glds16(src + (size_t)((i >> 2) * 32 + (i & 3) * 4) * 1024, buf + i * 1024);
  }
  BAR();
  c.await(FL_Q + w4 * 64 + lane, true);
  const int hd = (cg & 15) * 2 + (lane >> 5), col = (cg >> 4) * 32 + (lane & 31);
  const unsigned dst = isw ? OFF_W8 + ((hd * 128 + og * 8) * 96 + col) * 16
                           : OFF_WOF + ((((og >> 1) * 32 + cg * 2 + (g >> 1)) * 8 + (og & 1) * 4) * 64 + (lane & 31)) * 16;
  const unsigned dstep = isw ? 1536 : 512;
  const unsigned la = (unsigned)(size_t)(LAS char*)buf + qr * 128 + (isw ? g * 32 + p * 8 : (g >> 1) * 64 + (p & 1) * 32 + ((g & 1) * 2 + (p >> 1)) * 8);
  wait_vm<0>();
  u32x2 lo[8], hi[8];
#pragma unroll
  for (int i = 0; i < 8; ++i) {
    lo[i] = __builtin_bit_cast(u32x2, __builtin_amdgcn_ds_read_tr16_b64_v4i16((LAS s16x4*)(size_t)(la + i * 1024)));
    hi[i] = __builtin_bit_cast(u32x2, __builtin_amdgcn_ds_read_tr16_b64_v4i16((LAS s16x4*)(size_t)(la + i * 1024 + 512)));
  }
  SB();
#pragma unroll
  for (int i = 0; i < 4; ++i) {
    const int pp = w4 * 256 + i * 64 + lane;
    dma16(rw, smem + CV_X0 + (w4 * 4 + i) * 1024, ((pp >> 2) * 1024 + (pp & 3)) * 16, OFF_X0 + j * 64);
    dma16(rw, smem + CV_ST + (w4 * 4 + i) * 1024, pp * 16, OFF_ST0);
  }
  SB();
#pragma unroll
  for (int i = 0; i < 8; ++i) st16(rw, dst + i * dstep, (u32x4){lo[i][0], lo[i][1], hi[i][0], hi[i][1]});
  if (lane < 16 + 1 - (gw & 1)) st16(rw, OFF_KV + (gw * 33 / 2 + (gw & 1) + lane) * 16, (u32x4){0u, 0u, 0u, 0u});
  if (isw) {
    float c0 = 0.f, c1 = 0.f;
#pragma unroll
    for (int i = 0; i < 8; ++i)
#pragma unroll
      for (int e = 0; e < 2; ++e) {
        c0 = opq(opq(c0 + bflo(lo[i][e])) + bflo(hi[i][e]));
        c1 = opq(opq(c1 + bfhi(lo[i][e])) + bfhi(hi[i][e]));
      }
    __builtin_amdgcn_raw_buffer_store_b32(__float_as_uint(c0 + c1), rw, OFF_CSP + ((hd * 96 + col) * 16 + og) * 4, 0, 16);
  }
  STAMPW(1, 4, 4);
  wait_vm<9>();
  BAR();
  conv_store(c);
  BAR();
}

template <int L> DI void g1_m(const Cx& c) {
  constexpr bool C4 = L == 0;
  constexpr int kid = 2 + 2 * L;
  const int lane = c.lane, w = c.w, tid = c.tid, xcd = c.bid & 7, idx = c.bid >> 3;
  const int band = (xcd & 1) * 4 + (idx & 3), h = (xcd >> 1) * 8 + (idx >> 2), r = lane & 15, kq = lane >> 4;
  char* smem = c.smem;
  const float* Rs = (const float*)(smem + G1_SCR);
  const float* MRs = Rs + 256;
  const float* CSs = Rs + 512;
  float* P = (float*)(smem + G1_SCR) + 640;
  const char* ab = C4 ? smem + kq * 4096 + ((64 * w + r) ^ (16 * (kq & 1))) * 8 : smem + kq * 4096 + (64 * w + r) * 16;
  const char* ab2 = ab + 128 - (kq & 1) * 256;
  const char* bb = smem + G1_AB + kq * 1536 + r * 16;
  STAMP(kid, 0);
  f32x4 acc[4][6];
#pragma unroll
  for (int m = 0; m < 4; ++m)
#pragma unroll
    for (int p = 0; p < 6; ++p) { acc[m][p] = (f32x4){0.f, 0.f, 0.f, 0.f}; asm volatile("" : "+v"(acc[m][p])); }
  bf16x8 F[2][10];
  auto rd1 = [&](int buf, const char* a, const char* a2, const char* b, int t, int i) {
    if (i < 4) {
      if (C4) {
        const char* p = ((i & 1) ? a2 : a) + t * 16384 + (i >> 1) * 256;
        const u32x2 lo = *(const u32x2*)p, hi = *(const u32x2*)(p + 2048);
        F[buf][i] = as_frag((u32x4){lo[0], lo[1], hi[0], hi[1]});
      } else F[buf][i] = *(const bf16x8*)(a + t * 16384 + i * 256);
    } else F[buf][i] = *(const bf16x8*)(b + t * 6144 + (i - 4) * 256);
  };
  auto step = [&](int cb, const char* a, const char* a2, const char* b, int t, bool rd) {
    const int ord[10] = {0, 4, 5, 6, 7, 8, 9, 1, 2, 3};
#pragma unroll
    for (int i = 0; i < 24; ++i) {
      const int m = i / 6, p = i % 6;
      if (p < 2) acc[m][p] = MFMA16(F[cb][4 + p], F[cb][m], acc[m][p]);
      else acc[m][p] = MFMA16(F[cb][m], F[cb][4 + p], acc[m][p]);
      SB();
      if (rd && (i & 1) && (i >> 1) < 10) { rd1(cb ^ 1, a, a2, b, t, ord[i >> 1]); SB(); }
    }
  };
  BAR();
  STAMP(kid, 1);
#pragma unroll
  for (int i = 0; i < 10; ++i) rd1(0, ab, ab2, bb, 0, i);
  int so = 0;
  for (int it = 0; it < 15; ++it) {
    step(0, ab + so, ab2 + so, bb + so, 1, true);
    WAIT_LGKM0();
    BAR();
    so = (so == 2 * G1_STAGE) ? 0 : so + G1_STAGE;
    step(1, ab + so, ab2 + so, bb + so, 0, true);
  }
  step(0, ab + so, ab2 + so, bb + so, 1, true);
  BAR();
  step(1, ab, ab2, bb, 0, false);
  STAMP(kid, 2);
  f32x4 Z[2][2], Zz[2];
#pragma unroll
  for (int i = 0; i < 2; ++i) { Z[i][0] = (f32x4){0.f, 0.f, 0.f, 0.f}; Z[i][1] = Z[i][0]; Zz[i] = Z[i][0]; }
  {
    float ck[4];
#pragma unroll
    for (int p = 0; p < 4; ++p) ck[p] = CSs[32 + p * 16 + r];
    const bf16x8 ones = as_frag((u32x4){0x3f803f80u, 0x3f803f80u, 0x3f803f80u, 0x3f803f80u});
#pragma unroll
    for (int jj = 0; jj < 2; ++jj) {
      u32x4 fr[4];
#pragma unroll
      for (int mm = 0; mm < 2; ++mm) {
        const int m = 2 * jj + mm;
        const f32x4 rs = *(const f32x4*)(Rs + 64 * w + 16 * m + 4 * kq), mr = *(const f32x4*)(MRs + 64 * w + 16 * m + 4 * kq);
#pragma unroll
        for (int p = 0; p < 2; ++p) {
          fr[p][2 * mm] = pk2v(phi2(lo2(acc[m][2 + p]) * lo2(rs) + (lo2(mr) * ck[p] + 1e-6f)));
          fr[p][2 * mm + 1] = pk2v(phi2(hi2(acc[m][2 + p]) * hi2(rs) + (hi2(mr) * ck[p] + 1e-6f)));
          fr[2 + p][2 * mm] = pk2v(lo2(acc[m][4 + p]) * lo2(rs) + lo2(mr) * ck[2 + p]);
          fr[2 + p][2 * mm + 1] = pk2v(hi2(acc[m][4 + p]) * hi2(rs) + hi2(mr) * ck[2 + p]);
        }
      }
#pragma unroll
      for (int dh = 0; dh < 2; ++dh) {
        Z[0][dh] = MFMA16(as_frag(fr[dh]), as_frag(fr[2]), Z[0][dh]);
        Z[1][dh] = MFMA16(as_frag(fr[dh]), as_frag(fr[3]), Z[1][dh]);
        Zz[dh] = MFMA16(as_frag(fr[dh]), ones, Zz[dh]);
      }
    }
  }
  {
    float* mine = P + w * 1056;
#pragma unroll
    for (int i = 0; i < 4; ++i) *(f32x4*)(mine + i * 256 + lane * 4) = Z[i >> 1][i & 1];
    if (r == 0) { *(f32x4*)(mine + 1024 + 4 * kq) = Zz[0]; *(f32x4*)(mine + 1040 + 4 * kq) = Zz[1]; }
  }
  c.half_sync(2 * L + 1);
  STAMP(kid, 3);
  float* kvacc = (float*)(c.ws + OFF_KV) + L * KV_LAYER;
#pragma unroll
  for (int i = 0; i < 4; ++i) {
    const int e = tid + 256 * i;
    atomicAdd(kvacc + h * 1024 + e, (P[e] + P[1056 + e]) + (P[2112 + e] + P[3168 + e]));
  }
  if (tid < 32) atomicAdd(kvacc + 32768 + h * 32 + tid, (P[1024 + tid] + P[2080 + tid]) + (P[3136 + tid] + P[4192 + tid]));
  SB();
  {
    const f32x4 c0 = *(const f32x4*)(CSs + 4 * kq), c1 = *(const f32x4*)(CSs + 16 + 4 * kq);
#pragma unroll
    for (int m = 0; m < 4; ++m) {
      const float rs = Rs[64 * w + 16 * m + r], mr = MRs[64 * w + 16 * m + r];
      const u32x4 o = {pk2v(phi2(lo2(acc[m][0]) * rs + (lo2(c0) * mr + 1e-6f))), pk2v(phi2(hi2(acc[m][0]) * rs + (hi2(c0) * mr + 1e-6f))),
                       pk2v(phi2(lo2(acc[m][1]) * rs + (lo2(c1) * mr + 1e-6f))), pk2v(phi2(hi2(acc[m][1]) * rs + (hi2(c1) * mr + 1e-6f)))};
      st16(c.rw, OFF_PQ + L * 4 * MB + ((((h >> 2) * 128 + band * 16 + w * 4 + m) * 4 + (h & 3)) * 64 + lane) * 16, o);
    }
  }
  STAMP(kid, 6);
  wait_vm<0>();
  c.half_sync(2 * L + 2);
  STAMP(kid, 7);
  if (w == 0) c.signal(FL_G1 + L * 256 + h * 8 + band);
}

template <int L> DI void g1_h(const Cx& c) {
  constexpr bool C4 = L == 0;
  constexpr unsigned OFF_X = C4 ? OFF_X1 : OFF_X3;
  const int lane = c.lane, lw = c.w - 4, t = c.tid, xcd = c.bid & 7, idx = c.bid >> 3;
  const int band = (xcd & 1) * 4 + (idx & 3), h = (xcd >> 1) * 8 + (idx >> 2);
  char* smem = c.smem;
  char* ws = c.ws;
  const rsrc_t rw = c.rw;
  float* Rs = (float*)(smem + G1_SCR);
  const unsigned l0 = lane * 16, l1 = (lane ^ 8) * 16;
  auto issue = [&](int st) {
    char* base = smem + (st % 3) * G1_STAGE;
#pragma unroll
    for (int p = 0; p < 8; ++p) {
      if (C4) dma16(rw, base + (8 * lw + p) * 1024, (p & 4) ? l1 : l0, OFF_X + ((st * 16 + lw * 4 + (p >> 1)) * 1024 + band * 128 + (p & 1) * 64) * 16);
      else dma16(rw, base + (8 * lw + p) * 1024, l0, OFF_X + ((st * 8 + lw * 2 + (p >> 2)) * S + band * 256 + (p & 3) * 64) * 16);
    }
#pragma unroll
    for (int p = 0; p < 3; ++p) dma16(rw, base + G1_AB + (3 * lw + p) * 1024, l0, OFF_W8 + (h * 128 + st * 8) * 1536 + (3 * lw + p) * 1024);
  };
  issue(0); issue(1);
  wait_vm<G1_NP>();
  BAR();
  issue(2);
  u32x4 xv[4];
  float v[4] = {0.f, 0.f, 0.f, 0.f};
  if (C4) {
    const u32x4* xp = (const u32x4*)(ws + OFF_X) + (size_t)(t >> 2) * 1024 + band * 128 + h * 4 + (t & 3);
    asm volatile("global_load_dwordx4 %0, %4, off sc1\n\tglobal_load_dwordx4 %1, %5, off sc1\n\tglobal_load_dwordx4 %2, %6, off sc1\n\tglobal_load_dwordx4 %3, %7, off sc1"
                 : "=&v"(xv[0]), "=&v"(xv[1]), "=&v"(xv[2]), "=&v"(xv[3]) : "v"(xp), "v"(xp + 65536), "v"(xp + 131072), "v"(xp + 196608) : "memory");
  }
#pragma unroll
  for (int i = 0; i < 13; ++i) {
    if (C4 && i < 2) wait_vm<G1_NP + 4>();
    else if (C4 && i == 2) asm volatile("s_waitcnt vmcnt(%4)" : "+v"(xv[0]), "+v"(xv[1]), "+v"(xv[2]), "+v"(xv[3]) : "i"(G1_NP) : "memory");
    else wait_vm<G1_NP>();
    BAR();
    issue(i + 3);
    if (C4 && i >= 2 && i < 6) {
      const u32x4 x = xv[i - 2];
#pragma unroll
      for (int e = 0; e < 4; ++e) {
        const float lo = bflo(x[e]), hi = bfhi(x[e]);
        v[e & 2] = opq(v[e & 2] + lo); v[(e & 2) + 1] = opq(fmaf(lo, lo, v[(e & 2) + 1]));
        v[e & 2] = opq(v[e & 2] + hi); v[(e & 2) + 1] = opq(fmaf(hi, hi, v[(e & 2) + 1]));
      }
      asm volatile("" : "+v"(v[0]), "+v"(v[1]), "+v"(v[2]), "+v"(v[3]));
    }
    if (C4 && i == 6) {
#pragma unroll
      for (int e = 0; e < 4; ++e) { v[e] += row_ror<4>(v[e]); v[e] += row_ror<8>(v[e]); v[e] = swap16_sum(v[e], v[e]); v[e] = swap32_sum(v[e], v[e]); }
      const bool up = (lane & 4) != 0;
      if (lane < 8) publish8(ws + OFF_GX + ((band * 4 + lw) * 256 + h * 8 + 2 * (lane & 3) + (lane >> 2)) * 8, up ? v[2] : v[0], up ? v[3] : v[1]);
    }
  }
  const char* gp = ws + (C4 ? OFF_GX + (band * 1024 + t) * 8 + 4096 : OFF_ST3 + ((band * 4 + lw) * 512 + lane) * 8);
  const char* cp = ws + OFF_CSP + (h * 96 + (t < 96 ? t : 0)) * 64;
  u32x2 g[8];
  f32x4 c0, c1, c2, c3;
  asm volatile("global_load_dwordx4 %0, %4, off sc1\n\tglobal_load_dwordx4 %1, %4, off offset:16 sc1\n\t"
               "global_load_dwordx4 %2, %4, off offset:32 sc1\n\tglobal_load_dwordx4 %3, %4, off offset:48 sc1"
               : "=&v"(c0), "=&v"(c1), "=&v"(c2), "=&v"(c3) : "v"(cp) : "memory");
  if (C4) {
    asm volatile("global_load_dwordx2 %0, %4, off offset:-4096 sc1\n\tglobal_load_dwordx2 %1, %4, off offset:-2048 sc1\n\t"
                 "global_load_dwordx2 %2, %4, off sc1\n\tglobal_load_dwordx2 %3, %4, off offset:2048 sc1"
                 : "=&v"(g[0]), "=&v"(g[1]), "=&v"(g[2]), "=&v"(g[3]) : "v"(gp) : "memory");
    wait_vm<G1_NP + 8>();
  } else {
    asm volatile("global_load_dwordx2 %0, %8, off sc1\n\tglobal_load_dwordx2 %1, %8, off offset:512 sc1\n\tglobal_load_dwordx2 %2, %8, off offset:1024 sc1\n\t"
                 "global_load_dwordx2 %3, %8, off offset:1536 sc1\n\tglobal_load_dwordx2 %4, %8, off offset:2048 sc1\n\tglobal_load_dwordx2 %5, %8, off offset:2560 sc1\n\t"
                 "global_load_dwordx2 %6, %8, off offset:3072 sc1\n\tglobal_load_dwordx2 %7, %8, off offset:3584 sc1"
                 : "=&v"(g[0]), "=&v"(g[1]), "=&v"(g[2]), "=&v"(g[3]), "=&v"(g[4]), "=&v"(g[5]), "=&v"(g[6]), "=&v"(g[7]) : "v"(gp) : "memory");
    wait_vm<G1_NP + 12>();
  }
  BAR();
  if (C4) asm volatile("s_waitcnt vmcnt(0)" : "+v"(g[0]), "+v"(g[1]), "+v"(g[2]), "+v"(g[3]), "+v"(c0), "+v"(c1), "+v"(c2), "+v"(c3)::"memory");
  else asm volatile("s_waitcnt vmcnt(0)" : "+v"(g[0]), "+v"(g[1]), "+v"(g[2]), "+v"(g[3]), "+v"(g[4]), "+v"(g[5]), "+v"(g[6]), "+v"(g[7]),
                    "+v"(c0), "+v"(c1), "+v"(c2), "+v"(c3)::"memory");
  BAR();
  if (C4) {
#pragma unroll 1
    for (int spin = 0; spin < SPIN_MAX; ++spin) {
      if (!__any(g[0][1] == ~0u || g[1][1] == ~0u || g[2][1] == ~0u || g[3][1] == ~0u)) break;
      __builtin_amdgcn_s_sleep(4);
#pragma unroll
      for (int k = 0; k < 4; ++k) g[k] = poll8(gp + (k - 2) * 2048);
    }
  }
  float sm = 0.f, sq = 0.f, cs = 0.f;
#pragma unroll
  for (int k = 0; k < (C4 ? 4 : 8); ++k) { sm = opq(sm + __uint_as_float(g[k][0])); sq = opq(sq + __uint_as_float(g[k][1])); }
#pragma unroll
  for (int k = 0; k < 4; ++k) cs = opq(opq(opq(opq(cs + c0[k]) + c1[k]) + c2[k]) + c3[k]);
  const f32x2 st = ln_stats(sm, sq);
  Rs[t] = st[1]; Rs[256 + t] = -st[0] * st[1];
  if (t < 96) Rs[512 + t] = cs;
  WAIT_LGKM0();
  BAR();
}

template <int L> DI void g2_m(const Cx& c) {
  constexpr int kid = 3 + 2 * L;
  constexpr unsigned KVO = OFF_KV + L * KV_LAYER * 4;
  const int lane = c.lane, w = c.w, xcd = c.bid & 7, idx = c.bid >> 3, sb = (xcd >> 1) * 8 + (idx & 7), kq = lane >> 4;
  const rsrc_t rw = c.rw;
  const unsigned l16 = lane * 16;
  u32x4* O = (u32x4*)c.smem;
  unsigned pf = c.touch_code();
  c.await(FL_G1 + L * 256 + ((lane >> 3) * 4 + w) * 8 + (lane & 7), true);
  KEEP(pf);
  STAMP(kid, 0);
  constexpr int PL = 2, PD = PL + 1;
  u32x4 pq[PD][4];
  f32x4 kv[PD][4];
  const unsigned spq = OFF_PQ + L * 4 * MB + (sb * 16 + w) * 1024, skv = KVO + w * 4096;
  auto ld = [&](int i, int n) {
    if (n < 4) kv[i % PD][n] = __builtin_bit_cast(f32x4, ld16(rw, l16 + n * 1024, skv + i * 16384));
    else pq[i % PD][n - 4] = ld16(rw, l16, spq + (i * 512 + (n - 4) * 4) * 1024);
    SB();
  };
  const f32x4 zall = __builtin_bit_cast(f32x4, ld16(rw, (lane >> 3) * 512 + (lane & 7) * 16, KVO + 131072 + w * 128));
  SB();
#pragma unroll
  for (int n = 0; n < 8 * PL; ++n) ld(n >> 3, n & 7);
  u32x2* Zs = (u32x2*)(c.smem + G2_Z) + w * 64;
  Zs[lane] = (u32x2){pk2v(lo2(zall)), pk2v(hi2(zall))};
  const f32x4 zero = {0.f, 0.f, 0.f, 0.f}, eps = {1e-6f, 1e-6f, 1e-6f, 1e-6f};
  bf16x8 k0, k1, zb;
  auto prep = [&](int i) {
    const int b = i % PD;
    const u32x2 za = Zs[i * 8 + kq], zc = Zs[i * 8 + 4 + kq];
    k0 = as_frag((u32x4){pk2v(lo2(kv[b][0])), pk2v(hi2(kv[b][0])), pk2v(lo2(kv[b][1])), pk2v(hi2(kv[b][1]))});
    k1 = as_frag((u32x4){pk2v(lo2(kv[b][2])), pk2v(hi2(kv[b][2])), pk2v(lo2(kv[b][3])), pk2v(hi2(kv[b][3]))});
    zb = as_frag((u32x4){za[0], za[1], zc[0], zc[1]});
    asm volatile("" : "+v"(k0), "+v"(k1), "+v"(zb));
  };
  prep(0);
#pragma unroll
  for (int i = 0; i < 8; ++i) {
    const int b = i % PD;
    f32x4 x0[4], x1[4], dn[4];
    __builtin_amdgcn_s_setprio(2);
#pragma unroll
    for (int st = 0; st < 4; ++st) {
      dn[st] = MFMA16(zb, as_frag(pq[b][st]), eps);
      x0[st] = MFMA16(k0, as_frag(pq[b][st]), zero);
      x1[st] = MFMA16(k1, as_frag(pq[b][st]), zero);
    }
    __builtin_amdgcn_s_setprio(0);
    SB();
#pragma unroll
    for (int st = 0; st < 4; ++st) {
      const float inv = __builtin_amdgcn_rcpf(dn[st][0]);
      O[((i * 4 + w) * 4 + st) * 64 + lane] =
          (u32x4){pk2(opq(x0[st][0] * inv), opq(x0[st][1] * inv)), pk2(opq(x0[st][2] * inv), opq(x0[st][3] * inv)),
                  pk2(opq(x1[st][0] * inv), opq(x1[st][1] * inv)), pk2(opq(x1[st][2] * inv), opq(x1[st][3] * inv))};
      SB();
      if (i + PL < 8) { ld(i + PL, 2 * st); ld(i + PL, 2 * st + 1); }
    }
    if (i < 7) prep(i + 1);
    WAIT_LGKM0();
    BAR();
    if (i == 0) STAMP(kid, 1);
  }
  if (L == 0) {
    const int band = (xcd & 1) * 4 + (idx & 3);
    pf = c.touch_code();
    if (w == 0) c.await(FL_X3 + band * 32 + (lane & 31), lane < 32);
    wait_vm<0>();
    KEEP(pf);
    STAMP(kid, 5);
    BAR();
  }
}

template <int L> DI void g2_h(const Cx& cx, char* out) {
  constexpr int kid = 3 + 2 * L;
  const int lane = cx.lane, c = cx.w - 4, xcd = cx.bid & 7, idx = cx.bid >> 3;
  const int sb = (xcd >> 1) * 8 + (idx & 7), nb = (xcd & 1) * 4 + (idx >> 3);
  const int n0 = nb * 128 + c * 32, s0 = sb * 64, r = lane & 31, hf = lane >> 5;
  const rsrc_t rw = cx.rw;
  const unsigned l16 = lane * 16;
  constexpr int CD = 5, LA = 6;
  u32x4 wo[CD][8], of[8];
  const unsigned swo = OFF_WOF + (nb * 4 + c) * 8192;
  auto ldw = [&](int i, int n) { wo[i % CD][n] = ld16(rw, l16 + (n & 3) * 1024, swo + i * 262144 + (n >> 2) * 4096); SB(); };
#pragma unroll
  for (int n = 0; n < 8 * CD; ++n) ldw(n >> 3, n & 7);
  f32x16 acc[2];
#pragma unroll
  for (int i = 0; i < 16; ++i) { acc[0][i] = 0.f; acc[1][i] = 0.f; }
  asm volatile("" : "+v"(acc[0]), "+v"(acc[1]));
  u32x4 res[2][2], yv[2][2];
  auto ldres = [&](int k) {
    const int s2 = (k >> 1) & 1, g = k & 1;
    if (L == 1) { if (k < 4) res[s2][g] = ld16(rw, (2 * hf * S + r) * 16 + s2 * 512, OFF_X3 + (((n0 >> 3) + g) * S + s0) * 16); }
    else {
      const unsigned so = (k < 4 ? OFF_X1 : OFF_Y) + (((n0 >> 2) + 2 * g) * S + s0) * 8, vo = (4 * hf * S + r) * 8 + s2 * 256;
      const u32x2 lo = ld8(rw, vo, so), hi = ld8(rw, vo, so + S * 8);
      if (k < 4) res[s2][g] = (u32x4){lo[0], lo[1], hi[0], hi[1]}; else yv[s2][g] = (u32x4){lo[0], lo[1], hi[0], hi[1]};
    }
    SB();
  };
  const u32x4* Ob = (const u32x4*)cx.smem + (r >> 4) * 64 + hf * 16 + (r & 15);
  auto rdo = [&](int f) { const int u = f & 15; of[f & 7] = Ob[((f >> 4) * 16 + (u >> 2) * 4 + (u & 1) * 2) * 64 + ((u >> 1) & 1) * 32]; SB(); };
  __builtin_amdgcn_s_setprio(1);
  BAR();
#pragma unroll
  for (int f = 0; f < LA; ++f) rdo(f);
#pragma unroll
  for (int i = 0; i < 8; ++i)
#pragma unroll
    for (int n = 0; n < 16; ++n) {
      const int f = i * 16 + n;
      acc[n & 1] = MFMA32(as_frag(wo[i % CD][n >> 1]), as_frag(of[f & 7]), acc[n & 1]);
      SB();
      if (f + LA < 128) { if (((f + LA) & 15) == 0) BAR(); rdo(f + LA); }
      if ((n & 1) && i + CD < 8) ldw(i + CD, n >> 1);
      if (i == 5 && (n & 1)) ldres(n >> 1);
    }
  __builtin_amdgcn_s_setprio(0);
  STAMPW(kid, 2, 4);
  u32x4 x[2][2];
#pragma unroll
  for (int s2 = 0; s2 < 2; ++s2)
#pragma unroll
    for (int g = 0; g < 2; ++g)
#pragma unroll
      for (int e = 0; e < 4; ++e) x[s2][g][e] = pk2(bflo(res[s2][g][e]) + acc[s2][8 * g + 2 * e], bfhi(res[s2][g][e]) + acc[s2][8 * g + 2 * e + 1]);
  if (L == 1) {
#pragma unroll
    for (int s2 = 0; s2 < 2; ++s2)
#pragma unroll
      for (int g = 0; g < 2; ++g) *(u32x4*)(out + ((size_t)(s0 + 32 * s2 + r) * D + n0 + 16 * hf + 8 * g) * 2) = x[s2][g];
    STAMPW(kid, 6, 4); DRAIN(); STAMPW(kid, 7, 4);
    return;
  }
  f32x2* Red = (f32x2*)(cx.smem + G2_RED);
  auto rowpart = [&]() {
    float v[4] = {0.f, 0.f, 0.f, 0.f};
#pragma unroll
    for (int e = 0; e < 8; ++e)
#pragma unroll
      for (int s2 = 0; s2 < 2; ++s2) { const unsigned u = x[s2][e >> 2][e & 3]; v[2 * s2] = dot2(u, 0x3f803f80u, v[2 * s2]); v[2 * s2 + 1] = dot2(u, u, v[2 * s2 + 1]); }
    return (f32x2){swap32_sum(v[0], v[2]), swap32_sum(v[1], v[3])};
  };
  Red[c * 64 + lane] = rowpart();
  cx.half_sync(2);
  char* gx = cx.ws + OFF_GX2 + (sb * 512 + lane) * 8;
  if (c == 0) {
    const f32x2 t = (Red[lane] + Red[64 + lane]) + (Red[128 + lane] + Red[192 + lane]);
    publish8(gx + nb * 512, t[0], t[1]);
  }
  STAMPW(kid, 4, 4);
  float sm, sq;
#pragma unroll 1
  for (int spin = 0; spin < SPIN_MAX; ++spin) {
    u32x2 g[8];
#pragma unroll
    for (int k = 0; k < 8; ++k) g[k] = poll8(gx + k * 512);
    bool empty = false;
    sm = 0.f; sq = 0.f;
#pragma unroll
    for (int k = 0; k < 8; ++k) { empty |= g[k][1] == ~0u; sm += __uint_as_float(g[k][0]); sq += __uint_as_float(g[k][1]); }
    if (!__any(empty)) break;
    __builtin_amdgcn_s_sleep(1);
  }
  STAMPW(kid, 3, 4);
  const f32x2 st = ln_stats(sm, sq);
  const auto mm = __builtin_amdgcn_permlane32_swap(__float_as_uint(st[0]), __float_as_uint(st[0]), false, false);
  const auto rr = __builtin_amdgcn_permlane32_swap(__float_as_uint(st[1]), __float_as_uint(st[1]), false, false);
#pragma unroll
  for (int s2 = 0; s2 < 2; ++s2) {
    const float mu = __uint_as_float(mm[s2]), rs = __uint_as_float(rr[s2]);
#pragma unroll
    for (int g = 0; g < 2; ++g) {
#pragma unroll
      for (int e = 0; e < 4; ++e) {
        const float a = bflo(x[s2][g][e]), b = bfhi(x[s2][g][e]);
        x[s2][g][e] = pk2(a + (a - mu) * rs * bflo(yv[s2][g][e]), b + (b - mu) * rs * bfhi(yv[s2][g][e]));
      }
      st16(rw, OFF_X3 + (((n0 >> 3) + 2 * hf + g) * S + s0 + 32 * s2 + r) * 16, x[s2][g]);
    }
  }
  Red[256 + c * 64 + lane] = rowpart();
  cx.half_sync(3);
  if (c == 0) {
    const f32x2 t = (Red[256 + lane] + Red[320 + lane]) + (Red[384 + lane] + Red[448 + lane]);
    st8(rw, OFF_ST3 + ((sb * 8 + nb) * 64 + lane) * 8, __builtin_bit_cast(u32x2, t));
  }
  STAMPW(kid, 6, 4);
  wait_vm<0>();
  cx.half_sync(4);
  if (c == 0) cx.signal(FL_X3 + sb * 8 + nb);
  STAMPW(kid, 7, 4);
  BAR();
}

__global__ __launch_bounds__(512, 2) void fused_kernel(const uint16_t* __restrict__ q, const uint16_t* __restrict__ k,
                                                       const uint16_t* __restrict__ v, const uint16_t* __restrict__ shift_k,
                                                       const uint16_t* __restrict__ ssm_k, const uint16_t* __restrict__ ssm_D,
                                                       const uint16_t* __restrict__ wqkv, const uint16_t* __restrict__ wo,
                                                       char* __restrict__ ws, char* __restrict__ out) {
  extern __shared__ __attribute__((aligned(16))) char smem[];
  unsigned long long pc;
  asm volatile("s_getpc_b64 %0" : "=s"(pc));
  STAMP(0, 0);
  Cx c;
  c.ws = ws; c.smem = smem; c.rw = mk_rsrc(ws, WS_USED); c.rc = mk_rsrc((void*)(pc & ~255ull), CODE_BYTES);
  c.lane = threadIdx.x & 63; c.w = __builtin_amdgcn_readfirstlane(threadIdx.x >> 6); c.tid = threadIdx.x & 255; c.bid = blockIdx.x;
  if (c.w < 4) {
    front_m(c, k, v, shift_k, ssm_k, ssm_D);
    c.fresh(); g1_m<0>(c); c.fresh(); g2_m<0>(c);
    c.fresh(); g1_m<1>(c); c.fresh(); g2_m<1>(c);
  } else {
    front_h(c, q, wqkv, wo);
    c.fresh(); g1_h<0>(c); c.fresh(); g2_h<0>(c, out);
    c.fresh(); g1_h<1>(c); c.fresh(); g2_h<1>(c, out);
  }
}

extern "C" void kernel_launch(void* const* d_in, const int* in_sizes, int n_in, void* d_out, int out_size, void* d_ws,
                              size_t ws_size, hipStream_t stream) {
  (void)hipFuncSetAttribute((const void*)fused_kernel, hipFuncAttributeMaxDynamicSharedMemorySize, LDS_BYTES);
  fused_kernel<<<256, 512, LDS_BYTES, stream>>>((const uint16_t*)d_in[0], (const uint16_t*)d_in[1], (const uint16_t*)d_in[2],
                                                (const uint16_t*)d_in[3], (const uint16_t*)d_in[4], (const uint16_t*)d_in[5],
                                                (const uint16_t*)d_in[7], (const uint16_t*)d_in[8], (char*)d_ws, (char*)d_out);
}
```

```cpp
#include <hip/hip_runtime.h>
#include <stdint.h>

typedef __attribute__((ext_vector_type(8))) short bf16x8;
typedef __attribute__((ext_vector_type(4))) short s16x4;
typedef __attribute__((ext_vector_type(4))) float f32x4;
typedef __attribute__((ext_vector_type(2))) float f32x2;
typedef __attribute__((ext_vector_type(16))) float f32x16;
typedef __attribute__((ext_vector_type(4))) unsigned u32x4;
typedef __attribute__((ext_vector_type(2))) unsigned u32x2;
typedef __attribute__((ext_vector_type(2))) __bf16 bf16x2_t;
typedef __amdgpu_buffer_rsrc_t rsrc_t;
#define DI __device__ __forceinline__
#define LAS __attribute__((address_space(3)))
#define STAMPW(kid, slot, wv) do {} while (0)
#define DRAIN() do {} while (0)
#define STAMP(kid, slot) STAMPW(kid, slot, 0)

constexpr int S = 2048, D = 1024;
constexpr unsigned MB = 1024 * 1024;
constexpr unsigned OFF_UB = 0,
                   OFF_X0 = 4 * MB,
                   OFF_W8 = 8 * MB,
                   OFF_WOF = 14 * MB,
                   OFF_Y = 16 * MB,
                   OFF_X1 = 20 * MB,
                   OFF_PQ = 24 * MB,
                   OFF_X3 = 32 * MB,
                   OFF_SM = 36 * MB, WS_USED = 37 * MB;
constexpr int KV_LAYER = 32 * 1024 + 32 * 32;
constexpr unsigned OFF_ST0 = OFF_SM,
                   OFF_CSP = OFF_ST0 + 16384,
                   OFF_KV = OFF_CSP + 16 * 3072 * 4,
                   OFF_GX = OFF_KV + 2 * KV_LAYER * 4,
                   OFF_GX2 = OFF_GX + 65536,
                   OFF_ST3 = OFF_GX2 + 131072,
                   OFF_FLG = OFF_ST3 + 131072,
                   OFF_EPO = OFF_FLG + 2304 * 32;
constexpr int FL_U = 0, FL_Q = 1024, FL_X1 = 1280, FL_G1 = 1536  , FL_X3 = 2048;
constexpr unsigned CODE_BYTES = 56 * 1024;
constexpr int SPIN_MAX = 1 << 17;

DI float bflo(unsigned u) { return __uint_as_float(u << 16); }
DI float bfhi(unsigned u) { return __uint_as_float(u & 0xffff0000u); }
DI unsigned pk2v(f32x2 v) { return __builtin_bit_cast(unsigned, __builtin_convertvector(v, bf16x2_t)); }
DI unsigned pk2(float a, float b) { return pk2v((f32x2){a, b}); }
DI float rbf(float a) { return bflo(pk2(a, a)); }
DI bf16x8 as_frag(u32x4 v) { return __builtin_bit_cast(bf16x8, v); }
DI f32x2 lo2(f32x4 v) { return (f32x2){v[0], v[1]}; }
DI f32x2 hi2(f32x4 v) { return (f32x2){v[2], v[3]}; }
DI float dot2(unsigned a, unsigned b, float c) {
  return __builtin_amdgcn_fdot2_f32_bf16(__builtin_bit_cast(bf16x2_t, a), __builtin_bit_cast(bf16x2_t, b), c, false);
}
#define MFMA16(a, b, c) __builtin_amdgcn_mfma_f32_16x16x32_bf16((a), (b), (c), 0, 0, 0)
#define MFMA32(a, b, c) __builtin_amdgcn_mfma_f32_32x32x16_bf16((a), (b), (c), 0, 0, 0)
#define WAIT_LGKM0() asm volatile("s_waitcnt lgkmcnt(0)" ::: "memory")
#define BAR() do { __builtin_amdgcn_s_barrier(); asm volatile("" ::: "memory"); } while (0)
#define SB() __builtin_amdgcn_sched_barrier(0)
#define KEEP(x) asm volatile("" ::"v"(x))
template <int N> DI void wait_vm() { asm volatile("s_waitcnt vmcnt(%0)" ::"i"(N) : "memory"); }
DI rsrc_t mk_rsrc(void* p, unsigned bytes) { return __builtin_amdgcn_make_buffer_rsrc(p, 0, bytes, 0x00020000); }
DI void glds16(const void* g, char* l) {
  __builtin_amdgcn_global_load_lds((const __attribute__((address_space(1))) void*)g, (LAS void*)l, 16, 0, 2);
}
template <class T> DI T ldnt(const void* p) { return __builtin_nontemporal_load((const T*)p); }
DI void dma16(rsrc_t r, char* l, unsigned voff, unsigned soff) { __builtin_amdgcn_raw_ptr_buffer_load_lds(r, (LAS void*)l, 16, voff, soff, 0, 16); }
DI u32x4 ld16(rsrc_t r, unsigned voff, unsigned soff) { return __builtin_amdgcn_raw_buffer_load_b128(r, voff, soff, 16); }
DI u32x2 ld8(rsrc_t r, unsigned voff, unsigned soff) { return __builtin_amdgcn_raw_buffer_load_b64(r, voff, soff, 16); }
DI void st16(rsrc_t r, unsigned off, u32x4 v) { __builtin_amdgcn_raw_buffer_store_b128(v, r, off, 0, 16); }
DI void st8(rsrc_t r, unsigned off, u32x2 v) { __builtin_amdgcn_raw_buffer_store_b64(v, r, off, 0, 16); }
DI void put8(char* p, u32x2 v) { __hip_atomic_store((unsigned long long*)p, __builtin_bit_cast(unsigned long long, v), __ATOMIC_RELAXED, __HIP_MEMORY_SCOPE_AGENT); }
DI u32x2 poll8(const char* p) { return __builtin_bit_cast(u32x2, __hip_atomic_load((const unsigned long long*)p, __ATOMIC_RELAXED, __HIP_MEMORY_SCOPE_AGENT)); }
DI void publish8(char* p, float sm, float sq) {
  unsigned qb = __float_as_uint(sq);
  if (qb == ~0u) qb = 0x7fc00000u;
  put8(p, (u32x2){__float_as_uint(sm), qb});
}
DI float opq(float m) { asm("" : "+v"(m)); return m; }
template <int N> DI float row_ror(float x) {
  return __int_as_float(__builtin_amdgcn_update_dpp(0, __float_as_int(x), 0x120 + N, 0xf, 0xf, false));
}
DI float swap16_sum(float a, float b) { const auto p = __builtin_amdgcn_permlane16_swap(__float_as_uint(a), __float_as_uint(b), false, false); return __uint_as_float(p[0]) + __uint_as_float(p[1]); }
DI float swap32_sum(float a, float b) { const auto p = __builtin_amdgcn_permlane32_swap(__float_as_uint(a), __float_as_uint(b), false, false); return __uint_as_float(p[0]) + __uint_as_float(p[1]); }

DI f32x2 ln_stats(float s, float q) {
  const float m = s * (1.0f / 1024.0f);
  const float v = fmaxf(q * (1.0f / 1024.0f) - m * m, 0.0f);
  const float sd = rbf(sqrtf(rbf(rbf(v) + 1e-5f)));
  return (f32x2){rbf(m), 1.0f / sd};
}
constexpr float LOG2E = 1.44269504088896f;
DI f32x2 phi2(f32x2 t2) {
  const f32x2 a = t2 * LOG2E - 1e-6f * LOG2E;
  f32x2 e, m;
  e[0] = __builtin_amdgcn_fmed3f(__builtin_amdgcn_exp2f(a[0]), 0.0f, 1.0f);
  e[1] = __builtin_amdgcn_fmed3f(__builtin_amdgcn_exp2f(a[1]), 0.0f, 1.0f);
  m[0] = fmaxf(t2[0], 1e-6f);
  m[1] = fmaxf(t2[1], 1e-6f);
  return m + e;
}

constexpr int LDS_BYTES = 163840, LDS_CNT = LDS_BYTES - 64;
constexpr int CV_PAD = 512, CV_HA = 286 * 16, CV_HB = CV_HA + 4160, CV_CH = CV_HB + 4128 + 32;
constexpr int CV_WB = CV_PAD + 4 * CV_CH, CV_X0 = CV_WB + 4 * 8192, CV_ST = CV_X0 + 16384;
constexpr int G1_AB = 32768, G1_STAGE = G1_AB + 12288, G1_SCR = 3 * G1_STAGE, G1_NP = 11;
constexpr int G2_Z = 131072, G2_RED = G2_Z + 2048;

struct Cx {
  char* ws; char* smem; rsrc_t rw, rc; int lane, w, tid, bid; u32x2 tag;
  DI void half_sync(int n) const {
    const unsigned c = (unsigned)(size_t)(LAS char*)(smem + LDS_CNT + (w >> 2) * 16);
    asm volatile("s_waitcnt lgkmcnt(0)" ::: "memory");
    if (lane == 0) asm volatile("ds_add_u32 %0, %1" ::"v"(c), "v"(1u) : "memory");
    unsigned x;
    do { asm volatile("ds_read_b32 %0, %1\n\ts_waitcnt lgkmcnt(0)" : "=v"(x) : "v"(c) : "memory"); } while (__builtin_amdgcn_readfirstlane(x) < 4u * n);
  }
  DI void signal(int idx) const { if (lane == 0) put8(ws + OFF_FLG + idx * 32, tag); }
  DI void await(int idx, bool pend) const {
    const char* fp = ws + OFF_FLG + idx * 32;
#pragma unroll 1
    for (int spin = 0; spin < SPIN_MAX; ++spin) {
      if (pend) { const u32x2 f = poll8(fp); pend = f[0] != tag[0] || f[1] != tag[1]; }
      if (!__any(pend)) break;
      __builtin_amdgcn_s_sleep(1);
    }
    asm volatile("" ::: "memory");
  }
  DI unsigned touch_code() const {
    unsigned x;
    asm volatile("buffer_load_dword %0, %1, %2, 0 offen" : "=v"(x) : "v"(((bid >> 3) * 16 + (lane & 15)) * 128), "s"(rc) : "memory");
    return x;
  }
  DI void fresh() {
    asm volatile("" : "+v"(tid), "+s"(w), "+s"(bid));
    lane = tid & 63;
  }
  DI void set_tag(u32x2 e) {
    tag = (u32x2){__builtin_amdgcn_readfirstlane(e[0]) * 1664525u + 1013904223u, __builtin_amdgcn_readfirstlane(e[1]) * 22695477u + 1u};
  }
};

DI void conv_store(const Cx& c) {
  const int j = (c.bid & 7) * 32 + (c.bid >> 3), t = c.tid + (c.w >> 2) * 256;
  const char* smem = c.smem;
  u32x4 yo[2];
#pragma unroll
  for (int i = 0; i < 2; ++i) {
    const int p = t + 512 * i;
    unsigned yy[4];
#pragma unroll
    for (int ch = 0; ch < 4; ++ch) yy[ch] = *(const unsigned*)(smem + CV_PAD + ch * CV_CH + CV_HA + p * 4);
    yo[i] = (u32x4){__builtin_amdgcn_perm(yy[1], yy[0], 0x05040100u), __builtin_amdgcn_perm(yy[3], yy[2], 0x05040100u),
                    __builtin_amdgcn_perm(yy[1], yy[0], 0x07060302u), __builtin_amdgcn_perm(yy[3], yy[2], 0x07060302u)};
    const u32x4 x0v = *(const u32x4*)(smem + CV_X0 + p * 16);
    const f32x4 stv = *(const f32x4*)(smem + CV_ST + p * 16);
    u32x4 xo;
#pragma unroll
    for (int e = 0; e < 4; ++e) {
      const float m = stv[e & 2], rs = stv[(e & 2) + 1], xa = bflo(x0v[e]), xb = bfhi(x0v[e]);
      xo[e] = pk2(xa + (xa - m) * rs * bflo(yo[i][e]), xb + (xb - m) * rs * bfhi(yo[i][e]));
    }
    st16(c.rw, OFF_X1 + (j * 1024 + p) * 16, xo);
  }
  SB();
#pragma unroll
  for (int i = 0; i < 2; ++i) st16(c.rw, OFF_Y + (j * 1024 + t + 512 * i) * 16, yo[i]);
  wait_vm<2>();
  WAIT_LGKM0();
  BAR();
}

DI void front_m(Cx& c, const uint16_t* k, const uint16_t* v, const uint16_t* shift_k, const uint16_t* ssm_k, const uint16_t* ssm_D) {
  const int lane = c.lane, w = c.w, tid = c.tid, bid = c.bid, s0 = bid * 8, j = (bid & 7) * 32 + (bid >> 3);
  char* smem = c.smem;
  u32x4* bounce = (u32x4*)(smem + CV_X0) + w * 256;
  u32x4 uu[4], hh[4];
  unsigned hn[4];
  float Dc;
  u32x2 e0;
  {
    u32x2 kk[11], vv[8];
#pragma unroll
    for (int r = 0; r < 11; ++r) kk[r] = ldnt<u32x2>(k + (size_t)((r < 3) ? max(s0 - 3 + r, 0) : s0 - 3 + r) * D + tid * 4);
#pragma unroll
    for (int r = 0; r < 8; ++r) vv[r] = ldnt<u32x2>(v + (size_t)(s0 + r) * D + tid * 4);
    const u32x4 wa = *(const u32x4*)(shift_k + tid * 16), wb = *(const u32x4*)(shift_k + tid * 16 + 8);
    SB();
    const uint16_t* hrow = ssm_k + (size_t)(4 * j + w) * S;
#pragma unroll
    for (int i = 0; i < 4; ++i) {
      const int ch = i * 64 + lane;
      hh[i] = ldnt<u32x4>(hrow + ch * 8);
      hn[i] = ldnt<unsigned>(hrow + min(ch * 8 + 8, S - 2));
    }
    Dc = bflo((unsigned)ssm_D[4 * j + w]);
    e0 = poll8(c.ws + OFF_EPO);
    SB();
    if (tid < 2) *(unsigned*)(smem + LDS_CNT + tid * 16) = 0u;
    WAIT_LGKM0();
    BAR();
    SB();
    const unsigned hm = bid ? ~0u : 0u;
#pragma unroll
    for (int r = 0; r < 3; ++r) { kk[r][0] &= hm; kk[r][1] &= hm; }
    const int sw = (lane >> 1) & 3;
#pragma unroll
    for (int ch = 0; ch < 4; ++ch) {
      const unsigned w01 = (ch == 0) ? wa[0] : (ch == 1) ? wa[2] : (ch == 2) ? wb[0] : wb[2];
      const unsigned w23 = (ch == 0) ? wa[1] : (ch == 1) ? wa[3] : (ch == 2) ? wb[1] : wb[3];
      const float w0 = bflo(w01), w1 = bfhi(w01), w2 = bflo(w23), w3 = bfhi(w23);
      float kf[11], u[8];
#pragma unroll
      for (int r = 0; r < 11; ++r) kf[r] = (ch & 1) ? bfhi(kk[r][ch >> 1]) : bflo(kk[r][ch >> 1]);
#pragma unroll
      for (int r = 0; r < 8; ++r) {
        const float ks = kf[r] * w3 + kf[r + 1] * w2 + kf[r + 2] * w1 + kf[r + 3] * w0;
        u[r] = ks * ((ch & 1) ? bfhi(vv[r][ch >> 1]) : bflo(vv[r][ch >> 1]));
      }
      bounce[lane * 4 + (ch ^ sw)] = (u32x4){pk2(u[7], u[6]), pk2(u[5], u[4]), pk2(u[3], u[2]), pk2(u[1], u[0])};
    }
#pragma unroll
    for (int i = 0; i < 4; ++i) {
      const int u = 64 * i + lane;
      st16(c.rw, OFF_UB + (bid * 1024 + w * 256 + u) * 16, bounce[(u & ~3) | ((u & 3) ^ ((u >> 3) & 3))]);
    }
    STAMP(0, 2);
    SB();
  }
  char* my = smem + CV_PAD + w * CV_CH;
  if (lane < 30) *(u32x4*)(my + lane * 16) = (u32x4){0u, 0u, 0u, 0u};
  if (lane >= 32 && lane < 34) *(u32x4*)(my + CV_HA + (lane - 32) * 16) = (u32x4){0u, 0u, 0u, 0u};
  if (lane == 34) *(u32x4*)(my + CV_HB) = (u32x4){0u, 0u, 0u, 0u};
  if (lane == 0) *(u32x4*)(my + CV_HB + 16) = (u32x4){0u, 0u, 0u, hh[0][0] << 16};
#pragma unroll
  for (int i = 0; i < 4; ++i) {
    const int ch = i * 64 + lane;
    *(u32x4*)(my + CV_HA + 32 + ch * 16) = hh[i];
    *(u32x4*)(my + CV_HB + 32 + ch * 16) =
        (u32x4){__builtin_amdgcn_alignbyte(hh[i][1], hh[i][0], 2), __builtin_amdgcn_alignbyte(hh[i][2], hh[i][1], 2),
                __builtin_amdgcn_alignbyte(hh[i][3], hh[i][2], 2), __builtin_amdgcn_alignbyte(hn[i], hh[i][3], 2)};
  }
  asm volatile("" : "+v"(Dc), "+v"(e0));
  c.set_tag(e0);
  wait_vm<0>();
  c.signal(FL_U + w * 256 + bid);
  STAMP(0, 3);
  c.await(FL_U + (j >> 6) * 256 + w * 64 + lane, true);
  STAMP(0, 4);
#pragma unroll
  for (int i = 0; i < 4; ++i) uu[i] = ld16(c.rw, ((i * 16 + (lane >> 2)) * 1024 + (lane & 3)) * 16, OFF_UB + (w * 65536 + 4 * j) * 16);
#pragma unroll
  for (int i = 0; i < 4; ++i) *(u32x4*)(smem + CV_PAD + (lane & 3) * CV_CH + (30 + w * 64 + i * 16 + (lane >> 2)) * 16) = uu[i];
  const int t0 = lane & 15, kq = lane >> 4;
  const LAS unsigned* fbk[4];
#pragma unroll
  for (int i = 0; i < 4; ++i) {
    fbk[i] = (const LAS unsigned*)(my + ((t0 & 1) ? CV_HA : CV_HB)) + 4 * kq + ((1 + t0) >> 1) + 256 * i;
    asm volatile("" : "+v"(fbk[i]));
  }
  const char* gb = my + (2 * t0 + 3 - kq) * 16;
  f32x4 acc[8];
#pragma unroll
  for (int b = 0; b < 8; ++b) { acc[b] = (f32x4){0.f, 0.f, 0.f, 0.f}; asm volatile("" : "+v"(acc[b])); }
  WAIT_LGKM0();
  BAR();
  STAMP(1, 0); STAMP(1, 1);
  bf16x8 F[2][8], G[2][8];
  u32x2 fh[8];
  auto rdn = [&](int buf, int g0, int n) {
    if (n == 0 || n > 16) { const int g1 = n ? n - 16 : 0; G[buf][g1] = *(const bf16x8*)(gb + (32 * g1 + 4 * (7 - g0)) * 16); }
    else {
      const int e1 = (n - 1) >> 1;
      const LAS unsigned* p = fbk[e1 >> 1] + 128 * (e1 & 1) + 16 * g0;
      if ((n - 1) & 1) F[buf][e1] = as_frag((u32x4){fh[e1][0], fh[e1][1], p[2], p[3]});
      else fh[e1] = (u32x2){p[0], p[1]};
    }
  };
  auto cstep = [&](int cb, int g0n) {
    int n = 0;
#pragma unroll
    for (int g1 = 0; g1 < 8; ++g1)
#pragma unroll
      for (int e1 = 0; e1 + g1 < 8; ++e1) {
        acc[g1 + e1] = MFMA16(F[cb][e1], G[cb][g1], acc[g1 + e1]);
        SB();
        if (n < 24) { rdn(cb ^ 1, g0n, n); SB(); }
        ++n;
      }
  };
#pragma unroll
  for (int n = 0; n < 24; ++n) rdn(0, 0, n);
  for (int gp = 0; gp < 4; ++gp) {
    cstep(0, 1);
    cstep(1, 2);
    gb -= 128;
#pragma unroll
    for (int i = 0; i < 4; ++i) fbk[i] += 32;
  }
  asm volatile("" ::"v"(acc[7]), "v"(acc[0]));
  STAMP(1, 2);
#pragma unroll
  for (int b = 0; b < 8; ++b) {
    const int s = 256 * b + 16 * t0 + 4 * kq;
    const u32x2 ur = *(const u32x2*)(my + (30 + (s >> 3)) * 16 + ((s & 4) ? 0 : 8));
    *(u32x2*)(my + CV_HA + s * 2) = (u32x2){pk2(acc[b][0] + bfhi(ur[1]) * Dc, acc[b][1] + bflo(ur[1]) * Dc),
                                            pk2(acc[b][2] + bfhi(ur[0]) * Dc, acc[b][3] + bflo(ur[0]) * Dc)};
  }
  WAIT_LGKM0();
  BAR();
  STAMP(1, 3);
  conv_store(c);
  STAMP(1, 6);
  if (w == 0) c.signal(FL_X1 + bid);
  const unsigned pf = c.touch_code();
  c.await(FL_X1 + w * 64 + lane, true);
  KEEP(pf);
  STAMP(1, 7);
  BAR();
  if (bid == 0 && tid == 0) put8(c.ws + OFF_EPO, c.tag);
}

DI void front_h(Cx& c, const uint16_t* q, const uint16_t* wqkv, const uint16_t* wo) {
  const int lane = c.lane, w4 = c.w - 4, tid = c.tid, bid = c.bid, s0 = bid * 8, j = (bid & 7) * 32 + (bid >> 3);
  char* smem = c.smem;
  const rsrc_t rw = c.rw;
  u32x4* bounce = (u32x4*)(smem + CV_X0) + c.w * 256;
  float* red = (float*)smem;
  u32x2 qq[8];
#pragma unroll
  for (int r = 0; r < 8; ++r) qq[r] = ldnt<u32x2>(q + (size_t)(s0 + r) * D + tid * 4);
  SB();
  u32x2 e0 = poll8(c.ws + OFF_EPO);
  const unsigned pf = c.touch_code();
  SB();
  BAR();
  const int sw = (lane >> 1) & 3;
#pragma unroll
  for (int r = 0; r < 4; ++r) bounce[lane * 4 + (r ^ sw)] = (u32x4){qq[2 * r][0], qq[2 * r][1], qq[2 * r + 1][0], qq[2 * r + 1][1]};
#pragma unroll
  for (int i = 0; i < 4; ++i) {
    const int u = 64 * i + lane;
    st16(rw, OFF_X0 + (bid * 1024 + w4 * 256 + u) * 16, bounce[(u & ~3) | ((u & 3) ^ ((u >> 3) & 3))]);
  }
  if (tid < 48) st16(rw, OFF_GX + bid * 768 + tid * 16, (u32x4){~0u, ~0u, ~0u, ~0u});
  float a[16];
#pragma unroll
  for (int r = 0; r < 8; ++r) {
    a[2 * r] = dot2(qq[r][0], 0x3f803f80u, dot2(qq[r][1], 0x3f803f80u, 0.f));
    a[2 * r + 1] = dot2(qq[r][0], qq[r][0], dot2(qq[r][1], qq[r][1], 0.f));
  }
#pragma unroll
  for (int i = 0; i < 8; ++i) a[i] = swap32_sum(a[i], a[i + 8]);
#pragma unroll
  for (int i = 0; i < 4; ++i) a[i] = swap16_sum(a[i], a[i + 4]);
#pragma unroll
  for (int i = 0; i < 4; ++i) { a[i] += row_ror<1>(a[i]); a[i] += row_ror<2>(a[i]); a[i] += row_ror<4>(a[i]); a[i] += row_ror<8>(a[i]); }
  if ((lane & 15) == 0) *(f32x4*)&red[w4 * 16 + (lane >> 4) * 4] = (f32x4){a[0], a[1], a[2], a[3]};
  asm volatile("" : "+v"(e0));
  c.set_tag(e0);
  wait_vm<0>();
  KEEP(pf);
  c.half_sync(1);
  if (w4 == 0) {
    if (lane < 8) {
      const float sm = (red[2 * lane] + red[16 + 2 * lane]) + (red[32 + 2 * lane] + red[48 + 2 * lane]);
      const float sq = (red[2 * lane + 1] + red[17 + 2 * lane]) + (red[33 + 2 * lane] + red[49 + 2 * lane]);
      st8(rw, OFF_ST0 + (s0 + lane) * 8, __builtin_bit_cast(u32x2, ln_stats(sm, sq)));
    }
    wait_vm<0>();
    c.signal(FL_Q + bid);
  }
  STAMPW(0, 5, 4);
  const int gw = bid * 4 + w4;
  char* buf = smem + CV_WB + w4 * 8192;
  const int g = lane >> 4, qr = (lane >> 2) & 3, p = lane & 3, rr = lane >> 3;
  const bool isw = gw < 768;
  const int cg = isw ? gw % 48 : (gw - 768) & 15, og = isw ? gw / 48 : (gw - 768) >> 4;
  if (isw) {
    const uint16_t* src = wqkv + (size_t)(og * 64 + rr) * 3072 + cg * 64 + (lane & 7) * 8;
#pragma unroll
    for (int i = 0; i < 8; ++i) glds16(src + (size_t)i * 8 * 3072, buf + i * 1024);
  } else {
    const uint16_t* src = wo + (size_t)(og * 64 + (rr & 3) + 16 * (rr >> 2)) * 1024 + cg * 64 + (lane & 7) * 8;
#pragma unroll
    for (int i = 0; i < 8; ++i) glds16(src + (size_t)((i >> 2) * 32 + (i & 3) * 4) * 1024, buf + i * 1024);
  }
  BAR();
  c.await(FL_Q + w4 * 64 + lane, true);
  const int hd = (cg & 15) * 2 + (lane >> 5), col = (cg >> 4) * 32 + (lane & 31);
  const unsigned dst = isw ? OFF_W8 + ((hd * 128 + og * 8) * 96 + col) * 16
                           : OFF_WOF + ((((og >> 1) * 32 + cg * 2 + (g >> 1)) * 8 + (og & 1) * 4) * 64 + (lane & 31)) * 16;
  const unsigned dstep = isw ? 1536 : 512;
  const unsigned la = (unsigned)(size_t)(LAS char*)buf + qr * 128 + (isw ? g * 32 + p * 8 : (g >> 1) * 64 + (p & 1) * 32 + ((g & 1) * 2 + (p >> 1)) * 8);
  wait_vm<0>();
  u32x2 lo[8], hi[8];
#pragma unroll
  for (int i = 0; i < 8; ++i) {
    lo[i] = __builtin_bit_cast(u32x2, __builtin_amdgcn_ds_read_tr16_b64_v4i16((LAS s16x4*)(size_t)(la + i * 1024)));
    hi[i] = __builtin_bit_cast(u32x2, __builtin_amdgcn_ds_read_tr16_b64_v4i16((LAS s16x4*)(size_t)(la + i * 1024 + 512)));
  }
  SB();
#pragma unroll
  for (int i = 0; i < 4; ++i) {
    const int pp = w4 * 256 + i * 64 + lane;
    dma16(rw, smem + CV_X0 + (w4 * 4 + i) * 1024, ((pp >> 2) * 1024 + (pp & 3)) * 16, OFF_X0 + j * 64);
    dma16(rw, smem + CV_ST + (w4 * 4 + i) * 1024, pp * 16, OFF_ST0);
  }
  SB();
#pragma unroll
  for (int i = 0; i < 8; ++i) st16(rw, dst + i * dstep, (u32x4){lo[i][0], lo[i][1], hi[i][0], hi[i][1]});
  if (lane < 16 + 1 - (gw & 1)) st16(rw, OFF_KV + (gw * 33 / 2 + (gw & 1) + lane) * 16, (u32x4){0u, 0u, 0u, 0u});
  if (isw) {
    float c0 = 0.f, c1 = 0.f;
#pragma unroll
    for (int i = 0; i < 8; ++i)
#pragma unroll
      for (int e = 0; e < 2; ++e) {
        c0 = opq(opq(c0 + bflo(lo[i][e])) + bflo(hi[i][e]));
        c1 = opq(opq(c1 + bfhi(lo[i][e])) + bfhi(hi[i][e]));
      }
    __builtin_amdgcn_raw_buffer_store_b32(__float_as_uint(c0 + c1), rw, OFF_CSP + ((hd * 96 + col) * 16 + og) * 4, 0, 16);
  }
  STAMPW(1, 4, 4);
  wait_vm<9>();
  BAR();
  conv_store(c);
  BAR();
}

template <int L> DI void g1_m(const Cx& c) {
  constexpr bool C4 = L == 0;
  constexpr int kid = 2 + 2 * L;
  const int lane = c.lane, w = c.w, tid = c.tid, xcd = c.bid & 7, idx = c.bid >> 3;
  const int band = (xcd & 1) * 4 + (idx & 3), h = (xcd >> 1) * 8 + (idx >> 2), r = lane & 15, kq = lane >> 4;
  char* smem = c.smem;
  const float* Rs = (const float*)(smem + G1_SCR);
  const float* MRs = Rs + 256;
  const float* CSs = Rs + 512;
  float* P = (float*)(smem + G1_SCR) + 640;
  const char* ab = C4 ? smem + kq * 4096 + ((64 * w + r) ^ (16 * (kq & 1))) * 8 : smem + kq * 4096 + (64 * w + r) * 16;
  const char* ab2 = ab + 128 - (kq & 1) * 256;
  const char* bb = smem + G1_AB + kq * 1536 + r * 16;
  STAMP(kid, 0);
  f32x4 acc[4][6];
#pragma unroll
  for (int m = 0; m < 4; ++m)
#pragma unroll
    for (int p = 0; p < 6; ++p) { acc[m][p] = (f32x4){0.f, 0.f, 0.f, 0.f}; asm volatile("" : "+v"(acc[m][p])); }
  bf16x8 F[2][10];
  auto rd1 = [&](int buf, const char* a, const char* a2, const char* b, int t, int i) {
    if (i < 4) {
      if (C4) {
        const char* p = ((i & 1) ? a2 : a) + t * 16384 + (i >> 1) * 256;
        const u32x2 lo = *(const u32x2*)p, hi = *(const u32x2*)(p + 2048);
        F[buf][i] = as_frag((u32x4){lo[0], lo[1], hi[0], hi[1]});
      } else F[buf][i] = *(const bf16x8*)(a + t * 16384 + i * 256);
    } else F[buf][i] = *(const bf16x8*)(b + t * 6144 + (i - 4) * 256);
  };
  auto step = [&](int cb, const char* a, const char* a2, const char* b, int t, bool rd) {
    const int ord[10] = {0, 4, 5, 6, 7, 8, 9, 1, 2, 3};
#pragma unroll
    for (int i = 0; i < 24; ++i) {
      const int m = i / 6, p = i % 6;
      if (p < 2) acc[m][p] = MFMA16(F[cb][4 + p], F[cb][m], acc[m][p]);
      else acc[m][p] = MFMA16(F[cb][m], F[cb][4 + p], acc[m][p]);
      SB();
      if (rd && (i & 1) && (i >> 1) < 10) { rd1(cb ^ 1, a, a2, b, t, ord[i >> 1]); SB(); }
    }
  };
  BAR();
  STAMP(kid, 1);
#pragma unroll
  for (int i = 0; i < 10; ++i) rd1(0, ab, ab2, bb, 0, i);
  int so = 0;
  for (int it = 0; it < 15; ++it) {
    step(0, ab + so, ab2 + so, bb + so, 1, true);
    WAIT_LGKM0();
    BAR();
    so = (so == 2 * G1_STAGE) ? 0 : so + G1_STAGE;
    step(1, ab + so, ab2 + so, bb + so, 0, true);
  }
  step(0, ab + so, ab2 + so, bb + so, 1, true);
  BAR();
  step(1, ab, ab2, bb, 0, false);
  STAMP(kid, 2);
  f32x4 Z[2][2], Zz[2];
#pragma unroll
  for (int i = 0; i < 2; ++i) { Z[i][0] = (f32x4){0.f, 0.f, 0.f, 0.f}; Z[i][1] = Z[i][0]; Zz[i] = Z[i][0]; }
  {
    float ck[4];
#pragma unroll
    for (int p = 0; p < 4; ++p) ck[p] = CSs[32 + p * 16 + r];
    const bf16x8 ones = as_frag((u32x4){0x3f803f80u, 0x3f803f80u, 0x3f803f80u, 0x3f803f80u});
#pragma unroll
    for (int jj = 0; jj < 2; ++jj) {
      u32x4 fr[4];
#pragma unroll
      for (int mm = 0; mm < 2; ++mm) {
        const int m = 2 * jj + mm;
        const f32x4 rs = *(const f32x4*)(Rs + 64 * w + 16 * m + 4 * kq), mr = *(const f32x4*)(MRs + 64 * w + 16 * m + 4 * kq);
#pragma unroll
        for (int p = 0; p < 2; ++p) {
          fr[p][2 * mm] = pk2v(phi2(lo2(acc[m][2 + p]) * lo2(rs) + (lo2(mr) * ck[p] + 1e-6f)));
          fr[p][2 * mm + 1] = pk2v(phi2(hi2(acc[m][2 + p]) * hi2(rs) + (hi2(mr) * ck[p] + 1e-6f)));
          fr[2 + p][2 * mm] = pk2v(lo2(acc[m][4 + p]) * lo2(rs) + lo2(mr) * ck[2 + p]);
          fr[2 + p][2 * mm + 1] = pk2v(hi2(acc[m][4 + p]) * hi2(rs) + hi2(mr) * ck[2 + p]);
        }
      }
#pragma unroll
      for (int dh = 0; dh < 2; ++dh) {
        Z[0][dh] = MFMA16(as_frag(fr[dh]), as_frag(fr[2]), Z[0][dh]);
        Z[1][dh] = MFMA16(as_frag(fr[dh]), as_frag(fr[3]), Z[1][dh]);
        Zz[dh] = MFMA16(as_frag(fr[dh]), ones, Zz[dh]);
      }
    }
  }
  {
    float* mine = P + w * 1056;
#pragma unroll
    for (int i = 0; i < 4; ++i) *(f32x4*)(mine + i * 256 + lane * 4) = Z[i >> 1][i & 1];
    if (r == 0) { *(f32x4*)(mine + 1024 + 4 * kq) = Zz[0]; *(f32x4*)(mine + 1040 + 4 * kq) = Zz[1]; }
  }
  c.half_sync(2 * L + 1);
  STAMP(kid, 3);
  float* kvacc = (float*)(c.ws + OFF_KV) + L * KV_LAYER;
#pragma unroll
  for (int i = 0; i < 4; ++i) {
    const int e = tid + 256 * i;
    atomicAdd(kvacc + h * 1024 + e, (P[e] + P[1056 + e]) + (P[2112 + e] + P[3168 + e]));
  }
  if (tid < 32) atomicAdd(kvacc + 32768 + h * 32 + tid, (P[1024 + tid] + P[2080 + tid]) + (P[3136 + tid] + P[4192 + tid]));
  SB();
  {
    const f32x4 c0 = *(const f32x4*)(CSs + 4 * kq), c1 = *(const f32x4*)(CSs + 16 + 4 * kq);
#pragma unroll
    for (int m = 0; m < 4; ++m) {
      const float rs = Rs[64 * w + 16 * m + r], mr = MRs[64 * w + 16 * m + r];
      const u32x4 o = {pk2v(phi2(lo2(acc[m][0]) * rs + (lo2(c0) * mr + 1e-6f))), pk2v(phi2(hi2(acc[m][0]) * rs + (hi2(c0) * mr + 1e-6f))),
                       pk2v(phi2(lo2(acc[m][1]) * rs + (lo2(c1) * mr + 1e-6f))), pk2v(phi2(hi2(acc[m][1]) * rs + (hi2(c1) * mr + 1e-6f)))};
      st16(c.rw, OFF_PQ + L * 4 * MB + ((((h >> 2) * 128 + band * 16 + w * 4 + m) * 4 + (h & 3)) * 64 + lane) * 16, o);
    }
  }
  STAMP(kid, 6);
  wait_vm<0>();
  c.half_sync(2 * L + 2);
  STAMP(kid, 7);
  if (w == 0) c.signal(FL_G1 + L * 256 + h * 8 + band);
}

template <int L> DI void g1_h(const Cx& c) {
  constexpr bool C4 = L == 0;
  constexpr unsigned OFF_X = C4 ? OFF_X1 : OFF_X3;
  const int lane = c.lane, lw = c.w - 4, t = c.tid, xcd = c.bid & 7, idx = c.bid >> 3;
  const int band = (xcd & 1) * 4 + (idx & 3), h = (xcd >> 1) * 8 + (idx >> 2);
  char* smem = c.smem;
  char* ws = c.ws;
  const rsrc_t rw = c.rw;
  float* Rs = (float*)(smem + G1_SCR);
  const unsigned l0 = lane * 16, l1 = (lane ^ 8) * 16;
  auto issue = [&](int st) {
    char* base = smem + (st % 3) * G1_STAGE;
#pragma unroll
    for (int p = 0; p < 8; ++p) {
      if (C4) dma16(rw, base + (8 * lw + p) * 1024, (p & 4) ? l1 : l0, OFF_X + ((st * 16 + lw * 4 + (p >> 1)) * 1024 + band * 128 + (p & 1) * 64) * 16);
      else dma16(rw, base + (8 * lw + p) * 1024, l0, OFF_X + ((st * 8 + lw * 2 + (p >> 2)) * S + band * 256 + (p & 3) * 64) * 16);
    }
#pragma unroll
    for (int p = 0; p < 3; ++p) dma16(rw, base + G1_AB + (3 * lw + p) * 1024, l0, OFF_W8 + (h * 128 + st * 8) * 1536 + (3 * lw + p) * 1024);
  };
  issue(0); issue(1);
  wait_vm<G1_NP>();
  BAR();
  issue(2);
  u32x4 xv[4];
  float v[4] = {0.f, 0.f, 0.f, 0.f};
  if (C4) {
    const u32x4* xp = (const u32x4*)(ws + OFF_X) + (size_t)(t >> 2) * 1024 + band * 128 + h * 4 + (t & 3);
    asm volatile("global_load_dwordx4 %0, %4, off sc1\n\tglobal_load_dwordx4 %1, %5, off sc1\n\tglobal_load_dwordx4 %2, %6, off sc1\n\tglobal_load_dwordx4 %3, %7, off sc1"
                 : "=&v"(xv[0]), "=&v"(xv[1]), "=&v"(xv[2]), "=&v"(xv[3]) : "v"(xp), "v"(xp + 65536), "v"(xp + 131072), "v"(xp + 196608) : "memory");
  }
#pragma unroll
  for (int i = 0; i < 13; ++i) {
    if (C4 && i < 2) wait_vm<G1_NP + 4>();
    else if (C4 && i == 2) asm volatile("s_waitcnt vmcnt(%4)" : "+v"(xv[0]), "+v"(xv[1]), "+v"(xv[2]), "+v"(xv[3]) : "i"(G1_NP) : "memory");
    else wait_vm<G1_NP>();
    BAR();
    issue(i + 3);
    if (C4 && i >= 2 && i < 6) {
      const u32x4 x = xv[i - 2];
#pragma unroll
      for (int e = 0; e < 4; ++e) {
        const float lo = bflo(x[e]), hi = bfhi(x[e]);
        v[e & 2] = opq(v[e & 2] + lo); v[(e & 2) + 1] = opq(fmaf(lo, lo, v[(e & 2) + 1]));
        v[e & 2] = opq(v[e & 2] + hi); v[(e & 2) + 1] = opq(fmaf(hi, hi, v[(e & 2) + 1]));
      }
      asm volatile("" : "+v"(v[0]), "+v"(v[1]), "+v"(v[2]), "+v"(v[3]));
    }
    if (C4 && i == 6) {
#pragma unroll
      for (int e = 0; e < 4; ++e) { v[e] += row_ror<4>(v[e]); v[e] += row_ror<8>(v[e]); v[e] = swap16_sum(v[e], v[e]); v[e] = swap32_sum(v[e], v[e]); }
      const bool up = (lane & 4) != 0;
      if (lane < 8) publish8(ws + OFF_GX + ((band * 4 + lw) * 256 + h * 8 + 2 * (lane & 3) + (lane >> 2)) * 8, up ? v[2] : v[0], up ? v[3] : v[1]);
    }
  }
  const char* gp = ws + (C4 ? OFF_GX + (band * 1024 + t) * 8 + 4096 : OFF_ST3 + ((band * 4 + lw) * 512 + lane) * 8);
  const char* cp = ws + OFF_CSP + (h * 96 + (t < 96 ? t : 0)) * 64;
  u32x2 g[8];
  f32x4 c0, c1, c2, c3;
  asm volatile("global_load_dwordx4 %0, %4, off sc1\n\tglobal_load_dwordx4 %1, %4, off offset:16 sc1\n\t"
               "global_load_dwordx4 %2, %4, off offset:32 sc1\n\tglobal_load_dwordx4 %3, %4, off offset:48 sc1"
               : "=&v"(c0), "=&v"(c1), "=&v"(c2), "=&v"(c3) : "v"(cp) : "memory");
  if (C4) {
    asm volatile("global_load_dwordx2 %0, %4, off offset:-4096 sc1\n\tglobal_load_dwordx2 %1, %4, off offset:-2048 sc1\n\t"
                 "global_load_dwordx2 %2, %4, off sc1\n\tglobal_load_dwordx2 %3, %4, off offset:2048 sc1"
                 : "=&v"(g[0]), "=&v"(g[1]), "=&v"(g[2]), "=&v"(g[3]) : "v"(gp) : "memory");
    wait_vm<G1_NP + 8>();
  } else {
    asm volatile("global_load_dwordx2 %0, %8, off sc1\n\tglobal_load_dwordx2 %1, %8, off offset:512 sc1\n\tglobal_load_dwordx2 %2, %8, off offset:1024 sc1\n\t"
                 "global_load_dwordx2 %3, %8, off offset:1536 sc1\n\tglobal_load_dwordx2 %4, %8, off offset:2048 sc1\n\tglobal_load_dwordx2 %5, %8, off offset:2560 sc1\n\t"
                 "global_load_dwordx2 %6, %8, off offset:3072 sc1\n\tglobal_load_dwordx2 %7, %8, off offset:3584 sc1"
                 : "=&v"(g[0]), "=&v"(g[1]), "=&v"(g[2]), "=&v"(g[3]), "=&v"(g[4]), "=&v"(g[5]), "=&v"(g[6]), "=&v"(g[7]) : "v"(gp) : "memory");
    wait_vm<G1_NP + 12>();
  }
  BAR();
  if (C4) asm volatile("s_waitcnt vmcnt(0)" : "+v"(g[0]), "+v"(g[1]), "+v"(g[2]), "+v"(g[3]), "+v"(c0), "+v"(c1), "+v"(c2), "+v"(c3)::"memory");
  else asm volatile("s_waitcnt vmcnt(0)" : "+v"(g[0]), "+v"(g[1]), "+v"(g[2]), "+v"(g[3]), "+v"(g[4]), "+v"(g[5]), "+v"(g[6]), "+v"(g[7]),
                    "+v"(c0), "+v"(c1), "+v"(c2), "+v"(c3)::"memory");
  BAR();
  if (C4) {
#pragma unroll 1
    for (int spin = 0; spin < SPIN_MAX; ++spin) {
      if (!__any(g[0][1] == ~0u || g[1][1] == ~0u || g[2][1] == ~0u || g[3][1] == ~0u)) break;
      __builtin_amdgcn_s_sleep(4);
#pragma unroll
      for (int k = 0; k < 4; ++k) g[k] = poll8(gp + (k - 2) * 2048);
    }
  }
  float sm = 0.f, sq = 0.f, cs = 0.f;
#pragma unroll
  for (int k = 0; k < (C4 ? 4 : 8); ++k) { sm = opq(sm + __uint_as_float(g[k][0])); sq = opq(sq + __uint_as_float(g[k][1])); }
#pragma unroll
  for (int k = 0; k < 4; ++k) cs = opq(opq(opq(opq(cs + c0[k]) + c1[k]) + c2[k]) + c3[k]);
  const f32x2 st = ln_stats(sm, sq);
  Rs[t] = st[1]; Rs[256 + t] = -st[0] * st[1];
  if (t < 96) Rs[512 + t] = cs;
  WAIT_LGKM0();
  BAR();
}

template <int L> DI void g2_m(const Cx& c) {
  constexpr int kid = 3 + 2 * L;
  constexpr unsigned KVO = OFF_KV + L * KV_LAYER * 4;
  const int lane = c.lane, w = c.w, xcd = c.bid & 7, idx = c.bid >> 3, sb = (xcd >> 1) * 8 + (idx & 7), kq = lane >> 4;
  const rsrc_t rw = c.rw;
  const unsigned l16 = lane * 16;
  u32x4* O = (u32x4*)c.smem;
  unsigned pf = c.touch_code();
  c.await(FL_G1 + L * 256 + ((lane >> 3) * 4 + w) * 8 + (lane & 7), true);
  KEEP(pf);
  STAMP(kid, 0);
  constexpr int PL = 2, PD = PL + 1;
  u32x4 pq[PD][4];
  f32x4 kv[PD][4];
  const unsigned spq = OFF_PQ + L * 4 * MB + (sb * 16 + w) * 1024, skv = KVO + w * 4096;
  auto ld = [&](int i, int n) {
    if (n < 4) kv[i % PD][n] = __builtin_bit_cast(f32x4, ld16(rw, l16 + n * 1024, skv + i * 16384));
    else pq[i % PD][n - 4] = ld16(rw, l16, spq + (i * 512 + (n - 4) * 4) * 1024);
    SB();
  };
  const f32x4 zall = __builtin_bit_cast(f32x4, ld16(rw, (lane >> 3) * 512 + (lane & 7) * 16, KVO + 131072 + w * 128));
  SB();
#pragma unroll
  for (int n = 0; n < 8 * PL; ++n) ld(n >> 3, n & 7);
  u32x2* Zs = (u32x2*)(c.smem + G2_Z) + w * 64;
  Zs[lane] = (u32x2){pk2v(lo2(zall)), pk2v(hi2(zall))};
  const f32x4 zero = {0.f, 0.f, 0.f, 0.f}, eps = {1e-6f, 1e-6f, 1e-6f, 1e-6f};
  bf16x8 k0, k1, zb;
  auto prep = [&](int i) {
    const int b = i % PD;
    const u32x2 za = Zs[i * 8 + kq], zc = Zs[i * 8 + 4 + kq];
    k0 = as_frag((u32x4){pk2v(lo2(kv[b][0])), pk2v(hi2(kv[b][0])), pk2v(lo2(kv[b][1])), pk2v(hi2(kv[b][1]))});
    k1 = as_frag((u32x4){pk2v(lo2(kv[b][2])), pk2v(hi2(kv[b][2])), pk2v(lo2(kv[b][3])), pk2v(hi2(kv[b][3]))});
    zb = as_frag((u32x4){za[0], za[1], zc[0], zc[1]});
    asm volatile("" : "+v"(k0), "+v"(k1), "+v"(zb));
  };
  prep(0);
#pragma unroll
  for (int i = 0; i < 8; ++i) {
    const int b = i % PD;
    f32x4 x0[4], x1[4], dn[4];
    __builtin_amdgcn_s_setprio(2);
#pragma unroll
    for (int st = 0; st < 4; ++st) {
      dn[st] = MFMA16(zb, as_frag(pq[b][st]), eps);
      x0[st] = MFMA16(k0, as_frag(pq[b][st]), zero);
      x1[st] = MFMA16(k1, as_frag(pq[b][st]), zero);
    }
    __builtin_amdgcn_s_setprio(0);
    SB();
#pragma unroll
    for (int st = 0; st < 4; ++st) {
      const float inv = __builtin_amdgcn_rcpf(dn[st][0]);
      O[((i * 4 + w) * 4 + st) * 64 + lane] =
          (u32x4){pk2(opq(x0[st][0] * inv), opq(x0[st][1] * inv)), pk2(opq(x0[st][2] * inv), opq(x0[st][3] * inv)),
                  pk2(opq(x1[st][0] * inv), opq(x1[st][1] * inv)), pk2(opq(x1[st][2] * inv), opq(x1[st][3] * inv))};
      SB();
      if (i + PL < 8) { ld(i + PL, 2 * st); ld(i + PL, 2 * st + 1); }
    }
    if (i < 7) prep(i + 1);
    WAIT_LGKM0();
    BAR();
    if (i == 0) STAMP(kid, 1);
  }
  if (L == 0) {
    const int band = (xcd & 1) * 4 + (idx & 3);
    pf = c.touch_code();
    if (w == 0) c.await(FL_X3 + band * 32 + (lane & 31), lane < 32);
    wait_vm<0>();
    KEEP(pf);
    STAMP(kid, 5);
    BAR();
  }
}

template <int L> DI void g2_h(const Cx& cx, char* out) {
  constexpr int kid = 3 + 2 * L;
  const int lane = cx.lane, c = cx.w - 4, xcd = cx.bid & 7, idx = cx.bid >> 3;
  const int sb = (xcd >> 1) * 8 + (idx & 7), nb = (xcd & 1) * 4 + (idx >> 3);
  const int n0 = nb * 128 + c * 32, s0 = sb * 64, r = lane & 31, hf = lane >> 5;
  const rsrc_t rw = cx.rw;
  const unsigned l16 = lane * 16;
  constexpr int CD = 5, LA = 6;
  u32x4 wo[CD][8], of[8];
  const unsigned swo = OFF_WOF + (nb * 4 + c) * 8192;
  auto ldw = [&](int i, int n) { wo[i % CD][n] = ld16(rw, l16 + (n & 3) * 1024, swo + i * 262144 + (n >> 2) * 4096); SB(); };
#pragma unroll
  for (int n = 0; n < 8 * CD; ++n) ldw(n >> 3, n & 7);
  f32x16 acc[2];
#pragma unroll
  for (int i = 0; i < 16; ++i) { acc[0][i] = 0.f; acc[1][i] = 0.f; }
  asm volatile("" : "+v"(acc[0]), "+v"(acc[1]));
  u32x4 res[2][2], yv[2][2];
  auto ldres = [&](int k) {
    const int s2 = (k >> 1) & 1, g = k & 1;
    if (L == 1) { if (k < 4) res[s2][g] = ld16(rw, (2 * hf * S + r) * 16 + s2 * 512, OFF_X3 + (((n0 >> 3) + g) * S + s0) * 16); }
    else {
      const unsigned so = (k < 4 ? OFF_X1 : OFF_Y) + (((n0 >> 2) + 2 * g) * S + s0) * 8, vo = (4 * hf * S + r) * 8 + s2 * 256;
      const u32x2 lo = ld8(rw, vo, so), hi = ld8(rw, vo, so + S * 8);
      if (k < 4) res[s2][g] = (u32x4){lo[0], lo[1], hi[0], hi[1]}; else yv[s2][g] = (u32x4){lo[0], lo[1], hi[0], hi[1]};
    }
    SB();
  };
  const u32x4* Ob = (const u32x4*)cx.smem + (r >> 4) * 64 + hf * 16 + (r & 15);
  auto rdo = [&](int f) { const int u = f & 15; of[f & 7] = Ob[((f >> 4) * 16 + (u >> 2) * 4 + (u & 1) * 2) * 64 + ((u >> 1) & 1) * 32]; SB(); };
  __builtin_amdgcn_s_setprio(1);
  BAR();
#pragma unroll
  for (int f = 0; f < LA; ++f) rdo(f);
#pragma unroll
  for (int i = 0; i < 8; ++i)
#pragma unroll
    for (int n = 0; n < 16; ++n) {
      const int f = i * 16 + n;
      acc[n & 1] = MFMA32(as_frag(wo[i % CD][n >> 1]), as_frag(of[f & 7]), acc[n & 1]);
      SB();
      if (f + LA < 128) { if (((f + LA) & 15) == 0) BAR(); rdo(f + LA); }
      if ((n & 1) && i + CD < 8) ldw(i + CD, n >> 1);
      if (i == 5 && (n & 1)) ldres(n >> 1);
    }
  __builtin_amdgcn_s_setprio(0);
  STAMPW(kid, 2, 4);
  u32x4 x[2][2];
#pragma unroll
  for (int s2 = 0; s2 < 2; ++s2)
#pragma unroll
    for (int g = 0; g < 2; ++g)
#pragma unroll
      for (int e = 0; e < 4; ++e) x[s2][g][e] = pk2(bflo(res[s2][g][e]) + acc[s2][8 * g + 2 * e], bfhi(res[s2][g][e]) + acc[s2][8 * g + 2 * e + 1]);
  if (L == 1) {
#pragma unroll
    for (int s2 = 0; s2 < 2; ++s2)
#pragma unroll
      for (int g = 0; g < 2; ++g) *(u32x4*)(out + ((size_t)(s0 + 32 * s2 + r) * D + n0 + 16 * hf + 8 * g) * 2) = x[s2][g];
    STAMPW(kid, 6, 4); DRAIN(); STAMPW(kid, 7, 4);
    return;
  }
  f32x2* Red = (f32x2*)(cx.smem + G2_RED);
  auto rowpart = [&]() {
    float v[4] = {0.f, 0.f, 0.f, 0.f};
#pragma unroll
    for (int e = 0; e < 8; ++e)
#pragma unroll
      for (int s2 = 0; s2 < 2; ++s2) { const unsigned u = x[s2][e >> 2][e & 3]; v[2 * s2] = dot2(u, 0x3f803f80u, v[2 * s2]); v[2 * s2 + 1] = dot2(u, u, v[2 * s2 + 1]); }
    return (f32x2){swap32_sum(v[0], v[2]), swap32_sum(v[1], v[3])};
  };
  Red[c * 64 + lane] = rowpart();
  cx.half_sync(2);
  char* gx = cx.ws + OFF_GX2 + (sb * 512 + lane) * 8;
  if (c == 0) {
    const f32x2 t = (Red[lane] + Red[64 + lane]) + (Red[128 + lane] + Red[192 + lane]);
    publish8(gx + nb * 512, t[0], t[1]);
  }
  STAMPW(kid, 4, 4);
  float sm, sq;
#pragma unroll 1
  for (int spin = 0; spin < SPIN_MAX; ++spin) {
    u32x2 g[8];
#pragma unroll
    for (int k = 0; k < 8; ++k) g[k] = poll8(gx + k * 512);
    bool empty = false;
    sm = 0.f; sq = 0.f;
#pragma unroll
    for (int k = 0; k < 8; ++k) { empty |= g[k][1] == ~0u; sm += __uint_as_float(g[k][0]); sq += __uint_as_float(g[k][1]); }
    if (!__any(empty)) break;
    __builtin_amdgcn_s_sleep(1);
  }
  STAMPW(kid, 3, 4);
  const f32x2 st = ln_stats(sm, sq);
  const auto mm = __builtin_amdgcn_permlane32_swap(__float_as_uint(st[0]), __float_as_uint(st[0]), false, false);
  const auto rr = __builtin_amdgcn_permlane32_swap(__float_as_uint(st[1]), __float_as_uint(st[1]), false, false);
#pragma unroll
  for (int s2 = 0; s2 < 2; ++s2) {
    const float mu = __uint_as_float(mm[s2]), rs = __uint_as_float(rr[s2]);
#pragma unroll
    for (int g = 0; g < 2; ++g) {
#pragma unroll
      for (int e = 0; e < 4; ++e) {
        const float a = bflo(x[s2][g][e]), b = bfhi(x[s2][g][e]);
        x[s2][g][e] = pk2(a + (a - mu) * rs * bflo(yv[s2][g][e]), b + (b - mu) * rs * bfhi(yv[s2][g][e]));
      }
      st16(rw, OFF_X3 + (((n0 >> 3) + 2 * hf + g) * S + s0 + 32 * s2 + r) * 16, x[s2][g]);
    }
  }
  Red[256 + c * 64 + lane] = rowpart();
  cx.half_sync(3);
  if (c == 0) {
    const f32x2 t = (Red[256 + lane] + Red[320 + lane]) + (Red[384 + lane] + Red[448 + lane]);
    st8(rw, OFF_ST3 + ((sb * 8 + nb) * 64 + lane) * 8, __builtin_bit_cast(u32x2, t));
  }
  STAMPW(kid, 6, 4);
  wait_vm<0>();
  cx.half_sync(4);
  if (c == 0) cx.signal(FL_X3 + sb * 8 + nb);
  STAMPW(kid, 7, 4);
  BAR();
}

__global__ __launch_bounds__(512, 2) void fused_kernel(const uint16_t* __restrict__ q, const uint16_t* __restrict__ k,
                                                       const uint16_t* __restrict__ v, const uint16_t* __restrict__ shift_k,
                                                       const uint16_t* __restrict__ ssm_k, const uint16_t* __restrict__ ssm_D,
                                                       const uint16_t* __restrict__ wqkv, const uint16_t* __restrict__ wo,
                                                       char* __restrict__ ws, char* __restrict__ out) {
  extern __shared__ __attribute__((aligned(16))) char smem[];
  unsigned long long pc;
  asm volatile("s_getpc_b64 %0" : "=s"(pc));
  STAMP(0, 0);
  Cx c;
  c.ws = ws; c.smem = smem; c.rw = mk_rsrc(ws, WS_USED); c.rc = mk_rsrc((void*)(pc & ~255ull), CODE_BYTES);
  c.lane = threadIdx.x & 63; c.w = __builtin_amdgcn_readfirstlane(threadIdx.x >> 6); c.tid = threadIdx.x & 255; c.bid = blockIdx.x;
  if (c.w < 4) {
    front_m(c, k, v, shift_k, ssm_k, ssm_D);
    c.fresh(); g1_m<0>(c); c.fresh(); g2_m<0>(c);
    c.fresh(); g1_m<1>(c); c.fresh(); g2_m<1>(c);
  } else {
    front_h(c, q, wqkv, wo);
    c.fresh(); g1_h<0>(c); c.fresh(); g2_h<0>(c, out);
    c.fresh(); g1_h<1>(c); c.fresh(); g2_h<1>(c, out);
  }
}

extern "C" void kernel_launch(void* const* d_in, const int* in_sizes, int n_in, void* d_out, int out_size, void* d_ws,
                              size_t ws_size, hipStream_t stream) {
  (void)hipFuncSetAttribute((const void*)fused_kernel, hipFuncAttributeMaxDynamicSharedMemorySize, LDS_BYTES);
  fused_kernel<<<256, 512, LDS_BYTES, stream>>>((const uint16_t*)d_in[0], (const uint16_t*)d_in[1], (const uint16_t*)d_in[2],
                                                (const uint16_t*)d_in[3], (const uint16_t*)d_in[4], (const uint16_t*)d_in[5],
                                                (const uint16_t*)d_in[7], (const uint16_t*)d_in[8], (char*)d_ws, (char*)d_out);
}
```

```cpp
#define MARK 0
#include <hip/hip_runtime.h>
#include <stdint.h>

typedef __attribute__((ext_vector_type(8))) short bf16x8;
typedef __attribute__((ext_vector_type(4))) short s16x4;
typedef __attribute__((ext_vector_type(4))) float f32x4;
typedef __attribute__((ext_vector_type(2))) float f32x2;
typedef __attribute__((ext_vector_type(16))) float f32x16;
typedef __attribute__((ext_vector_type(4))) unsigned u32x4;
typedef __attribute__((ext_vector_type(2))) unsigned u32x2;
typedef __attribute__((ext_vector_type(2))) __bf16 bf16x2_t;
typedef __amdgpu_buffer_rsrc_t rsrc_t;
#define DI __device__ __forceinline__
#define LAS __attribute__((address_space(3)))
#define STAMPW(kid, slot, wv) do {} while (0)
#define DRAIN() do {} while (0)
#define STAMP(kid, slot) STAMPW(kid, slot, 0)

constexpr int S = 2048, D = 1024;
constexpr unsigned MB = 1024 * 1024;
constexpr unsigned OFF_UB = 0,
                   OFF_X0 = 4 * MB,
                   OFF_W8 = 8 * MB,
                   OFF_WOF = 14 * MB,
                   OFF_Y = 16 * MB,
                   OFF_X1 = 20 * MB,
                   OFF_PQ = 24 * MB,
                   OFF_X3 = 32 * MB,
                   OFF_SM = 36 * MB, WS_USED = 37 * MB;
constexpr int KV_LAYER = 32 * 1024 + 32 * 32;
constexpr unsigned OFF_ST0 = OFF_SM,
                   OFF_CSP = OFF_ST0 + 16384,
                   OFF_KV = OFF_CSP + 16 * 3072 * 4,
                   OFF_GX = OFF_KV + 2 * KV_LAYER * 4,
                   OFF_GX2 = OFF_GX + 65536,
                   OFF_ST3 = OFF_GX2 + 131072,
                   OFF_FLG = OFF_ST3 + 131072,
                   OFF_EPO = OFF_FLG + 2304 * 32;
constexpr int FL_U = 0, FL_Q = 1024, FL_X1 = 1280, FL_G1 = 1536  , FL_X3 = 2048;
constexpr unsigned CODE_BYTES = 56 * 1024;
constexpr int SPIN_MAX = 1 << 17;

DI float bflo(unsigned u) { return __uint_as_float(u << 16); }
DI float bfhi(unsigned u) { return __uint_as_float(u & 0xffff0000u); }
DI unsigned pk2v(f32x2 v) { return __builtin_bit_cast(unsigned, __builtin_convertvector(v, bf16x2_t)); }
DI unsigned pk2(float a, float b) { return pk2v((f32x2){a, b}); }
DI float rbf(float a) { return bflo(pk2(a, a)); }
DI bf16x8 as_frag(u32x4 v) { return __builtin_bit_cast(bf16x8, v); }
DI f32x2 lo2(f32x4 v) { return (f32x2){v[0], v[1]}; }
DI f32x2 hi2(f32x4 v) { return (f32x2){v[2], v[3]}; }
DI float dot2(unsigned a, unsigned b, float c) {
  return __builtin_amdgcn_fdot2_f32_bf16(__builtin_bit_cast(bf16x2_t, a), __builtin_bit_cast(bf16x2_t, b), c, false);
}
#define MFMA16(a, b, c) __builtin_amdgcn_mfma_f32_16x16x32_bf16((a), (b), (c), 0, 0, 0)
#define MFMA32(a, b, c) __builtin_amdgcn_mfma_f32_32x32x16_bf16((a), (b), (c), 0, 0, 0)
#define WAIT_LGKM0() asm volatile("s_waitcnt lgkmcnt(0)" ::: "memory")
#define BAR() do { __builtin_amdgcn_s_barrier(); asm volatile("" ::: "memory"); } while (0)
#define SB() __builtin_amdgcn_sched_barrier(0)
#define KEEP(x) asm volatile("" ::"v"(x))
template <int N> DI void wait_vm() { asm volatile("s_waitcnt vmcnt(%0)" ::"i"(N) : "memory"); }
DI rsrc_t mk_rsrc(void* p, unsigned bytes) { return __builtin_amdgcn_make_buffer_rsrc(p, 0, bytes, 0x00020000); }
DI void glds16(const void* g, char* l) {
  __builtin_amdgcn_global_load_lds((const __attribute__((address_space(1))) void*)g, (LAS void*)l, 16, 0, 0);
}
DI void dma16(rsrc_t r, char* l, unsigned voff, unsigned soff) { __builtin_amdgcn_raw_ptr_buffer_load_lds(r, (LAS void*)l, 16, voff, soff, 0, 16); }
DI u32x4 ld16(rsrc_t r, unsigned voff, unsigned soff) { return __builtin_amdgcn_raw_buffer_load_b128(r, voff, soff, 16); }
DI u32x2 ld8(rsrc_t r, unsigned voff, unsigned soff) { return __builtin_amdgcn_raw_buffer_load_b64(r, voff, soff, 16); }
DI void st16(rsrc_t r, unsigned off, u32x4 v) { __builtin_amdgcn_raw_buffer_store_b128(v, r, off, 0, 16); }
DI void st8(rsrc_t r, unsigned off, u32x2 v) { __builtin_amdgcn_raw_buffer_store_b64(v, r, off, 0, 16); }
DI void put8(char* p, u32x2 v) { __hip_atomic_store((unsigned long long*)p, __builtin_bit_cast(unsigned long long, v), __ATOMIC_RELAXED, __HIP_MEMORY_SCOPE_AGENT); }
DI u32x2 poll8(const char* p) { return __builtin_bit_cast(u32x2, __hip_atomic_load((const unsigned long long*)p, __ATOMIC_RELAXED, __HIP_MEMORY_SCOPE_AGENT)); }
DI void publish8(char* p, float sm, float sq) {
  unsigned qb = __float_as_uint(sq);
  if (qb == ~0u) qb = 0x7fc00000u;
  put8(p, (u32x2){__float_as_uint(sm), qb});
}
DI float opq(float m) { asm("" : "+v"(m)); return m; }
template <int N> DI float row_ror(float x) {
  return __int_as_float(__builtin_amdgcn_update_dpp(0, __float_as_int(x), 0x120 + N, 0xf, 0xf, false));
}
DI float swap16_sum(float a, float b) { const auto p = __builtin_amdgcn_permlane16_swap(__float_as_uint(a), __float_as_uint(b), false, false); return __uint_as_float(p[0]) + __uint_as_float(p[1]); }
DI float swap32_sum(float a, float b) { const auto p = __builtin_amdgcn_permlane32_swap(__float_as_uint(a), __float_as_uint(b), false, false); return __uint_as_float(p[0]) + __uint_as_float(p[1]); }

DI f32x2 ln_stats(float s, float q) {
  const float m = s * (1.0f / 1024.0f);
  const float v = fmaxf(q * (1.0f / 1024.0f) - m * m, 0.0f);
  const float sd = rbf(sqrtf(rbf(rbf(v) + 1e-5f)));
  return (f32x2){rbf(m), 1.0f / sd};
}
constexpr float LOG2E = 1.44269504088896f;
DI f32x2 phi2(f32x2 t2) {
  const f32x2 a = t2 * LOG2E - 1e-6f * LOG2E;
  f32x2 e, m;
  e[0] = __builtin_amdgcn_fmed3f(__builtin_amdgcn_exp2f(a[0]), 0.0f, 1.0f);
  e[1] = __builtin_amdgcn_fmed3f(__builtin_amdgcn_exp2f(a[1]), 0.0f, 1.0f);
  m[0] = fmaxf(t2[0], 1e-6f);
  m[1] = fmaxf(t2[1], 1e-6f);
  return m + e;
}

constexpr int LDS_BYTES = 163840, LDS_CNT = LDS_BYTES - 64;
constexpr int CV_PAD = 512, CV_HA = 286 * 16, CV_HB = CV_HA + 4160, CV_CH = CV_HB + 4128 + 32;
constexpr int CV_WB = CV_PAD + 4 * CV_CH, CV_X0 = CV_WB + 4 * 8192, CV_ST = CV_X0 + 16384;
constexpr int G1_AB = 32768, G1_STAGE = G1_AB + 12288, G1_SCR = 3 * G1_STAGE, G1_NP = 11;
constexpr int G2_Z = 131072, G2_RED = G2_Z + 2048;

struct Cx {
  char* ws; char* smem; rsrc_t rw, rc; int lane, w, tid, bid; u32x2 tag;
  unsigned long long t0, tm;
  DI void mark(int m) { if (m == MARK) { tm = __builtin_amdgcn_s_memrealtime(); __builtin_amdgcn_s_waitcnt(0xC07F); } }
  DI void half_sync(int n) const {
    const unsigned c = (unsigned)(size_t)(LAS char*)(smem + LDS_CNT + (w >> 2) * 16);
    asm volatile("s_waitcnt lgkmcnt(0)" ::: "memory");
    if (lane == 0) asm volatile("ds_add_u32 %0, %1" ::"v"(c), "v"(1u) : "memory");
    unsigned x;
    do { asm volatile("ds_read_b32 %0, %1\n\ts_waitcnt lgkmcnt(0)" : "=v"(x) : "v"(c) : "memory"); } while (__builtin_amdgcn_readfirstlane(x) < 4u * n);
  }
  DI void signal(int idx) const { if (lane == 0) put8(ws + OFF_FLG + idx * 32, tag); }
  DI void await(int idx, bool pend) const {
    const char* fp = ws + OFF_FLG + idx * 32;
#pragma unroll 1
    for (int spin = 0; spin < SPIN_MAX; ++spin) {
      if (pend) { const u32x2 f = poll8(fp); pend = f[0] != tag[0] || f[1] != tag[1]; }
      if (!__any(pend)) break;
      __builtin_amdgcn_s_sleep(1);
    }
    asm volatile("" ::: "memory");
  }
  DI unsigned touch_code() const {
    unsigned x;
    asm volatile("buffer_load_dword %0, %1, %2, 0 offen" : "=v"(x) : "v"(((bid >> 3) * 16 + (lane & 15)) * 128), "s"(rc) : "memory");
    return x;
  }
  DI void fresh() {
    asm volatile("" : "+v"(tid), "+s"(w), "+s"(bid));
    lane = tid & 63;
  }
  DI void set_tag(u32x2 e) {
    tag = (u32x2){__builtin_amdgcn_readfirstlane(e[0]) * 1664525u + 1013904223u, __builtin_amdgcn_readfirstlane(e[1]) * 22695477u + 1u};
  }
};

DI void conv_store(const Cx& c) {
  const int j = (c.bid & 7) * 32 + (c.bid >> 3), t = c.tid + (c.w >> 2) * 256;
  const char* smem = c.smem;
  u32x4 yo[2];
#pragma unroll
  for (int i = 0; i < 2; ++i) {
    const int p = t + 512 * i;
    unsigned yy[4];
#pragma unroll
    for (int ch = 0; ch < 4; ++ch) yy[ch] = *(const unsigned*)(smem + CV_PAD + ch * CV_CH + CV_HA + p * 4);
    yo[i] = (u32x4){__builtin_amdgcn_perm(yy[1], yy[0], 0x05040100u), __builtin_amdgcn_perm(yy[3], yy[2], 0x05040100u),
                    __builtin_amdgcn_perm(yy[1], yy[0], 0x07060302u), __builtin_amdgcn_perm(yy[3], yy[2], 0x07060302u)};
    const u32x4 x0v = *(const u32x4*)(smem + CV_X0 + p * 16);
    const f32x4 stv = *(const f32x4*)(smem + CV_ST + p * 16);
    u32x4 xo;
#pragma unroll
    for (int e = 0; e < 4; ++e) {
      const float m = stv[e & 2], rs = stv[(e & 2) + 1], xa = bflo(x0v[e]), xb = bfhi(x0v[e]);
      xo[e] = pk2(xa + (xa - m) * rs * bflo(yo[i][e]), xb + (xb - m) * rs * bfhi(yo[i][e]));
    }
    st16(c.rw, OFF_X1 + (j * 1024 + p) * 16, xo);
  }
  SB();
#pragma unroll
  for (int i = 0; i < 2; ++i) st16(c.rw, OFF_Y + (j * 1024 + t + 512 * i) * 16, yo[i]);
  wait_vm<2>();
  WAIT_LGKM0();
  BAR();
}

DI void front_m(Cx& c, const uint16_t* k, const uint16_t* v, const uint16_t* shift_k, const uint16_t* ssm_k, const uint16_t* ssm_D) {
  const int lane = c.lane, w = c.w, tid = c.tid, bid = c.bid, s0 = bid * 8, j = (bid & 7) * 32 + (bid >> 3);
  char* smem = c.smem;
  u32x4* bounce = (u32x4*)(smem + CV_X0) + w * 256;
  u32x4 uu[4], hh[4];
  unsigned hn[4];
  float Dc;
  u32x2 e0;
  {
    u32x2 kk[11], vv[8];
#pragma unroll
    for (int r = 0; r < 11; ++r) kk[r] = *(const u32x2*)(k + (size_t)((r < 3) ? max(s0 - 3 + r, 0) : s0 - 3 + r) * D + tid * 4);
#pragma unroll
    for (int r = 0; r < 8; ++r) vv[r] = *(const u32x2*)(v + (size_t)(s0 + r) * D + tid * 4);
    const u32x4 wa = *(const u32x4*)(shift_k + tid * 16), wb = *(const u32x4*)(shift_k + tid * 16 + 8);
    SB();
    const uint16_t* hrow = ssm_k + (size_t)(4 * j + w) * S;
#pragma unroll
    for (int i = 0; i < 4; ++i) {
      const int ch = i * 64 + lane;
      hh[i] = *(const u32x4*)(hrow + ch * 8);
      hn[i] = *(const unsigned*)(hrow + min(ch * 8 + 8, S - 2));
    }
    Dc = bflo((unsigned)ssm_D[4 * j + w]);
    e0 = poll8(c.ws + OFF_EPO);
    SB();
    if (tid < 2) *(unsigned*)(smem + LDS_CNT + tid * 16) = 0u;
    WAIT_LGKM0();
    BAR();
    SB();
    const unsigned hm = bid ? ~0u : 0u;
#pragma unroll
    for (int r = 0; r < 3; ++r) { kk[r][0] &= hm; kk[r][1] &= hm; }
    const int sw = (lane >> 1) & 3;
#pragma unroll
    for (int ch = 0; ch < 4; ++ch) {
      const unsigned w01 = (ch == 0) ? wa[0] : (ch == 1) ? wa[2] : (ch == 2) ? wb[0] : wb[2];
      const unsigned w23 = (ch == 0) ? wa[1] : (ch == 1) ? wa[3] : (ch == 2) ? wb[1] : wb[3];
      const float w0 = bflo(w01), w1 = bfhi(w01), w2 = bflo(w23), w3 = bfhi(w23);
      float kf[11], u[8];
#pragma unroll
      for (int r = 0; r < 11; ++r) kf[r] = (ch & 1) ? bfhi(kk[r][ch >> 1]) : bflo(kk[r][ch >> 1]);
#pragma unroll
      for (int r = 0; r < 8; ++r) {
        const float ks = kf[r] * w3 + kf[r + 1] * w2 + kf[r + 2] * w1 + kf[r + 3] * w0;
        u[r] = ks * ((ch & 1) ? bfhi(vv[r][ch >> 1]) : bflo(vv[r][ch >> 1]));
      }
      bounce[lane * 4 + (ch ^ sw)] = (u32x4){pk2(u[7], u[6]), pk2(u[5], u[4]), pk2(u[3], u[2]), pk2(u[1], u[0])};
    }
#pragma unroll
    for (int i = 0; i < 4; ++i) {
      const int u = 64 * i + lane;
      st16(c.rw, OFF_UB + (bid * 1024 + w * 256 + u) * 16, bounce[(u & ~3) | ((u & 3) ^ ((u >> 3) & 3))]);
    }
    STAMP(0, 2);
    SB();
  }
  char* my = smem + CV_PAD + w * CV_CH;
  if (lane < 30) *(u32x4*)(my + lane * 16) = (u32x4){0u, 0u, 0u, 0u};
  if (lane >= 32 && lane < 34) *(u32x4*)(my + CV_HA + (lane - 32) * 16) = (u32x4){0u, 0u, 0u, 0u};
  if (lane == 34) *(u32x4*)(my + CV_HB) = (u32x4){0u, 0u, 0u, 0u};
  if (lane == 0) *(u32x4*)(my + CV_HB + 16) = (u32x4){0u, 0u, 0u, hh[0][0] << 16};
#pragma unroll
  for (int i = 0; i < 4; ++i) {
    const int ch = i * 64 + lane;
    *(u32x4*)(my + CV_HA + 32 + ch * 16) = hh[i];
    *(u32x4*)(my + CV_HB + 32 + ch * 16) =
        (u32x4){__builtin_amdgcn_alignbyte(hh[i][1], hh[i][0], 2), __builtin_amdgcn_alignbyte(hh[i][2], hh[i][1], 2),
                __builtin_amdgcn_alignbyte(hh[i][3], hh[i][2], 2), __builtin_amdgcn_alignbyte(hn[i], hh[i][3], 2)};
  }
  asm volatile("" : "+v"(Dc), "+v"(e0));
  c.set_tag(e0);
  wait_vm<0>();
  c.signal(FL_U + w * 256 + bid);
  STAMP(0, 3);
  c.await(FL_U + (j >> 6) * 256 + w * 64 + lane, true);
  STAMP(0, 4);
#pragma unroll
  for (int i = 0; i < 4; ++i) uu[i] = ld16(c.rw, ((i * 16 + (lane >> 2)) * 1024 + (lane & 3)) * 16, OFF_UB + (w * 65536 + 4 * j) * 16);
#pragma unroll
  for (int i = 0; i < 4; ++i) *(u32x4*)(smem + CV_PAD + (lane & 3) * CV_CH + (30 + w * 64 + i * 16 + (lane >> 2)) * 16) = uu[i];
  const int t0 = lane & 15, kq = lane >> 4;
  const LAS unsigned* fbk[4];
#pragma unroll
  for (int i = 0; i < 4; ++i) {
    fbk[i] = (const LAS unsigned*)(my + ((t0 & 1) ? CV_HA : CV_HB)) + 4 * kq + ((1 + t0) >> 1) + 256 * i;
    asm volatile("" : "+v"(fbk[i]));
  }
  const char* gb = my + (2 * t0 + 3 - kq) * 16;
  f32x4 acc[8];
#pragma unroll
  for (int b = 0; b < 8; ++b) { acc[b] = (f32x4){0.f, 0.f, 0.f, 0.f}; asm volatile("" : "+v"(acc[b])); }
  WAIT_LGKM0();
  BAR();
  STAMP(1, 1);
  bf16x8 F[2][8], G[2][8];
  u32x2 fh[8];
  auto rdn = [&](int buf, int g0, int n) {
    if (n == 0 || n > 16) { const int g1 = n ? n - 16 : 0; G[buf][g1] = *(const bf16x8*)(gb + (32 * g1 + 4 * (7 - g0)) * 16); }
    else {
      const int e1 = (n - 1) >> 1;
      const LAS unsigned* p = fbk[e1 >> 1] + 128 * (e1 & 1) + 16 * g0;
      if ((n - 1) & 1) F[buf][e1] = as_frag((u32x4){fh[e1][0], fh[e1][1], p[2], p[3]});
      else fh[e1] = (u32x2){p[0], p[1]};
    }
  };
  auto cstep = [&](int cb, int g0n) {
    int n = 0;
#pragma unroll
    for (int g1 = 0; g1 < 8; ++g1)
#pragma unroll
      for (int e1 = 0; e1 + g1 < 8; ++e1) {
        acc[g1 + e1] = MFMA16(F[cb][e1], G[cb][g1], acc[g1 + e1]);
        SB();
        if (n < 24) { rdn(cb ^ 1, g0n, n); SB(); }
        ++n;
      }
  };
#pragma unroll
  for (int n = 0; n < 24; ++n) rdn(0, 0, n);
  for (int gp = 0; gp < 4; ++gp) {
    cstep(0, 1);
    cstep(1, 2);
    gb -= 128;
#pragma unroll
    for (int i = 0; i < 4; ++i) fbk[i] += 32;
  }
  asm volatile("" ::"v"(acc[7]), "v"(acc[0]));
  STAMP(1, 2);
#pragma unroll
  for (int b = 0; b < 8; ++b) {
    const int s = 256 * b + 16 * t0 + 4 * kq;
    const u32x2 ur = *(const u32x2*)(my + (30 + (s >> 3)) * 16 + ((s & 4) ? 0 : 8));
    *(u32x2*)(my + CV_HA + s * 2) = (u32x2){pk2(acc[b][0] + bfhi(ur[1]) * Dc, acc[b][1] + bflo(ur[1]) * Dc),
                                            pk2(acc[b][2] + bfhi(ur[0]) * Dc, acc[b][3] + bflo(ur[0]) * Dc)};
  }
  WAIT_LGKM0();
  BAR();
  STAMP(1, 3);
  conv_store(c);
  STAMP(1, 6);
  if (w == 0) c.signal(FL_X1 + bid);
  const unsigned pf = c.touch_code();
  c.await(FL_X1 + w * 64 + lane, true);
  KEEP(pf);
  STAMP(1, 7);
  BAR();
  if (bid == 0 && tid == 0) put8(c.ws + OFF_EPO, c.tag);
}

DI void front_h(Cx& c, const uint16_t* q, const uint16_t* wqkv, const uint16_t* wo) {
  const int lane = c.lane, w4 = c.w - 4, tid = c.tid, bid = c.bid, s0 = bid * 8, j = (bid & 7) * 32 + (bid >> 3);
  char* smem = c.smem;
  const rsrc_t rw = c.rw;
  u32x4* bounce = (u32x4*)(smem + CV_X0) + c.w * 256;
  float* red = (float*)smem;
  u32x2 qq[8];
#pragma unroll
  for (int r = 0; r < 8; ++r) qq[r] = *(const u32x2*)(q + (size_t)(s0 + r) * D + tid * 4);
  SB();
  u32x2 e0 = poll8(c.ws + OFF_EPO);
  const unsigned pf = c.touch_code();
  SB();
  BAR();
  const int sw = (lane >> 1) & 3;
#pragma unroll
  for (int r = 0; r < 4; ++r) bounce[lane * 4 + (r ^ sw)] = (u32x4){qq[2 * r][0], qq[2 * r][1], qq[2 * r + 1][0], qq[2 * r + 1][1]};
#pragma unroll
  for (int i = 0; i < 4; ++i) {
    const int u = 64 * i + lane;
    st16(rw, OFF_X0 + (bid * 1024 + w4 * 256 + u) * 16, bounce[(u & ~3) | ((u & 3) ^ ((u >> 3) & 3))]);
  }
  if (tid < 48) st16(rw, OFF_GX + bid * 768 + tid * 16, (u32x4){~0u, ~0u, ~0u, ~0u});
  float a[16];
#pragma unroll
  for (int r = 0; r < 8; ++r) {
    a[2 * r] = dot2(qq[r][0], 0x3f803f80u, dot2(qq[r][1], 0x3f803f80u, 0.f));
    a[2 * r + 1] = dot2(qq[r][0], qq[r][0], dot2(qq[r][1], qq[r][1], 0.f));
  }
#pragma unroll
  for (int i = 0; i < 8; ++i) a[i] = swap32_sum(a[i], a[i + 8]);
#pragma unroll
  for (int i = 0; i < 4; ++i) a[i] = swap16_sum(a[i], a[i + 4]);
#pragma unroll
  for (int i = 0; i < 4; ++i) { a[i] += row_ror<1>(a[i]); a[i] += row_ror<2>(a[i]); a[i] += row_ror<4>(a[i]); a[i] += row_ror<8>(a[i]); }
  if ((lane & 15) == 0) *(f32x4*)&red[w4 * 16 + (lane >> 4) * 4] = (f32x4){a[0], a[1], a[2], a[3]};
  asm volatile("" : "+v"(e0));
  c.set_tag(e0);
  wait_vm<0>();
  KEEP(pf);
  c.half_sync(1);
  if (w4 == 0) {
    if (lane < 8) {
      const float sm = (red[2 * lane] + red[16 + 2 * lane]) + (red[32 + 2 * lane] + red[48 + 2 * lane]);
      const float sq = (red[2 * lane + 1] + red[17 + 2 * lane]) + (red[33 + 2 * lane] + red[49 + 2 * lane]);
      st8(rw, OFF_ST0 + (s0 + lane) * 8, __builtin_bit_cast(u32x2, ln_stats(sm, sq)));
    }
    wait_vm<0>();
    c.signal(FL_Q + bid);
  }
  STAMPW(0, 5, 4);
  const int gw = bid * 4 + w4;
  char* buf = smem + CV_WB + w4 * 8192;
  const int g = lane >> 4, qr = (lane >> 2) & 3, p = lane & 3, rr = lane >> 3;
  const bool isw = gw < 768;
  const int cg = isw ? gw % 48 : (gw - 768) & 15, og = isw ? gw / 48 : (gw - 768) >> 4;
  if (isw) {
    const uint16_t* src = wqkv + (size_t)(og * 64 + rr) * 3072 + cg * 64 + (lane & 7) * 8;
#pragma unroll
    for (int i = 0; i < 8; ++i) glds16(src + (size_t)i * 8 * 3072, buf + i * 1024);
  } else {
    const uint16_t* src = wo + (size_t)(og * 64 + (rr & 3) + 16 * (rr >> 2)) * 1024 + cg * 64 + (lane & 7) * 8;
#pragma unroll
    for (int i = 0; i < 8; ++i) glds16(src + (size_t)((i >> 2) * 32 + (i & 3) * 4) * 1024, buf + i * 1024);
  }
  BAR();
  c.mark(1);
  c.await(FL_Q + w4 * 64 + lane, true);
  const int hd = (cg & 15) * 2 + (lane >> 5), col = (cg >> 4) * 32 + (lane & 31);
  const unsigned dst = isw ? OFF_W8 + ((hd * 128 + og * 8) * 96 + col) * 16
                           : OFF_WOF + ((((og >> 1) * 32 + cg * 2 + (g >> 1)) * 8 + (og & 1) * 4) * 64 + (lane & 31)) * 16;
  const unsigned dstep = isw ? 1536 : 512;
  const unsigned la = (unsigned)(size_t)(LAS char*)buf + qr * 128 + (isw ? g * 32 + p * 8 : (g >> 1) * 64 + (p & 1) * 32 + ((g & 1) * 2 + (p >> 1)) * 8);
  wait_vm<0>();
  u32x2 lo[8], hi[8];
#pragma unroll
  for (int i = 0; i < 8; ++i) {
    lo[i] = __builtin_bit_cast(u32x2, __builtin_amdgcn_ds_read_tr16_b64_v4i16((LAS s16x4*)(size_t)(la + i * 1024)));
    hi[i] = __builtin_bit_cast(u32x2, __builtin_amdgcn_ds_read_tr16_b64_v4i16((LAS s16x4*)(size_t)(la + i * 1024 + 512)));
  }
  SB();
#pragma unroll
  for (int i = 0; i < 4; ++i) {
    const int pp = w4 * 256 + i * 64 + lane;
    dma16(rw, smem + CV_X0 + (w4 * 4 + i) * 1024, ((pp >> 2) * 1024 + (pp & 3)) * 16, OFF_X0 + j * 64);
    dma16(rw, smem + CV_ST + (w4 * 4 + i) * 1024, pp * 16, OFF_ST0);
  }
  SB();
#pragma unroll
  for (int i = 0; i < 8; ++i) st16(rw, dst + i * dstep, (u32x4){lo[i][0], lo[i][1], hi[i][0], hi[i][1]});
  if (lane < 16 + 1 - (gw & 1)) st16(rw, OFF_KV + (gw * 33 / 2 + (gw & 1) + lane) * 16, (u32x4){0u, 0u, 0u, 0u});
  if (isw) {
    float c0 = 0.f, c1 = 0.f;
#pragma unroll
    for (int i = 0; i < 8; ++i)
#pragma unroll
      for (int e = 0; e < 2; ++e) {
        c0 = opq(opq(c0 + bflo(lo[i][e])) + bflo(hi[i][e]));
        c1 = opq(opq(c1 + bfhi(lo[i][e])) + bfhi(hi[i][e]));
      }
    __builtin_amdgcn_raw_buffer_store_b32(__float_as_uint(c0 + c1), rw, OFF_CSP + ((hd * 96 + col) * 16 + og) * 4, 0, 16);
  }
  STAMPW(1, 4, 4);
  wait_vm<9>();
  BAR();
  conv_store(c);
  BAR();
}

template <int L> DI void g1_m(const Cx& c) {
  constexpr bool C4 = L == 0;
  constexpr int kid = 2 + 2 * L;
  const int lane = c.lane, w = c.w, tid = c.tid, xcd = c.bid & 7, idx = c.bid >> 3;
  const int band = (xcd & 1) * 4 + (idx & 3), h = (xcd >> 1) * 8 + (idx >> 2), r = lane & 15, kq = lane >> 4;
  char* smem = c.smem;
  const float* Rs = (const float*)(smem + G1_SCR);
  const float* MRs = Rs + 256;
  const float* CSs = Rs + 512;
  float* P = (float*)(smem + G1_SCR) + 640;
  const char* ab = C4 ? smem + kq * 4096 + ((64 * w + r) ^ (16 * (kq & 1))) * 8 : smem + kq * 4096 + (64 * w + r) * 16;
  const char* ab2 = ab + 128 - (kq & 1) * 256;
  const char* bb = smem + G1_AB + kq * 1536 + r * 16;
  STAMP(kid, 0);
  f32x4 acc[4][6];
#pragma unroll
  for (int m = 0; m < 4; ++m)
#pragma unroll
    for (int p = 0; p < 6; ++p) { acc[m][p] = (f32x4){0.f, 0.f, 0.f, 0.f}; asm volatile("" : "+v"(acc[m][p])); }
  bf16x8 F[2][10];
  auto rd1 = [&](int buf, const char* a, const char* a2, const char* b, int t, int i) {
    if (i < 4) {
      if (C4) {
        const char* p = ((i & 1) ? a2 : a) + t * 16384 + (i >> 1) * 256;
        const u32x2 lo = *(const u32x2*)p, hi = *(const u32x2*)(p + 2048);
        F[buf][i] = as_frag((u32x4){lo[0], lo[1], hi[0], hi[1]});
      } else F[buf][i] = *(const bf16x8*)(a + t * 16384 + i * 256);
    } else F[buf][i] = *(const bf16x8*)(b + t * 6144 + (i - 4) * 256);
  };
  auto step = [&](int cb, const char* a, const char* a2, const char* b, int t, bool rd) {
    const int ord[10] = {0, 4, 5, 6, 7, 8, 9, 1, 2, 3};
#pragma unroll
    for (int i = 0; i < 24; ++i) {
      const int m = i / 6, p = i % 6;
      if (p < 2) acc[m][p] = MFMA16(F[cb][4 + p], F[cb][m], acc[m][p]);
      else acc[m][p] = MFMA16(F[cb][m], F[cb][4 + p], acc[m][p]);
      SB();
      if (rd && (i & 1) && (i >> 1) < 10) { rd1(cb ^ 1, a, a2, b, t, ord[i >> 1]); SB(); }
    }
  };
  BAR();
  STAMP(kid, 1);
#pragma unroll
  for (int i = 0; i < 10; ++i) rd1(0, ab, ab2, bb, 0, i);
  int so = 0;
  for (int it = 0; it < 15; ++it) {
    step(0, ab + so, ab2 + so, bb + so, 1, true);
    WAIT_LGKM0();
    BAR();
    so = (so == 2 * G1_STAGE) ? 0 : so + G1_STAGE;
    step(1, ab + so, ab2 + so, bb + so, 0, true);
  }
  step(0, ab + so, ab2 + so, bb + so, 1, true);
  BAR();
  step(1, ab, ab2, bb, 0, false);
  STAMP(kid, 2);
  f32x4 Z[2][2], Zz[2];
#pragma unroll
  for (int i = 0; i < 2; ++i) { Z[i][0] = (f32x4){0.f, 0.f, 0.f, 0.f}; Z[i][1] = Z[i][0]; Zz[i] = Z[i][0]; }
  {
    float ck[4];
#pragma unroll
    for (int p = 0; p < 4; ++p) ck[p] = CSs[32 + p * 16 + r];
    const bf16x8 ones = as_frag((u32x4){0x3f803f80u, 0x3f803f80u, 0x3f803f80u, 0x3f803f80u});
#pragma unroll
    for (int jj = 0; jj < 2; ++jj) {
      u32x4 fr[4];
#pragma unroll
      for (int mm = 0; mm < 2; ++mm) {
        const int m = 2 * jj + mm;
        const f32x4 rs = *(const f32x4*)(Rs + 64 * w + 16 * m + 4 * kq), mr = *(const f32x4*)(MRs + 64 * w + 16 * m + 4 * kq);
#pragma unroll
        for (int p = 0; p < 2; ++p) {
          fr[p][2 * mm] = pk2v(phi2(lo2(acc[m][2 + p]) * lo2(rs) + (lo2(mr) * ck[p] + 1e-6f)));
          fr[p][2 * mm + 1] = pk2v(phi2(hi2(acc[m][2 + p]) * hi2(rs) + (hi2(mr) * ck[p] + 1e-6f)));
          fr[2 + p][2 * mm] = pk2v(lo2(acc[m][4 + p]) * lo2(rs) + lo2(mr) * ck[2 + p]);
          fr[2 + p][2 * mm + 1] = pk2v(hi2(acc[m][4 + p]) * hi2(rs) + hi2(mr) * ck[2 + p]);
        }
      }
#pragma unroll
      for (int dh = 0; dh < 2; ++dh) {
        Z[0][dh] = MFMA16(as_frag(fr[dh]), as_frag(fr[2]), Z[0][dh]);
        Z[1][dh] = MFMA16(as_frag(fr[dh]), as_frag(fr[3]), Z[1][dh]);
        Zz[dh] = MFMA16(as_frag(fr[dh]), ones, Zz[dh]);
      }
    }
  }
  {
    float* mine = P + w * 1056;
#pragma unroll
    for (int i = 0; i < 4; ++i) *(f32x4*)(mine + i * 256 + lane * 4) = Z[i >> 1][i & 1];
    if (r == 0) { *(f32x4*)(mine + 1024 + 4 * kq) = Zz[0]; *(f32x4*)(mine + 1040 + 4 * kq) = Zz[1]; }
  }
  c.half_sync(2 * L + 1);
  STAMP(kid, 3);
  float* kvacc = (float*)(c.ws + OFF_KV) + L * KV_LAYER;
#pragma unroll
  for (int i = 0; i < 4; ++i) {
    const int e = tid + 256 * i;
    atomicAdd(kvacc + h * 1024 + e, (P[e] + P[1056 + e]) + (P[2112 + e] + P[3168 + e]));
  }
  if (tid < 32) atomicAdd(kvacc + 32768 + h * 32 + tid, (P[1024 + tid] + P[2080 + tid]) + (P[3136 + tid] + P[4192 + tid]));
  SB();
  {
    const f32x4 c0 = *(const f32x4*)(CSs + 4 * kq), c1 = *(const f32x4*)(CSs + 16 + 4 * kq);
#pragma unroll
    for (int m = 0; m < 4; ++m) {
      const float rs = Rs[64 * w + 16 * m + r], mr = MRs[64 * w + 16 * m + r];
      const u32x4 o = {pk2v(phi2(lo2(acc[m][0]) * rs + (lo2(c0) * mr + 1e-6f))), pk2v(phi2(hi2(acc[m][0]) * rs + (hi2(c0) * mr + 1e-6f))),
                       pk2v(phi2(lo2(acc[m][1]) * rs + (lo2(c1) * mr + 1e-6f))), pk2v(phi2(hi2(acc[m][1]) * rs + (hi2(c1) * mr + 1e-6f)))};
      st16(c.rw, OFF_PQ + L * 4 * MB + ((((h >> 2) * 128 + band * 16 + w * 4 + m) * 4 + (h & 3)) * 64 + lane) * 16, o);
    }
  }
  STAMP(kid, 6);
  wait_vm<0>();
  c.half_sync(2 * L + 2);
  STAMP(kid, 7);
  if (w == 0) c.signal(FL_G1 + L * 256 + h * 8 + band);
}

template <int L> DI void g1_h(const Cx& c) {
  constexpr bool C4 = L == 0;
  constexpr unsigned OFF_X = C4 ? OFF_X1 : OFF_X3;
  const int lane = c.lane, lw = c.w - 4, t = c.tid, xcd = c.bid & 7, idx = c.bid >> 3;
  const int band = (xcd & 1) * 4 + (idx & 3), h = (xcd >> 1) * 8 + (idx >> 2);
  char* smem = c.smem;
  char* ws = c.ws;
  const rsrc_t rw = c.rw;
  float* Rs = (float*)(smem + G1_SCR);
  const unsigned l0 = lane * 16, l1 = (lane ^ 8) * 16;
  auto issue = [&](int st) {
    char* base = smem + (st % 3) * G1_STAGE;
#pragma unroll
    for (int p = 0; p < 8; ++p) {
      if (C4) dma16(rw, base + (8 * lw + p) * 1024, (p & 4) ? l1 : l0, OFF_X + ((st * 16 + lw * 4 + (p >> 1)) * 1024 + band * 128 + (p & 1) * 64) * 16);
      else dma16(rw, base + (8 * lw + p) * 1024, l0, OFF_X + ((st * 8 + lw * 2 + (p >> 2)) * S + band * 256 + (p & 3) * 64) * 16);
    }
#pragma unroll
    for (int p = 0; p < 3; ++p) dma16(rw, base + G1_AB + (3 * lw + p) * 1024, l0, OFF_W8 + (h * 128 + st * 8) * 1536 + (3 * lw + p) * 1024);
  };
  issue(0); issue(1);
  wait_vm<G1_NP>();
  BAR();
  issue(2);
  u32x4 xv[4];
  float v[4] = {0.f, 0.f, 0.f, 0.f};
  if (C4) {
    const u32x4* xp = (const u32x4*)(ws + OFF_X) + (size_t)(t >> 2) * 1024 + band * 128 + h * 4 + (t & 3);
    asm volatile("global_load_dwordx4 %0, %4, off sc1\n\tglobal_load_dwordx4 %1, %5, off sc1\n\tglobal_load_dwordx4 %2, %6, off sc1\n\tglobal_load_dwordx4 %3, %7, off sc1"
                 : "=&v"(xv[0]), "=&v"(xv[1]), "=&v"(xv[2]), "=&v"(xv[3]) : "v"(xp), "v"(xp + 65536), "v"(xp + 131072), "v"(xp + 196608) : "memory");
  }
#pragma unroll
  for (int i = 0; i < 13; ++i) {
    if (C4 && i < 2) wait_vm<G1_NP + 4>();
    else if (C4 && i == 2) asm volatile("s_waitcnt vmcnt(%4)" : "+v"(xv[0]), "+v"(xv[1]), "+v"(xv[2]), "+v"(xv[3]) : "i"(G1_NP) : "memory");
    else wait_vm<G1_NP>();
    BAR();
    issue(i + 3);
    if (C4 && i >= 2 && i < 6) {
      const u32x4 x = xv[i - 2];
#pragma unroll
      for (int e = 0; e < 4; ++e) {
        const float lo = bflo(x[e]), hi = bfhi(x[e]);
        v[e & 2] = opq(v[e & 2] + lo); v[(e & 2) + 1] = opq(fmaf(lo, lo, v[(e & 2) + 1]));
        v[e & 2] = opq(v[e & 2] + hi); v[(e & 2) + 1] = opq(fmaf(hi, hi, v[(e & 2) + 1]));
      }
      asm volatile("" : "+v"(v[0]), "+v"(v[1]), "+v"(v[2]), "+v"(v[3]));
    }
    if (C4 && i == 6) {
#pragma unroll
      for (int e = 0; e < 4; ++e) { v[e] += row_ror<4>(v[e]); v[e] += row_ror<8>(v[e]); v[e] = swap16_sum(v[e], v[e]); v[e] = swap32_sum(v[e], v[e]); }
      const bool up = (lane & 4) != 0;
      if (lane < 8) publish8(ws + OFF_GX + ((band * 4 + lw) * 256 + h * 8 + 2 * (lane & 3) + (lane >> 2)) * 8, up ? v[2] : v[0], up ? v[3] : v[1]);
    }
  }
  const char* gp = ws + (C4 ? OFF_GX + (band * 1024 + t) * 8 + 4096 : OFF_ST3 + ((band * 4 + lw) * 512 + lane) * 8);
  const char* cp = ws + OFF_CSP + (h * 96 + (t < 96 ? t : 0)) * 64;
  u32x2 g[8];
  f32x4 c0, c1, c2, c3;
  asm volatile("global_load_dwordx4 %0, %4, off sc1\n\tglobal_load_dwordx4 %1, %4, off offset:16 sc1\n\t"
               "global_load_dwordx4 %2, %4, off offset:32 sc1\n\tglobal_load_dwordx4 %3, %4, off offset:48 sc1"
               : "=&v"(c0), "=&v"(c1), "=&v"(c2), "=&v"(c3) : "v"(cp) : "memory");
  if (C4) {
    asm volatile("global_load_dwordx2 %0, %4, off offset:-4096 sc1\n\tglobal_load_dwordx2 %1, %4, off offset:-2048 sc1\n\t"
                 "global_load_dwordx2 %2, %4, off sc1\n\tglobal_load_dwordx2 %3, %4, off offset:2048 sc1"
                 : "=&v"(g[0]), "=&v"(g[1]), "=&v"(g[2]), "=&v"(g[3]) : "v"(gp) : "memory");
    wait_vm<G1_NP + 8>();
  } else {
    asm volatile("global_load_dwordx2 %0, %8, off sc1\n\tglobal_load_dwordx2 %1, %8, off offset:512 sc1\n\tglobal_load_dwordx2 %2, %8, off offset:1024 sc1\n\t"
                 "global_load_dwordx2 %3, %8, off offset:1536 sc1\n\tglobal_load_dwordx2 %4, %8, off offset:2048 sc1\n\tglobal_load_dwordx2 %5, %8, off offset:2560 sc1\n\t"
                 "global_load_dwordx2 %6, %8, off offset:3072 sc1\n\tglobal_load_dwordx2 %7, %8, off offset:3584 sc1"
                 : "=&v"(g[0]), "=&v"(g[1]), "=&v"(g[2]), "=&v"(g[3]), "=&v"(g[4]), "=&v"(g[5]), "=&v"(g[6]), "=&v"(g[7]) : "v"(gp) : "memory");
    wait_vm<G1_NP + 12>();
  }
  BAR();
  if (C4) asm volatile("s_waitcnt vmcnt(0)" : "+v"(g[0]), "+v"(g[1]), "+v"(g[2]), "+v"(g[3]), "+v"(c0), "+v"(c1), "+v"(c2), "+v"(c3)::"memory");
  else asm volatile("s_waitcnt vmcnt(0)" : "+v"(g[0]), "+v"(g[1]), "+v"(g[2]), "+v"(g[3]), "+v"(g[4]), "+v"(g[5]), "+v"(g[6]), "+v"(g[7]),
                    "+v"(c0), "+v"(c1), "+v"(c2), "+v"(c3)::"memory");
  BAR();
  if (C4) {
#pragma unroll 1
    for (int spin = 0; spin < SPIN_MAX; ++spin) {
      if (!__any(g[0][1] == ~0u || g[1][1] == ~0u || g[2][1] == ~0u || g[3][1] == ~0u)) break;
      __builtin_amdgcn_s_sleep(4);
#pragma unroll
      for (int k = 0; k < 4; ++k) g[k] = poll8(gp + (k - 2) * 2048);
    }
  }
  float sm = 0.f, sq = 0.f, cs = 0.f;
#pragma unroll
  for (int k = 0; k < (C4 ? 4 : 8); ++k) { sm = opq(sm + __uint_as_float(g[k][0])); sq = opq(sq + __uint_as_float(g[k][1])); }
#pragma unroll
  for (int k = 0; k < 4; ++k) cs = opq(opq(opq(opq(cs + c0[k]) + c1[k]) + c2[k]) + c3[k]);
  const f32x2 st = ln_stats(sm, sq);
  Rs[t] = st[1]; Rs[256 + t] = -st[0] * st[1];
  if (t < 96) Rs[512 + t] = cs;
  WAIT_LGKM0();
  BAR();
}

template <int L> DI void g2_m(const Cx& c) {
  constexpr int kid = 3 + 2 * L;
  constexpr unsigned KVO = OFF_KV + L * KV_LAYER * 4;
  const int lane = c.lane, w = c.w, xcd = c.bid & 7, idx = c.bid >> 3, sb = (xcd >> 1) * 8 + (idx & 7), kq = lane >> 4;
  const rsrc_t rw = c.rw;
  const unsigned l16 = lane * 16;
  u32x4* O = (u32x4*)c.smem;
  unsigned pf = c.touch_code();
  c.await(FL_G1 + L * 256 + ((lane >> 3) * 4 + w) * 8 + (lane & 7), true);
  KEEP(pf);
  STAMP(kid, 0);
  constexpr int PL = 2, PD = PL + 1;
  u32x4 pq[PD][4];
  f32x4 kv[PD][4];
  const unsigned spq = OFF_PQ + L * 4 * MB + (sb * 16 + w) * 1024, skv = KVO + w * 4096;
  auto ld = [&](int i, int n) {
    if (n < 4) kv[i % PD][n] = __builtin_bit_cast(f32x4, ld16(rw, l16 + n * 1024, skv + i * 16384));
    else pq[i % PD][n - 4] = ld16(rw, l16, spq + (i * 512 + (n - 4) * 4) * 1024);
    SB();
  };
  const f32x4 zall = __builtin_bit_cast(f32x4, ld16(rw, (lane >> 3) * 512 + (lane & 7) * 16, KVO + 131072 + w * 128));
  SB();
#pragma unroll
  for (int n = 0; n < 8 * PL; ++n) ld(n >> 3, n & 7);
  u32x2* Zs = (u32x2*)(c.smem + G2_Z) + w * 64;
  Zs[lane] = (u32x2){pk2v(lo2(zall)), pk2v(hi2(zall))};
  const f32x4 zero = {0.f, 0.f, 0.f, 0.f}, eps = {1e-6f, 1e-6f, 1e-6f, 1e-6f};
  bf16x8 k0, k1, zb;
  auto prep = [&](int i) {
    const int b = i % PD;
    const u32x2 za = Zs[i * 8 + kq], zc = Zs[i * 8 + 4 + kq];
    k0 = as_frag((u32x4){pk2v(lo2(kv[b][0])), pk2v(hi2(kv[b][0])), pk2v(lo2(kv[b][1])), pk2v(hi2(kv[b][1]))});
    k1 = as_frag((u32x4){pk2v(lo2(kv[b][2])), pk2v(hi2(kv[b][2])), pk2v(lo2(kv[b][3])), pk2v(hi2(kv[b][3]))});
    zb = as_frag((u32x4){za[0], za[1], zc[0], zc[1]});
    asm volatile("" : "+v"(k0), "+v"(k1), "+v"(zb));
  };
  prep(0);
#pragma unroll
  for (int i = 0; i < 8; ++i) {
    const int b = i % PD;
    f32x4 x0[4], x1[4], dn[4];
    __builtin_amdgcn_s_setprio(2);
#pragma unroll
    for (int st = 0; st < 4; ++st) {
      dn[st] = MFMA16(zb, as_frag(pq[b][st]), eps);
      x0[st] = MFMA16(k0, as_frag(pq[b][st]), zero);
      x1[st] = MFMA16(k1, as_frag(pq[b][st]), zero);
    }
    __builtin_amdgcn_s_setprio(0);
    SB();
#pragma unroll
    for (int st = 0; st < 4; ++st) {
      const float inv = __builtin_amdgcn_rcpf(dn[st][0]);
      O[((i * 4 + w) * 4 + st) * 64 + lane] =
          (u32x4){pk2(opq(x0[st][0] * inv), opq(x0[st][1] * inv)), pk2(opq(x0[st][2] * inv), opq(x0[st][3] * inv)),
                  pk2(opq(x1[st][0] * inv), opq(x1[st][1] * inv)), pk2(opq(x1[st][2] * inv), opq(x1[st][3] * inv))};
      SB();
      if (i + PL < 8) { ld(i + PL, 2 * st); ld(i + PL, 2 * st + 1); }
    }
    if (i < 7) prep(i + 1);
    WAIT_LGKM0();
    BAR();
    if (i == 0) STAMP(kid, 1);
  }
  if (L == 0) {
    const int band = (xcd & 1) * 4 + (idx & 3);
    pf = c.touch_code();
    if (w == 0) c.await(FL_X3 + band * 32 + (lane & 31), lane < 32);
    wait_vm<0>();
    KEEP(pf);
    STAMP(kid, 5);
    BAR();
  }
}

template <int L> DI void g2_h(const Cx& cx, char* out) {
  constexpr int kid = 3 + 2 * L;
  const int lane = cx.lane, c = cx.w - 4, xcd = cx.bid & 7, idx = cx.bid >> 3;
  const int sb = (xcd >> 1) * 8 + (idx & 7), nb = (xcd & 1) * 4 + (idx >> 3);
  const int n0 = nb * 128 + c * 32, s0 = sb * 64, r = lane & 31, hf = lane >> 5;
  const rsrc_t rw = cx.rw;
  const unsigned l16 = lane * 16;
  constexpr int CD = 5, LA = 6;
  u32x4 wo[CD][8], of[8];
  const unsigned swo = OFF_WOF + (nb * 4 + c) * 8192;
  auto ldw = [&](int i, int n) { wo[i % CD][n] = ld16(rw, l16 + (n & 3) * 1024, swo + i * 262144 + (n >> 2) * 4096); SB(); };
#pragma unroll
  for (int n = 0; n < 8 * CD; ++n) ldw(n >> 3, n & 7);
  f32x16 acc[2];
#pragma unroll
  for (int i = 0; i < 16; ++i) { acc[0][i] = 0.f; acc[1][i] = 0.f; }
  asm volatile("" : "+v"(acc[0]), "+v"(acc[1]));
  u32x4 res[2][2], yv[2][2];
  auto ldres = [&](int k) {
    const int s2 = (k >> 1) & 1, g = k & 1;
    if (L == 1) { if (k < 4) res[s2][g] = ld16(rw, (2 * hf * S + r) * 16 + s2 * 512, OFF_X3 + (((n0 >> 3) + g) * S + s0) * 16); }
    else {
      const unsigned so = (k < 4 ? OFF_X1 : OFF_Y) + (((n0 >> 2) + 2 * g) * S + s0) * 8, vo = (4 * hf * S + r) * 8 + s2 * 256;
      const u32x2 lo = ld8(rw, vo, so), hi = ld8(rw, vo, so + S * 8);
      if (k < 4) res[s2][g] = (u32x4){lo[0], lo[1], hi[0], hi[1]}; else yv[s2][g] = (u32x4){lo[0], lo[1], hi[0], hi[1]};
    }
    SB();
  };
  const u32x4* Ob = (const u32x4*)cx.smem + (r >> 4) * 64 + hf * 16 + (r & 15);
  auto rdo = [&](int f) { const int u = f & 15; of[f & 7] = Ob[((f >> 4) * 16 + (u >> 2) * 4 + (u & 1) * 2) * 64 + ((u >> 1) & 1) * 32]; SB(); };
  __builtin_amdgcn_s_setprio(1);
  BAR();
#pragma unroll
  for (int f = 0; f < LA; ++f) rdo(f);
#pragma unroll
  for (int i = 0; i < 8; ++i)
#pragma unroll
    for (int n = 0; n < 16; ++n) {
      const int f = i * 16 + n;
      acc[n & 1] = MFMA32(as_frag(wo[i % CD][n >> 1]), as_frag(of[f & 7]), acc[n & 1]);
      SB();
      if (f + LA < 128) { if (((f + LA) & 15) == 0) BAR(); rdo(f + LA); }
      if ((n & 1) && i + CD < 8) ldw(i + CD, n >> 1);
      if (i == 5 && (n & 1)) ldres(n >> 1);
    }
  __builtin_amdgcn_s_setprio(0);
  STAMPW(kid, 2, 4);
  u32x4 x[2][2];
#pragma unroll
  for (int s2 = 0; s2 < 2; ++s2)
#pragma unroll
    for (int g = 0; g < 2; ++g)
#pragma unroll
      for (int e = 0; e < 4; ++e) x[s2][g][e] = pk2(bflo(res[s2][g][e]) + acc[s2][8 * g + 2 * e], bfhi(res[s2][g][e]) + acc[s2][8 * g + 2 * e + 1]);
  if (L == 1) {
#pragma unroll
    for (int s2 = 0; s2 < 2; ++s2)
#pragma unroll
      for (int g = 0; g < 2; ++g) *(u32x4*)(out + ((size_t)(s0 + 32 * s2 + r) * D + n0 + 16 * hf + 8 * g) * 2) = x[s2][g];
    STAMPW(kid, 6, 4); DRAIN(); STAMPW(kid, 7, 4);
    return;
  }
  f32x2* Red = (f32x2*)(cx.smem + G2_RED);
  auto rowpart = [&]() {
    float v[4] = {0.f, 0.f, 0.f, 0.f};
#pragma unroll
    for (int e = 0; e < 8; ++e)
#pragma unroll
      for (int s2 = 0; s2 < 2; ++s2) { const unsigned u = x[s2][e >> 2][e & 3]; v[2 * s2] = dot2(u, 0x3f803f80u, v[2 * s2]); v[2 * s2 + 1] = dot2(u, u, v[2 * s2 + 1]); }
    return (f32x2){swap32_sum(v[0], v[2]), swap32_sum(v[1], v[3])};
  };
  Red[c * 64 + lane] = rowpart();
  cx.half_sync(2);
  char* gx = cx.ws + OFF_GX2 + (sb * 512 + lane) * 8;
  if (c == 0) {
    const f32x2 t = (Red[lane] + Red[64 + lane]) + (Red[128 + lane] + Red[192 + lane]);
    publish8(gx + nb * 512, t[0], t[1]);
  }
  STAMPW(kid, 4, 4);
  float sm, sq;
#pragma unroll 1
  for (int spin = 0; spin < SPIN_MAX; ++spin) {
    u32x2 g[8];
#pragma unroll
    for (int k = 0; k < 8; ++k) g[k] = poll8(gx + k * 512);
    bool empty = false;
    sm = 0.f; sq = 0.f;
#pragma unroll
    for (int k = 0; k < 8; ++k) { empty |= g[k][1] == ~0u; sm += __uint_as_float(g[k][0]); sq += __uint_as_float(g[k][1]); }
    if (!__any(empty)) break;
    __builtin_amdgcn_s_sleep(1);
  }
  STAMPW(kid, 3, 4);
  const f32x2 st = ln_stats(sm, sq);
  const auto mm = __builtin_amdgcn_permlane32_swap(__float_as_uint(st[0]), __float_as_uint(st[0]), false, false);
  const auto rr = __builtin_amdgcn_permlane32_swap(__float_as_uint(st[1]), __float_as_uint(st[1]), false, false);
#pragma unroll
  for (int s2 = 0; s2 < 2; ++s2) {
    const float mu = __uint_as_float(mm[s2]), rs = __uint_as_float(rr[s2]);
#pragma unroll
    for (int g = 0; g < 2; ++g) {
#pragma unroll
      for (int e = 0; e < 4; ++e) {
        const float a = bflo(x[s2][g][e]), b = bfhi(x[s2][g][e]);
        x[s2][g][e] = pk2(a + (a - mu) * rs * bflo(yv[s2][g][e]), b + (b - mu) * rs * bfhi(yv[s2][g][e]));
      }
      st16(rw, OFF_X3 + (((n0 >> 3) + 2 * hf + g) * S + s0 + 32 * s2 + r) * 16, x[s2][g]);
    }
  }
  Red[256 + c * 64 + lane] = rowpart();
  cx.half_sync(3);
  if (c == 0) {
    const f32x2 t = (Red[256 + lane] + Red[320 + lane]) + (Red[384 + lane] + Red[448 + lane]);
    st8(rw, OFF_ST3 + ((sb * 8 + nb) * 64 + lane) * 8, __builtin_bit_cast(u32x2, t));
  }
  STAMPW(kid, 6, 4);
  wait_vm<0>();
  cx.half_sync(4);
  if (c == 0) cx.signal(FL_X3 + sb * 8 + nb);
  STAMPW(kid, 7, 4);
  BAR();
}

__global__ __launch_bounds__(512, 2) void fused_kernel(const uint16_t* __restrict__ q, const uint16_t* __restrict__ k,
                                                       const uint16_t* __restrict__ v, const uint16_t* __restrict__ shift_k,
                                                       const uint16_t* __restrict__ ssm_k, const uint16_t* __restrict__ ssm_D,
                                                       const uint16_t* __restrict__ wqkv, const uint16_t* __restrict__ wo,
                                                       char* __restrict__ ws, char* __restrict__ out) {
  extern __shared__ __attribute__((aligned(16))) char smem[];
  unsigned long long pc;
  asm volatile("s_getpc_b64 %0" : "=s"(pc));
  STAMP(0, 0);
  Cx c;
  c.ws = ws; c.smem = smem; c.rw = mk_rsrc(ws, WS_USED); c.rc = mk_rsrc((void*)(pc & ~255ull), CODE_BYTES);
  c.lane = threadIdx.x & 63; c.w = __builtin_amdgcn_readfirstlane(threadIdx.x >> 6); c.tid = threadIdx.x & 255; c.bid = blockIdx.x;
  c.t0 = __builtin_amdgcn_s_memrealtime(); c.tm = c.t0;
  if (c.w < 4) {
    front_m(c, k, v, shift_k, ssm_k, ssm_D);
    c.fresh(); g1_m<0>(c); c.fresh(); g2_m<0>(c);
    c.fresh(); g1_m<1>(c); c.fresh(); g2_m<1>(c);
  } else {
    front_h(c, q, wqkv, wo); c.mark(2);
    c.fresh(); g1_h<0>(c); c.mark(3); c.fresh(); g2_h<0>(c, out); c.mark(4);
    c.fresh(); g1_h<1>(c); c.mark(5); c.fresh(); g2_h<1>(c, out); c.mark(6);
    const unsigned long long te = __builtin_amdgcn_s_memrealtime();
    while (__builtin_amdgcn_s_memrealtime() - te < c.tm - c.t0) __builtin_amdgcn_s_sleep(1);
  }
}

extern "C" void kernel_launch(void* const* d_in, const int* in_sizes, int n_in, void* d_out, int out_size, void* d_ws,
                              size_t ws_size, hipStream_t stream) {
  (void)hipFuncSetAttribute((const void*)fused_kernel, hipFuncAttributeMaxDynamicSharedMemorySize, LDS_BYTES);
  fused_kernel<<<256, 512, LDS_BYTES, stream>>>((const uint16_t*)d_in[0], (const uint16_t*)d_in[1], (const uint16_t*)d_in[2],
                                                (const uint16_t*)d_in[3], (const uint16_t*)d_in[4], (const uint16_t*)d_in[5],
                                                (const uint16_t*)d_in[7], (const uint16_t*)d_in[8], (char*)d_ws, (char*)d_out);
}
```

```cpp
#include <hip/hip_runtime.h>
#include <stdint.h>

typedef __attribute__((ext_vector_type(8))) short bf16x8;
typedef __attribute__((ext_vector_type(4))) short s16x4;
typedef __attribute__((ext_vector_type(4))) float f32x4;
typedef __attribute__((ext_vector_type(2))) float f32x2;
typedef __attribute__((ext_vector_type(16))) float f32x16;
typedef __attribute__((ext_vector_type(4))) unsigned u32x4;
typedef __attribute__((ext_vector_type(2))) unsigned u32x2;
typedef __attribute__((ext_vector_type(2))) __bf16 bf16x2_t;
typedef __amdgpu_buffer_rsrc_t rsrc_t;
#define DI __device__ __forceinline__
#define LAS __attribute__((address_space(3)))
#define STAMPW(kid, slot, wv) do {} while (0)
#define DRAIN() do {} while (0)
#define STAMP(kid, slot) STAMPW(kid, slot, 0)

constexpr int S = 2048, D = 1024;
constexpr unsigned MB = 1024 * 1024;
constexpr unsigned OFF_UB = 0,
                   OFF_X0 = 4 * MB,
                   OFF_W8 = 8 * MB,
                   OFF_WOF = 14 * MB,
                   OFF_Y = 16 * MB,
                   OFF_X1 = 20 * MB,
                   OFF_PQ = 24 * MB,
                   OFF_X3 = 32 * MB,
                   OFF_SM = 36 * MB, WS_USED = 37 * MB;
constexpr int KV_LAYER = 32 * 1024 + 32 * 32;
constexpr unsigned OFF_ST0 = OFF_SM,
                   OFF_CSP = OFF_ST0 + 16384,
                   OFF_KV = OFF_CSP + 16 * 3072 * 4,
                   OFF_GX = OFF_KV + 2 * KV_LAYER * 4,
                   OFF_GX2 = OFF_GX + 65536,
                   OFF_ST3 = OFF_GX2 + 131072,
                   OFF_FLG = OFF_ST3 + 131072,
                   OFF_EPO = OFF_FLG + 3072 * 32;
constexpr int FL_U = 0, FL_Q = 1024, FL_X1 = 2048, FL_G1 = 2304  , FL_X3 = 2816;
constexpr unsigned CODE_BYTES = 56 * 1024;
constexpr int SPIN_MAX = 1 << 17;
#define WMODE 3

DI float bflo(unsigned u) { return __uint_as_float(u << 16); }
DI float bfhi(unsigned u) { return __uint_as_float(u & 0xffff0000u); }
DI unsigned pk2v(f32x2 v) { return __builtin_bit_cast(unsigned, __builtin_convertvector(v, bf16x2_t)); }
DI unsigned pk2(float a, float b) { return pk2v((f32x2){a, b}); }
DI float rbf(float a) { return bflo(pk2(a, a)); }
DI bf16x8 as_frag(u32x4 v) { return __builtin_bit_cast(bf16x8, v); }
DI f32x2 lo2(f32x4 v) { return (f32x2){v[0], v[1]}; }
DI f32x2 hi2(f32x4 v) { return (f32x2){v[2], v[3]}; }
DI float dot2(unsigned a, unsigned b, float c) {
  return __builtin_amdgcn_fdot2_f32_bf16(__builtin_bit_cast(bf16x2_t, a), __builtin_bit_cast(bf16x2_t, b), c, false);
}
#define MFMA16(a, b, c) __builtin_amdgcn_mfma_f32_16x16x32_bf16((a), (b), (c), 0, 0, 0)
#define MFMA32(a, b, c) __builtin_amdgcn_mfma_f32_32x32x16_bf16((a), (b), (c), 0, 0, 0)
#define WAIT_LGKM0() asm volatile("s_waitcnt lgkmcnt(0)" ::: "memory")
#define BAR() do { __builtin_amdgcn_s_barrier(); asm volatile("" ::: "memory"); } while (0)
#define SB() __builtin_amdgcn_sched_barrier(0)
#define KEEP(x) asm volatile("" ::"v"(x))
template <int N> DI void wait_vm() { asm volatile("s_waitcnt vmcnt(%0)" ::"i"(N) : "memory"); }
DI rsrc_t mk_rsrc(void* p, unsigned bytes) { return __builtin_amdgcn_make_buffer_rsrc(p, 0, bytes, 0x00020000); }
DI void glds16(const void* g, char* l) {
  __builtin_amdgcn_global_load_lds((const __attribute__((address_space(1))) void*)g, (LAS void*)l, 16, 0, 0);
}
DI void dma16(rsrc_t r, char* l, unsigned voff, unsigned soff) { __builtin_amdgcn_raw_ptr_buffer_load_lds(r, (LAS void*)l, 16, voff, soff, 0, 16); }
DI u32x4 ld16(rsrc_t r, unsigned voff, unsigned soff) { return __builtin_amdgcn_raw_buffer_load_b128(r, voff, soff, 16); }
DI u32x2 ld8(rsrc_t r, unsigned voff, unsigned soff) { return __builtin_amdgcn_raw_buffer_load_b64(r, voff, soff, 16); }
DI void st16(rsrc_t r, unsigned off, u32x4 v) { __builtin_amdgcn_raw_buffer_store_b128(v, r, off, 0, 16); }
DI void st8(rsrc_t r, unsigned off, u32x2 v) { __builtin_amdgcn_raw_buffer_store_b64(v, r, off, 0, 16); }
DI void put8(char* p, u32x2 v) { __hip_atomic_store((unsigned long long*)p, __builtin_bit_cast(unsigned long long, v), __ATOMIC_RELAXED, __HIP_MEMORY_SCOPE_AGENT); }
DI u32x2 poll8(const char* p) { return __builtin_bit_cast(u32x2, __hip_atomic_load((const unsigned long long*)p, __ATOMIC_RELAXED, __HIP_MEMORY_SCOPE_AGENT)); }
DI void publish8(char* p, float sm, float sq) {
  unsigned qb = __float_as_uint(sq);
  if (qb == ~0u) qb = 0x7fc00000u;
  put8(p, (u32x2){__float_as_uint(sm), qb});
}
DI float opq(float m) { asm("" : "+v"(m)); return m; }
template <int N> DI float row_ror(float x) {
  return __int_as_float(__builtin_amdgcn_update_dpp(0, __float_as_int(x), 0x120 + N, 0xf, 0xf, false));
}
DI float swap16_sum(float a, float b) { const auto p = __builtin_amdgcn_permlane16_swap(__float_as_uint(a), __float_as_uint(b), false, false); return __uint_as_float(p[0]) + __uint_as_float(p[1]); }
DI float swap32_sum(float a, float b) { const auto p = __builtin_amdgcn_permlane32_swap(__float_as_uint(a), __float_as_uint(b), false, false); return __uint_as_float(p[0]) + __uint_as_float(p[1]); }

DI f32x2 ln_stats(float s, float q) {
  const float m = s * (1.0f / 1024.0f);
  const float v = fmaxf(q * (1.0f / 1024.0f) - m * m, 0.0f);
  const float sd = rbf(sqrtf(rbf(rbf(v) + 1e-5f)));
  return (f32x2){rbf(m), 1.0f / sd};
}
constexpr float LOG2E = 1.44269504088896f;
DI f32x2 phi2(f32x2 t2) {
  const f32x2 a = t2 * LOG2E - 1e-6f * LOG2E;
  f32x2 e, m;
  e[0] = __builtin_amdgcn_fmed3f(__builtin_amdgcn_exp2f(a[0]), 0.0f, 1.0f);
  e[1] = __builtin_amdgcn_fmed3f(__builtin_amdgcn_exp2f(a[1]), 0.0f, 1.0f);
  m[0] = fmaxf(t2[0], 1e-6f);
  m[1] = fmaxf(t2[1], 1e-6f);
  return m + e;
}

constexpr int LDS_BYTES = 163840, LDS_CNT = LDS_BYTES - 64;
constexpr int CV_PAD = 512, CV_HA = 286 * 16, CV_HB = CV_HA + 4160, CV_CH = CV_HB + 4128 + 32;
constexpr int CV_WB = CV_PAD + 4 * CV_CH, CV_X0 = CV_WB + 4 * 8192, CV_ST = CV_X0 + 16384, CV_BN = CV_ST + 16384;
constexpr int G1_AB = 32768, G1_STAGE = G1_AB + 12288, G1_SCR = 3 * G1_STAGE, G1_NP = 11;
constexpr int G2_Z = 131072, G2_RED = G2_Z + 2048;

struct Cx {
  char* ws; char* smem; rsrc_t rw, rc; int lane, w, tid, bid; u32x2 tag;
  DI void mark(int) {}
  DI void endspin() {}
  DI void half_sync(int n) const {
    const unsigned c = (unsigned)(size_t)(LAS char*)(smem + LDS_CNT + (w >> 2) * 16);
    asm volatile("s_waitcnt lgkmcnt(0)" ::: "memory");
    if (lane == 0) asm volatile("ds_add_u32 %0, %1" ::"v"(c), "v"(1u) : "memory");
    unsigned x;
    do { asm volatile("ds_read_b32 %0, %1\n\ts_waitcnt lgkmcnt(0)" : "=v"(x) : "v"(c) : "memory"); } while (__builtin_amdgcn_readfirstlane(x) < 4u * n);
  }
  DI void signal(int idx) const { if (lane == 0) put8(ws + OFF_FLG + idx * 32, tag); }
  DI void await(int idx, bool pend) const {
    const char* fp = ws + OFF_FLG + idx * 32;
#pragma unroll 1
    for (int spin = 0; spin < SPIN_MAX; ++spin) {
      if (pend) { const u32x2 f = poll8(fp); pend = f[0] != tag[0] || f[1] != tag[1]; }
      if (!__any(pend)) break;
      __builtin_amdgcn_s_sleep(1);
    }
    asm volatile("" ::: "memory");
  }
  DI unsigned touch_code() const {
    unsigned x;
    asm volatile("buffer_load_dword %0, %1, %2, 0 offen" : "=v"(x) : "v"(((bid >> 3) * 16 + (lane & 15)) * 128), "s"(rc) : "memory");
    return x;
  }
  DI void fresh() {
    asm volatile("" : "+v"(tid), "+s"(w), "+s"(bid));
    lane = tid & 63;
  }
  DI void set_tag(u32x2 e) {
    tag = (u32x2){__builtin_amdgcn_readfirstlane(e[0]) * 1664525u + 1013904223u, __builtin_amdgcn_readfirstlane(e[1]) * 22695477u + 1u};
  }
};

DI void conv_store(const Cx& c) {
  const int j = (c.bid & 7) * 32 + (c.bid >> 3), t = c.tid + (c.w >> 2) * 256;
  const char* smem = c.smem;
  u32x4 yo[2];
#pragma unroll
  for (int i = 0; i < 2; ++i) {
    const int p = t + 512 * i;
    unsigned yy[4];
#pragma unroll
    for (int ch = 0; ch < 4; ++ch) yy[ch] = *(const unsigned*)(smem + CV_PAD + ch * CV_CH + CV_HA + p * 4);
    yo[i] = (u32x4){__builtin_amdgcn_perm(yy[1], yy[0], 0x05040100u), __builtin_amdgcn_perm(yy[3], yy[2], 0x05040100u),
                    __builtin_amdgcn_perm(yy[1], yy[0], 0x07060302u), __builtin_amdgcn_perm(yy[3], yy[2], 0x07060302u)};
    const u32x4 x0v = *(const u32x4*)(smem + CV_X0 + p * 16);
    const f32x4 stv = *(const f32x4*)(smem + CV_ST + p * 16);
    u32x4 xo;
#pragma unroll
    for (int e = 0; e < 4; ++e) {
      const float m = stv[e & 2], rs = stv[(e & 2) + 1], xa = bflo(x0v[e]), xb = bfhi(x0v[e]);
      xo[e] = pk2(xa + (xa - m) * rs * bflo(yo[i][e]), xb + (xb - m) * rs * bfhi(yo[i][e]));
    }
    st16(c.rw, OFF_X1 + (j * 1024 + p) * 16, xo);
  }
  SB();
#pragma unroll
  for (int i = 0; i < 2; ++i) st16(c.rw, OFF_Y + (j * 1024 + t + 512 * i) * 16, yo[i]);
  wait_vm<2>();
  WAIT_LGKM0();
  BAR();
}

DI void front_m(Cx& c, const uint16_t* k, const uint16_t* v, const uint16_t* shift_k, const uint16_t* ssm_k, const uint16_t* ssm_D) {
  const int lane = c.lane, w = c.w, tid = c.tid, bid = c.bid, s0 = bid * 8, j = (bid & 7) * 32 + (bid >> 3);
  char* smem = c.smem;
  u32x4* bounce = (u32x4*)(smem + CV_BN) + w * 256;
  u32x4 uu[4], hh[4];
  unsigned hn[4];
  float Dc;
  u32x2 e0;
  {
    u32x2 kk[11], vv[8];
#pragma unroll
    for (int r = 0; r < 11; ++r) kk[r] = *(const u32x2*)(k + (size_t)((r < 3) ? max(s0 - 3 + r, 0) : s0 - 3 + r) * D + tid * 4);
#pragma unroll
    for (int r = 0; r < 8; ++r) vv[r] = *(const u32x2*)(v + (size_t)(s0 + r) * D + tid * 4);
    const u32x4 wa = *(const u32x4*)(shift_k + tid * 16), wb = *(const u32x4*)(shift_k + tid * 16 + 8);
    SB();
    const uint16_t* hrow = ssm_k + (size_t)(4 * j + w) * S;
#pragma unroll
    for (int i = 0; i < 4; ++i) {
      const int ch = i * 64 + lane;
      hh[i] = *(const u32x4*)(hrow + ch * 8);
      hn[i] = *(const unsigned*)(hrow + min(ch * 8 + 8, S - 2));
    }
    Dc = bflo((unsigned)ssm_D[4 * j + w]);
    e0 = poll8(c.ws + OFF_EPO);
    SB();
    if (tid < 2) *(unsigned*)(smem + LDS_CNT + tid * 16) = 0u;
    WAIT_LGKM0();
    BAR();
    SB();
    const unsigned hm = bid ? ~0u : 0u;
#pragma unroll
    for (int r = 0; r < 3; ++r) { kk[r][0] &= hm; kk[r][1] &= hm; }
    const int sw = (lane >> 1) & 3;
#pragma unroll
    for (int ch = 0; ch < 4; ++ch) {
      const unsigned w01 = (ch == 0) ? wa[0] : (ch == 1) ? wa[2] : (ch == 2) ? wb[0] : wb[2];
      const unsigned w23 = (ch == 0) ? wa[1] : (ch == 1) ? wa[3] : (ch == 2) ? wb[1] : wb[3];
      const float w0 = bflo(w01), w1 = bfhi(w01), w2 = bflo(w23), w3 = bfhi(w23);
      float kf[11], u[8];
#pragma unroll
      for (int r = 0; r < 11; ++r) kf[r] = (ch & 1) ? bfhi(kk[r][ch >> 1]) : bflo(kk[r][ch >> 1]);
#pragma unroll
      for (int r = 0; r < 8; ++r) {
        const float ks = kf[r] * w3 + kf[r + 1] * w2 + kf[r + 2] * w1 + kf[r + 3] * w0;
        u[r] = ks * ((ch & 1) ? bfhi(vv[r][ch >> 1]) : bflo(vv[r][ch >> 1]));
      }
      bounce[lane * 4 + (ch ^ sw)] = (u32x4){pk2(u[7], u[6]), pk2(u[5], u[4]), pk2(u[3], u[2]), pk2(u[1], u[0])};
    }
#pragma unroll
    for (int i = 0; i < 4; ++i) {
      const int u = 64 * i + lane;
      st16(c.rw, OFF_UB + (bid * 1024 + w * 256 + u) * 16, bounce[(u & ~3) | ((u & 3) ^ ((u >> 3) & 3))]);
    }
    STAMP(0, 2);
    SB();
  }
  char* my = smem + CV_PAD + w * CV_CH;
  if (lane < 30) *(u32x4*)(my + lane * 16) = (u32x4){0u, 0u, 0u, 0u};
  if (lane >= 32 && lane < 34) *(u32x4*)(my + CV_HA + (lane - 32) * 16) = (u32x4){0u, 0u, 0u, 0u};
  if (lane == 34) *(u32x4*)(my + CV_HB) = (u32x4){0u, 0u, 0u, 0u};
  if (lane == 0) *(u32x4*)(my + CV_HB + 16) = (u32x4){0u, 0u, 0u, hh[0][0] << 16};
#pragma unroll
  for (int i = 0; i < 4; ++i) {
    const int ch = i * 64 + lane;
    *(u32x4*)(my + CV_HA + 32 + ch * 16) = hh[i];
    *(u32x4*)(my + CV_HB + 32 + ch * 16) =
        (u32x4){__builtin_amdgcn_alignbyte(hh[i][1], hh[i][0], 2), __builtin_amdgcn_alignbyte(hh[i][2], hh[i][1], 2),
                __builtin_amdgcn_alignbyte(hh[i][3], hh[i][2], 2), __builtin_amdgcn_alignbyte(hn[i], hh[i][3], 2)};
  }
  asm volatile("" : "+v"(Dc), "+v"(e0));
  c.set_tag(e0);
  wait_vm<0>();
  c.signal(FL_U + w * 256 + bid);
  STAMP(0, 3);
  c.await(FL_U + (j >> 6) * 256 + w * 64 + lane, true);
  STAMP(0, 4);
#pragma unroll
  for (int i = 0; i < 4; ++i) uu[i] = ld16(c.rw, ((i * 16 + (lane >> 2)) * 1024 + (lane & 3)) * 16, OFF_UB + (w * 65536 + 4 * j) * 16);
#pragma unroll
  for (int i = 0; i < 4; ++i) *(u32x4*)(smem + CV_PAD + (lane & 3) * CV_CH + (30 + w * 64 + i * 16 + (lane >> 2)) * 16) = uu[i];
  const int t0 = lane & 15, kq = lane >> 4;
  const LAS unsigned* fbk[4];
#pragma unroll
  for (int i = 0; i < 4; ++i) {
    fbk[i] = (const LAS unsigned*)(my + ((t0 & 1) ? CV_HA : CV_HB)) + 4 * kq + ((1 + t0) >> 1) + 256 * i;
    asm volatile("" : "+v"(fbk[i]));
  }
  const char* gb = my + (2 * t0 + 3 - kq) * 16;
  f32x4 acc[8];
#pragma unroll
  for (int b = 0; b < 8; ++b) { acc[b] = (f32x4){0.f, 0.f, 0.f, 0.f}; asm volatile("" : "+v"(acc[b])); }
  c.half_sync(1);
  STAMP(1, 0); STAMP(1, 1);
  bf16x8 F[2][8], G[2][8];
  u32x2 fh[8];
  auto rdn = [&](int buf, int g0, int n) {
    if (n == 0 || n > 16) { const int g1 = n ? n - 16 : 0; G[buf][g1] = *(const bf16x8*)(gb + (32 * g1 + 4 * (7 - g0)) * 16); }
    else {
      const int e1 = (n - 1) >> 1;
      const LAS unsigned* p = fbk[e1 >> 1] + 128 * (e1 & 1) + 16 * g0;
      if ((n - 1) & 1) F[buf][e1] = as_frag((u32x4){fh[e1][0], fh[e1][1], p[2], p[3]});
      else fh[e1] = (u32x2){p[0], p[1]};
    }
  };
  auto cstep = [&](int cb, int g0n) {
    int n = 0;
#pragma unroll
    for (int g1 = 0; g1 < 8; ++g1)
#pragma unroll
      for (int e1 = 0; e1 + g1 < 8; ++e1) {
        acc[g1 + e1] = MFMA16(F[cb][e1], G[cb][g1], acc[g1 + e1]);
        SB();
        if (n < 24) { rdn(cb ^ 1, g0n, n); SB(); }
        ++n;
      }
  };
#pragma unroll
  for (int n = 0; n < 24; ++n) rdn(0, 0, n);
  for (int gp = 0; gp < 4; ++gp) {
    cstep(0, 1);
    cstep(1, 2);
    gb -= 128;
#pragma unroll
    for (int i = 0; i < 4; ++i) fbk[i] += 32;
  }
  asm volatile("" ::"v"(acc[7]), "v"(acc[0]));
  STAMP(1, 2);
#pragma unroll
  for (int b = 0; b < 8; ++b) {
    const int s = 256 * b + 16 * t0 + 4 * kq;
    const u32x2 ur = *(const u32x2*)(my + (30 + (s >> 3)) * 16 + ((s & 4) ? 0 : 8));
    *(u32x2*)(my + CV_HA + s * 2) = (u32x2){pk2(acc[b][0] + bfhi(ur[1]) * Dc, acc[b][1] + bflo(ur[1]) * Dc),
                                            pk2(acc[b][2] + bfhi(ur[0]) * Dc, acc[b][3] + bflo(ur[0]) * Dc)};
  }
  WAIT_LGKM0();
  BAR();
  STAMP(1, 3);
  conv_store(c);
  STAMP(1, 6);
  if (w == 0) c.signal(FL_X1 + bid);
  const unsigned pf = c.touch_code();
  c.await(FL_X1 + w * 64 + lane, true);
  KEEP(pf);
  STAMP(1, 7);
  BAR();
  if (bid == 0 && tid == 0) put8(c.ws + OFF_EPO, c.tag);
}

DI void front_h(Cx& c, const uint16_t* q, const uint16_t* wqkv, const uint16_t* wo) {
  const int lane = c.lane, w4 = c.w - 4, tid = c.tid, bid = c.bid, s0 = bid * 8, j = (bid & 7) * 32 + (bid >> 3);
  char* smem = c.smem;
  const rsrc_t rw = c.rw;
  u32x4* bounce = (u32x4*)(smem + CV_BN) + c.w * 256;
  float* red = (float*)smem;
  const int gw = bid * 4 + w4;
  char* buf = smem + CV_WB + w4 * 8192;
  const int g = lane >> 4, qr = (lane >> 2) & 3, p = lane & 3, rr = lane >> 3;
  const bool isw = gw < 768;
  const int cg = isw ? gw % 48 : (gw - 768) & 15, og = isw ? gw / 48 : (gw - 768) >> 4;
  auto w_loads = [&]() {
    if (isw) {
      const uint16_t* src = wqkv + (size_t)(og * 64 + rr) * 3072 + cg * 64 + (lane & 7) * 8;
#pragma unroll
      for (int i = 0; i < 8; ++i) glds16(src + (size_t)i * 8 * 3072, buf + i * 1024);
    } else {
      const uint16_t* src = wo + (size_t)(og * 64 + (rr & 3) + 16 * (rr >> 2)) * 1024 + cg * 64 + (lane & 7) * 8;
#pragma unroll
      for (int i = 0; i < 8; ++i) glds16(src + (size_t)((i >> 2) * 32 + (i & 3) * 4) * 1024, buf + i * 1024);
    }
  };
  u32x2 qq[8];
#pragma unroll
  for (int r = 0; r < 8; ++r) qq[r] = *(const u32x2*)(q + (size_t)(s0 + r) * D + tid * 4);
  SB();
  u32x2 e0 = poll8(c.ws + OFF_EPO);
  const unsigned pf = c.touch_code();
  if (WMODE == 0) w_loads();
  SB();
  BAR();
  const int sw = (lane >> 1) & 3;
#pragma unroll
  for (int r = 0; r < 4; ++r) bounce[lane * 4 + (r ^ sw)] = (u32x4){qq[2 * r][0], qq[2 * r][1], qq[2 * r + 1][0], qq[2 * r + 1][1]};
#pragma unroll
  for (int i = 0; i < 4; ++i) {
    const int u = 64 * i + lane;
    st16(rw, OFF_X0 + (bid * 1024 + w4 * 256 + u) * 16, bounce[(u & ~3) | ((u & 3) ^ ((u >> 3) & 3))]);
  }
  if (tid < 48) st16(rw, OFF_GX + bid * 768 + tid * 16, (u32x4){~0u, ~0u, ~0u, ~0u});
  SB();
  if (WMODE == 1) w_loads();
  SB();
  float a[16];
#pragma unroll
  for (int r = 0; r < 8; ++r) {
    a[2 * r] = dot2(qq[r][0], 0x3f803f80u, dot2(qq[r][1], 0x3f803f80u, 0.f));
    a[2 * r + 1] = dot2(qq[r][0], qq[r][0], dot2(qq[r][1], qq[r][1], 0.f));
  }
#pragma unroll
  for (int i = 0; i < 8; ++i) a[i] = swap32_sum(a[i], a[i + 8]);
#pragma unroll
  for (int i = 0; i < 4; ++i) a[i] = swap16_sum(a[i], a[i + 4]);
#pragma unroll
  for (int i = 0; i < 4; ++i) { a[i] += row_ror<1>(a[i]); a[i] += row_ror<2>(a[i]); a[i] += row_ror<4>(a[i]); a[i] += row_ror<8>(a[i]); }
  if ((lane & 15) == 0) *(f32x4*)&red[w4 * 16 + (lane >> 4) * 4] = (f32x4){a[0], a[1], a[2], a[3]};
  c.half_sync(1);
  if (tid < 8) {
    const float sm = (red[2 * tid] + red[16 + 2 * tid]) + (red[32 + 2 * tid] + red[48 + 2 * tid]);
    const float sq = (red[2 * tid + 1] + red[17 + 2 * tid]) + (red[33 + 2 * tid] + red[49 + 2 * tid]);
    st8(rw, OFF_ST0 + (s0 + tid) * 8, __builtin_bit_cast(u32x2, ln_stats(sm, sq)));
  }
  asm volatile("" : "+v"(e0));
  c.set_tag(e0);
  wait_vm<0>();
  KEEP(pf);
  c.signal(FL_Q + w4 * 256 + bid);
  STAMPW(0, 5, 4);
  if (WMODE == 2) w_loads();
  {
    const unsigned cm = (unsigned)(size_t)(LAS char*)(smem + LDS_CNT);
    unsigned x;
    do { __builtin_amdgcn_s_sleep(2); asm volatile("ds_read_b32 %0, %1\n\ts_waitcnt lgkmcnt(0)" : "=v"(x) : "v"(cm) : "memory"); } while (__builtin_amdgcn_readfirstlane(x) < 4u);
  }
  c.mark(1);
  if (WMODE == 3) w_loads();
  c.await(FL_Q + w4 * 64 + lane, true);
  c.await(FL_Q + (j >> 6) * 256 + w4 * 64 + lane, true);
  const int hd = (cg & 15) * 2 + (lane >> 5), col = (cg >> 4) * 32 + (lane & 31);
  const unsigned dst = isw ? OFF_W8 + ((hd * 128 + og * 8) * 96 + col) * 16
                           : OFF_WOF + ((((og >> 1) * 32 + cg * 2 + (g >> 1)) * 8 + (og & 1) * 4) * 64 + (lane & 31)) * 16;
  const unsigned dstep = isw ? 1536 : 512;
  const unsigned la = (unsigned)(size_t)(LAS char*)buf + qr * 128 + (isw ? g * 32 + p * 8 : (g >> 1) * 64 + (p & 1) * 32 + ((g & 1) * 2 + (p >> 1)) * 8);
  u32x2 lo[8], hi[8];
#pragma unroll
  for (int i = 0; i < 8; ++i) {
    lo[i] = __builtin_bit_cast(u32x2, __builtin_amdgcn_ds_read_tr16_b64_v4i16((LAS s16x4*)(size_t)(la + i * 1024)));
    hi[i] = __builtin_bit_cast(u32x2, __builtin_amdgcn_ds_read_tr16_b64_v4i16((LAS s16x4*)(size_t)(la + i * 1024 + 512)));
  }
  SB();
#pragma unroll
  for (int i = 0; i < 4; ++i) {
    const int pp = w4 * 256 + i * 64 + lane;
    dma16(rw, smem + CV_X0 + (w4 * 4 + i) * 1024, ((pp >> 2) * 1024 + (pp & 3)) * 16, OFF_X0 + j * 64);
    dma16(rw, smem + CV_ST + (w4 * 4 + i) * 1024, pp * 16, OFF_ST0);
  }
  SB();
#pragma unroll
  for (int i = 0; i < 8; ++i) st16(rw, dst + i * dstep, (u32x4){lo[i][0], lo[i][1], hi[i][0], hi[i][1]});
  if (lane < 16 + 1 - (gw & 1)) st16(rw, OFF_KV + (gw * 33 / 2 + (gw & 1) + lane) * 16, (u32x4){0u, 0u, 0u, 0u});
  if (isw) {
    float c0 = 0.f, c1 = 0.f;
#pragma unroll
    for (int i = 0; i < 8; ++i)
#pragma unroll
      for (int e = 0; e < 2; ++e) {
        c0 = opq(opq(c0 + bflo(lo[i][e])) + bflo(hi[i][e]));
        c1 = opq(opq(c1 + bfhi(lo[i][e])) + bfhi(hi[i][e]));
      }
    __builtin_amdgcn_raw_buffer_store_b32(__float_as_uint(c0 + c1), rw, OFF_CSP + ((hd * 96 + col) * 16 + og) * 4, 0, 16);
  }
  STAMPW(1, 4, 4);
  wait_vm<9>();
  STAMPW(1, 5, 4);
  BAR();
  conv_store(c);
  BAR();
}

template <int L> DI void g1_m(const Cx& c) {
  constexpr bool C4 = L == 0;
  constexpr int kid = 2 + 2 * L;
  const int lane = c.lane, w = c.w, tid = c.tid, xcd = c.bid & 7, idx = c.bid >> 3;
  const int band = (xcd & 1) * 4 + (idx & 3), h = (xcd >> 1) * 8 + (idx >> 2), r = lane & 15, kq = lane >> 4;
  char* smem = c.smem;
  const float* Rs = (const float*)(smem + G1_SCR);
  const float* MRs = Rs + 256;
  const float* CSs = Rs + 512;
  float* P = (float*)(smem + G1_SCR) + 640;
  const char* ab = C4 ? smem + kq * 4096 + ((64 * w + r) ^ (16 * (kq & 1))) * 8 : smem + kq * 4096 + (64 * w + r) * 16;
  const char* ab2 = ab + 128 - (kq & 1) * 256;
  const char* bb = smem + G1_AB + kq * 1536 + r * 16;
  STAMP(kid, 0);
  f32x4 acc[4][6];
#pragma unroll
  for (int m = 0; m < 4; ++m)
#pragma unroll
    for (int p = 0; p < 6; ++p) { acc[m][p] = (f32x4){0.f, 0.f, 0.f, 0.f}; asm volatile("" : "+v"(acc[m][p])); }
  bf16x8 F[2][10];
  auto rd1 = [&](int buf, const char* a, const char* a2, const char* b, int t, int i) {
    if (i < 4) {
      if (C4) {
        const char* p = ((i & 1) ? a2 : a) + t * 16384 + (i >> 1) * 256;
        const u32x2 lo = *(const u32x2*)p, hi = *(const u32x2*)(p + 2048);
        F[buf][i] = as_frag((u32x4){lo[0], lo[1], hi[0], hi[1]});
      } else F[buf][i] = *(const bf16x8*)(a + t * 16384 + i * 256);
    } else F[buf][i] = *(const bf16x8*)(b + t * 6144 + (i - 4) * 256);
  };
  auto step = [&](int cb, const char* a, const char* a2, const char* b, int t, bool rd) {
    const int ord[10] = {0, 4, 5, 6, 7, 8, 9, 1, 2, 3};
#pragma unroll
    for (int i = 0; i < 24; ++i) {
      const int m = i / 6, p = i % 6;
      if (p < 2) acc[m][p] = MFMA16(F[cb][4 + p], F[cb][m], acc[m][p]);
      else acc[m][p] = MFMA16(F[cb][m], F[cb][4 + p], acc[m][p]);
      SB();
      if (rd && (i & 1) && (i >> 1) < 10) { rd1(cb ^ 1, a, a2, b, t, ord[i >> 1]); SB(); }
    }
  };
  BAR();
  STAMP(kid, 1);
#pragma unroll
  for (int i = 0; i < 10; ++i) rd1(0, ab, ab2, bb, 0, i);
  int so = 0;
  for (int it = 0; it < 15; ++it) {
    step(0, ab + so, ab2 + so, bb + so, 1, true);
    WAIT_LGKM0();
    BAR();
    so = (so == 2 * G1_STAGE) ? 0 : so + G1_STAGE;
    step(1, ab + so, ab2 + so, bb + so, 0, true);
  }
  step(0, ab + so, ab2 + so, bb + so, 1, true);
  BAR();
  step(1, ab, ab2, bb, 0, false);
  STAMP(kid, 2);
  f32x4 Z[2][2], Zz[2];
#pragma unroll
  for (int i = 0; i < 2; ++i) { Z[i][0] = (f32x4){0.f, 0.f, 0.f, 0.f}; Z[i][1] = Z[i][0]; Zz[i] = Z[i][0]; }
  {
    float ck[4];
#pragma unroll
    for (int p = 0; p < 4; ++p) ck[p] = CSs[32 + p * 16 + r];
    const bf16x8 ones = as_frag((u32x4){0x3f803f80u, 0x3f803f80u, 0x3f803f80u, 0x3f803f80u});
#pragma unroll
    for (int jj = 0; jj < 2; ++jj) {
      u32x4 fr[4];
#pragma unroll
      for (int mm = 0; mm < 2; ++mm) {
        const int m = 2 * jj + mm;
        const f32x4 rs = *(const f32x4*)(Rs + 64 * w + 16 * m + 4 * kq), mr = *(const f32x4*)(MRs + 64 * w + 16 * m + 4 * kq);
#pragma unroll
        for (int p = 0; p < 2; ++p) {
          fr[p][2 * mm] = pk2v(phi2(lo2(acc[m][2 + p]) * lo2(rs) + (lo2(mr) * ck[p] + 1e-6f)));
          fr[p][2 * mm + 1] = pk2v(phi2(hi2(acc[m][2 + p]) * hi2(rs) + (hi2(mr) * ck[p] + 1e-6f)));
          fr[2 + p][2 * mm] = pk2v(lo2(acc[m][4 + p]) * lo2(rs) + lo2(mr) * ck[2 + p]);
          fr[2 + p][2 * mm + 1] = pk2v(hi2(acc[m][4 + p]) * hi2(rs) + hi2(mr) * ck[2 + p]);
        }
      }
#pragma unroll
      for (int dh = 0; dh < 2; ++dh) {
        Z[0][dh] = MFMA16(as_frag(fr[dh]), as_frag(fr[2]), Z[0][dh]);
        Z[1][dh] = MFMA16(as_frag(fr[dh]), as_frag(fr[3]), Z[1][dh]);
        Zz[dh] = MFMA16(as_frag(fr[dh]), ones, Zz[dh]);
      }
    }
  }
  {
    float* mine = P + w * 1056;
#pragma unroll
    for (int i = 0; i < 4; ++i) *(f32x4*)(mine + i * 256 + lane * 4) = Z[i >> 1][i & 1];
    if (r == 0) { *(f32x4*)(mine + 1024 + 4 * kq) = Zz[0]; *(f32x4*)(mine + 1040 + 4 * kq) = Zz[1]; }
  }
  c.half_sync(2 * L + 2);
  STAMP(kid, 3);
  float* kvacc = (float*)(c.ws + OFF_KV) + L * KV_LAYER;
#pragma unroll
  for (int i = 0; i < 4; ++i) {
    const int e = tid + 256 * i;
    atomicAdd(kvacc + h * 1024 + e, (P[e] + P[1056 + e]) + (P[2112 + e] + P[3168 + e]));
  }
  if (tid < 32) atomicAdd(kvacc + 32768 + h * 32 + tid, (P[1024 + tid] + P[2080 + tid]) + (P[3136 + tid] + P[4192 + tid]));
  SB();
  {
    const f32x4 c0 = *(const f32x4*)(CSs + 4 * kq), c1 = *(const f32x4*)(CSs + 16 + 4 * kq);
#pragma unroll
    for (int m = 0; m < 4; ++m) {
      const float rs = Rs[64 * w + 16 * m + r], mr = MRs[64 * w + 16 * m + r];
      const u32x4 o = {pk2v(phi2(lo2(acc[m][0]) * rs + (lo2(c0) * mr + 1e-6f))), pk2v(phi2(hi2(acc[m][0]) * rs + (hi2(c0) * mr + 1e-6f))),
                       pk2v(phi2(lo2(acc[m][1]) * rs + (lo2(c1) * mr + 1e-6f))), pk2v(phi2(hi2(acc[m][1]) * rs + (hi2(c1) * mr + 1e-6f)))};
      st16(c.rw, OFF_PQ + L * 4 * MB + ((((h >> 2) * 128 + band * 16 + w * 4 + m) * 4 + (h & 3)) * 64 + lane) * 16, o);
    }
  }
  STAMP(kid, 6);
  wait_vm<0>();
  c.half_sync(2 * L + 3);
  STAMP(kid, 7);
  if (w == 0) c.signal(FL_G1 + L * 256 + h * 8 + band);
}

template <int L> DI void g1_h(const Cx& c) {
  constexpr bool C4 = L == 0;
  constexpr unsigned OFF_X = C4 ? OFF_X1 : OFF_X3;
  const int lane = c.lane, lw = c.w - 4, t = c.tid, xcd = c.bid & 7, idx = c.bid >> 3;
  const int band = (xcd & 1) * 4 + (idx & 3), h = (xcd >> 1) * 8 + (idx >> 2);
  char* smem = c.smem;
  char* ws = c.ws;
  const rsrc_t rw = c.rw;
  float* Rs = (float*)(smem + G1_SCR);
  const unsigned l0 = lane * 16, l1 = (lane ^ 8) * 16;
  auto issue = [&](int st) {
    char* base = smem + (st % 3) * G1_STAGE;
#pragma unroll
    for (int p = 0; p < 8; ++p) {
      if (C4) dma16(rw, base + (8 * lw + p) * 1024, (p & 4) ? l1 : l0, OFF_X + ((st * 16 + lw * 4 + (p >> 1)) * 1024 + band * 128 + (p & 1) * 64) * 16);
      else dma16(rw, base + (8 * lw + p) * 1024, l0, OFF_X + ((st * 8 + lw * 2 + (p >> 2)) * S + band * 256 + (p & 3) * 64) * 16);
    }
#pragma unroll
    for (int p = 0; p < 3; ++p) dma16(rw, base + G1_AB + (3 * lw + p) * 1024, l0, OFF_W8 + (h * 128 + st * 8) * 1536 + (3 * lw + p) * 1024);
  };
  issue(0); issue(1);
  wait_vm<G1_NP>();
  BAR();
  issue(2);
  u32x4 xv[4];
  float v[4] = {0.f, 0.f, 0.f, 0.f};
  if (C4) {
    const u32x4* xp = (const u32x4*)(ws + OFF_X) + (size_t)(t >> 2) * 1024 + band * 128 + h * 4 + (t & 3);
    asm volatile("global_load_dwordx4 %0, %4, off sc1\n\tglobal_load_dwordx4 %1, %5, off sc1\n\tglobal_load_dwordx4 %2, %6, off sc1\n\tglobal_load_dwordx4 %3, %7, off sc1"
                 : "=&v"(xv[0]), "=&v"(xv[1]), "=&v"(xv[2]), "=&v"(xv[3]) : "v"(xp), "v"(xp + 65536), "v"(xp + 131072), "v"(xp + 196608) : "memory");
  }
#pragma unroll
  for (int i = 0; i < 13; ++i) {
    if (C4 && i < 2) wait_vm<G1_NP + 4>();
    else if (C4 && i == 2) asm volatile("s_waitcnt vmcnt(%4)" : "+v"(xv[0]), "+v"(xv[1]), "+v"(xv[2]), "+v"(xv[3]) : "i"(G1_NP) : "memory");
    else wait_vm<G1_NP>();
    BAR();
    issue(i + 3);
    if (C4 && i >= 2 && i < 6) {
      const u32x4 x = xv[i - 2];
#pragma unroll
      for (int e = 0; e < 4; ++e) {
        const float lo = bflo(x[e]), hi = bfhi(x[e]);
        v[e & 2] = opq(v[e & 2] + lo); v[(e & 2) + 1] = opq(fmaf(lo, lo, v[(e & 2) + 1]));
        v[e & 2] = opq(v[e & 2] + hi); v[(e & 2) + 1] = opq(fmaf(hi, hi, v[(e & 2) + 1]));
      }
      asm volatile("" : "+v"(v[0]), "+v"(v[1]), "+v"(v[2]), "+v"(v[3]));
    }
    if (C4 && i == 6) {
#pragma unroll
      for (int e = 0; e < 4; ++e) { v[e] += row_ror<4>(v[e]); v[e] += row_ror<8>(v[e]); v[e] = swap16_sum(v[e], v[e]); v[e] = swap32_sum(v[e], v[e]); }
      const bool up = (lane & 4) != 0;
      if (lane < 8) publish8(ws + OFF_GX + ((band * 4 + lw) * 256 + h * 8 + 2 * (lane & 3) + (lane >> 2)) * 8, up ? v[2] : v[0], up ? v[3] : v[1]);
    }
  }
  const char* gp = ws + (C4 ? OFF_GX + (band * 1024 + t) * 8 + 4096 : OFF_ST3 + ((band * 4 + lw) * 512 + lane) * 8);
  const char* cp = ws + OFF_CSP + (h * 96 + (t < 96 ? t : 0)) * 64;
  u32x2 g[8];
  f32x4 c0, c1, c2, c3;
  asm volatile("global_load_dwordx4 %0, %4, off sc1\n\tglobal_load_dwordx4 %1, %4, off offset:16 sc1\n\t"
               "global_load_dwordx4 %2, %4, off offset:32 sc1\n\tglobal_load_dwordx4 %3, %4, off offset:48 sc1"
               : "=&v"(c0), "=&v"(c1), "=&v"(c2), "=&v"(c3) : "v"(cp) : "memory");
  if (C4) {
    asm volatile("global_load_dwordx2 %0, %4, off offset:-4096 sc1\n\tglobal_load_dwordx2 %1, %4, off offset:-2048 sc1\n\t"
                 "global_load_dwordx2 %2, %4, off sc1\n\tglobal_load_dwordx2 %3, %4, off offset:2048 sc1"
                 : "=&v"(g[0]), "=&v"(g[1]), "=&v"(g[2]), "=&v"(g[3]) : "v"(gp) : "memory");
    wait_vm<G1_NP + 8>();
  } else {
    asm volatile("global_load_dwordx2 %0, %8, off sc1\n\tglobal_load_dwordx2 %1, %8, off offset:512 sc1\n\tglobal_load_dwordx2 %2, %8, off offset:1024 sc1\n\t"
                 "global_load_dwordx2 %3, %8, off offset:1536 sc1\n\tglobal_load_dwordx2 %4, %8, off offset:2048 sc1\n\tglobal_load_dwordx2 %5, %8, off offset:2560 sc1\n\t"
                 "global_load_dwordx2 %6, %8, off offset:3072 sc1\n\tglobal_load_dwordx2 %7, %8, off offset:3584 sc1"
                 : "=&v"(g[0]), "=&v"(g[1]), "=&v"(g[2]), "=&v"(g[3]), "=&v"(g[4]), "=&v"(g[5]), "=&v"(g[6]), "=&v"(g[7]) : "v"(gp) : "memory");
    wait_vm<G1_NP + 12>();
  }
  BAR();
  if (C4) asm volatile("s_waitcnt vmcnt(0)" : "+v"(g[0]), "+v"(g[1]), "+v"(g[2]), "+v"(g[3]), "+v"(c0), "+v"(c1), "+v"(c2), "+v"(c3)::"memory");
  else asm volatile("s_waitcnt vmcnt(0)" : "+v"(g[0]), "+v"(g[1]), "+v"(g[2]), "+v"(g[3]), "+v"(g[4]), "+v"(g[5]), "+v"(g[6]), "+v"(g[7]),
                    "+v"(c0), "+v"(c1), "+v"(c2), "+v"(c3)::"memory");
  BAR();
  if (C4) {
#pragma unroll 1
    for (int spin = 0; spin < SPIN_MAX; ++spin) {
      if (!__any(g[0][1] == ~0u || g[1][1] == ~0u || g[2][1] == ~0u || g[3][1] == ~0u)) break;
      __builtin_amdgcn_s_sleep(4);
#pragma unroll
      for (int k = 0; k < 4; ++k) g[k] = poll8(gp + (k - 2) * 2048);
    }
  }
  float sm = 0.f, sq = 0.f, cs = 0.f;
#pragma unroll
  for (int k = 0; k < (C4 ? 4 : 8); ++k) { sm = opq(sm + __uint_as_float(g[k][0])); sq = opq(sq + __uint_as_float(g[k][1])); }
#pragma unroll
  for (int k = 0; k < 4; ++k) cs = opq(opq(opq(opq(cs + c0[k]) + c1[k]) + c2[k]) + c3[k]);
  const f32x2 st = ln_stats(sm, sq);
  Rs[t] = st[1]; Rs[256 + t] = -st[0] * st[1];
  if (t < 96) Rs[512 + t] = cs;
  WAIT_LGKM0();
  BAR();
}

template <int L> DI void g2_m(const Cx& c) {
  constexpr int kid = 3 + 2 * L;
  constexpr unsigned KVO = OFF_KV + L * KV_LAYER * 4;
  const int lane = c.lane, w = c.w, xcd = c.bid & 7, idx = c.bid >> 3, sb = (xcd >> 1) * 8 + (idx & 7), kq = lane >> 4;
  const rsrc_t rw = c.rw;
  const unsigned l16 = lane * 16;
  u32x4* O = (u32x4*)c.smem;
  unsigned pf = c.touch_code();
  c.await(FL_G1 + L * 256 + ((lane >> 3) * 4 + w) * 8 + (lane & 7), true);
  KEEP(pf);
  STAMP(kid, 0);
  constexpr int PL = 2, PD = PL + 1;
  u32x4 pq[PD][4];
  f32x4 kv[PD][4];
  const unsigned spq = OFF_PQ + L * 4 * MB + (sb * 16 + w) * 1024, skv = KVO + w * 4096;
  auto ld = [&](int i, int n) {
    if (n < 4) kv[i % PD][n] = __builtin_bit_cast(f32x4, ld16(rw, l16 + n * 1024, skv + i * 16384));
    else pq[i % PD][n - 4] = ld16(rw, l16, spq + (i * 512 + (n - 4) * 4) * 1024);
    SB();
  };
  const f32x4 zall = __builtin_bit_cast(f32x4, ld16(rw, (lane >> 3) * 512 + (lane & 7) * 16, KVO + 131072 + w * 128));
  SB();
#pragma unroll
  for (int n = 0; n < 8 * PL; ++n) ld(n >> 3, n & 7);
  u32x2* Zs = (u32x2*)(c.smem + G2_Z) + w * 64;
  Zs[lane] = (u32x2){pk2v(lo2(zall)), pk2v(hi2(zall))};
  const f32x4 zero = {0.f, 0.f, 0.f, 0.f}, eps = {1e-6f, 1e-6f, 1e-6f, 1e-6f};
  bf16x8 k0, k1, zb;
  auto prep = [&](int i) {
    const int b = i % PD;
    const u32x2 za = Zs[i * 8 + kq], zc = Zs[i * 8 + 4 + kq];
    k0 = as_frag((u32x4){pk2v(lo2(kv[b][0])), pk2v(hi2(kv[b][0])), pk2v(lo2(kv[b][1])), pk2v(hi2(kv[b][1]))});
    k1 = as_frag((u32x4){pk2v(lo2(kv[b][2])), pk2v(hi2(kv[b][2])), pk2v(lo2(kv[b][3])), pk2v(hi2(kv[b][3]))});
    zb = as_frag((u32x4){za[0], za[1], zc[0], zc[1]});
    asm volatile("" : "+v"(k0), "+v"(k1), "+v"(zb));
  };
  prep(0);
#pragma unroll
  for (int i = 0; i < 8; ++i) {
    const int b = i % PD;
    f32x4 x0[4], x1[4], dn[4];
    __builtin_amdgcn_s_setprio(2);
#pragma unroll
    for (int st = 0; st < 4; ++st) {
      dn[st] = MFMA16(zb, as_frag(pq[b][st]), eps);
      x0[st] = MFMA16(k0, as_frag(pq[b][st]), zero);
      x1[st] = MFMA16(k1, as_frag(pq[b][st]), zero);
    }
    __builtin_amdgcn_s_setprio(0);
    SB();
#pragma unroll
    for (int st = 0; st < 4; ++st) {
      const float inv = __builtin_amdgcn_rcpf(dn[st][0]);
      O[((i * 4 + w) * 4 + st) * 64 + lane] =
          (u32x4){pk2(opq(x0[st][0] * inv), opq(x0[st][1] * inv)), pk2(opq(x0[st][2] * inv), opq(x0[st][3] * inv)),
                  pk2(opq(x1[st][0] * inv), opq(x1[st][1] * inv)), pk2(opq(x1[st][2] * inv), opq(x1[st][3] * inv))};
      SB();
      if (i + PL < 8) { ld(i + PL, 2 * st); ld(i + PL, 2 * st + 1); }
    }
    if (i < 7) prep(i + 1);
    WAIT_LGKM0();
    BAR();
    if (i == 0) STAMP(kid, 1);
  }
  if (L == 0) {
    const int band = (xcd & 1) * 4 + (idx & 3);
    pf = c.touch_code();
    if (w == 0) c.await(FL_X3 + band * 32 + (lane & 31), lane < 32);
    wait_vm<0>();
    KEEP(pf);
    STAMP(kid, 5);
    BAR();
  }
}

template <int L> DI void g2_h(const Cx& cx, char* out) {
  constexpr int kid = 3 + 2 * L;
  const int lane = cx.lane, c = cx.w - 4, xcd = cx.bid & 7, idx = cx.bid >> 3;
  const int sb = (xcd >> 1) * 8 + (idx & 7), nb = (xcd & 1) * 4 + (idx >> 3);
  const int n0 = nb * 128 + c * 32, s0 = sb * 64, r = lane & 31, hf = lane >> 5;
  const rsrc_t rw = cx.rw;
  const unsigned l16 = lane * 16;
  constexpr int CD = 5, LA = 6;
  u32x4 wo[CD][8], of[8];
  const unsigned swo = OFF_WOF + (nb * 4 + c) * 8192;
  auto ldw = [&](int i, int n) { wo[i % CD][n] = ld16(rw, l16 + (n & 3) * 1024, swo + i * 262144 + (n >> 2) * 4096); SB(); };
#pragma unroll
  for (int n = 0; n < 8 * CD; ++n) ldw(n >> 3, n & 7);
  f32x16 acc[2];
#pragma unroll
  for (int i = 0; i < 16; ++i) { acc[0][i] = 0.f; acc[1][i] = 0.f; }
  asm volatile("" : "+v"(acc[0]), "+v"(acc[1]));
  u32x4 res[2][2], yv[2][2];
  auto ldres = [&](int k) {
    const int s2 = (k >> 1) & 1, g = k & 1;
    if (L == 1) { if (k < 4) res[s2][g] = ld16(rw, (2 * hf * S + r) * 16 + s2 * 512, OFF_X3 + (((n0 >> 3) + g) * S + s0) * 16); }
    else {
      const unsigned so = (k < 4 ? OFF_X1 : OFF_Y) + (((n0 >> 2) + 2 * g) * S + s0) * 8, vo = (4 * hf * S + r) * 8 + s2 * 256;
      const u32x2 lo = ld8(rw, vo, so), hi = ld8(rw, vo, so + S * 8);
      if (k < 4) res[s2][g] = (u32x4){lo[0], lo[1], hi[0], hi[1]}; else yv[s2][g] = (u32x4){lo[0], lo[1], hi[0], hi[1]};
    }
    SB();
  };
  const u32x4* Ob = (const u32x4*)cx.smem + (r >> 4) * 64 + hf * 16 + (r & 15);
  auto rdo = [&](int f) { const int u = f & 15; of[f & 7] = Ob[((f >> 4) * 16 + (u >> 2) * 4 + (u & 1) * 2) * 64 + ((u >> 1) & 1) * 32]; SB(); };
  __builtin_amdgcn_s_setprio(1);
  BAR();
#pragma unroll
  for (int f = 0; f < LA; ++f) rdo(f);
#pragma unroll
  for (int i = 0; i < 8; ++i)
#pragma unroll
    for (int n = 0; n < 16; ++n) {
      const int f = i * 16 + n;
      acc[n & 1] = MFMA32(as_frag(wo[i % CD][n >> 1]), as_frag(of[f & 7]), acc[n & 1]);
      SB();
      if (f + LA < 128) { if (((f + LA) & 15) == 0) BAR(); rdo(f + LA); }
      if ((n & 1) && i + CD < 8) ldw(i + CD, n >> 1);
      if (i == 5 && (n & 1)) ldres(n >> 1);
    }
  __builtin_amdgcn_s_setprio(0);
  STAMPW(kid, 2, 4);
  u32x4 x[2][2];
#pragma unroll
  for (int s2 = 0; s2 < 2; ++s2)
#pragma unroll
    for (int g = 0; g < 2; ++g)
#pragma unroll
      for (int e = 0; e < 4; ++e) x[s2][g][e] = pk2(bflo(res[s2][g][e]) + acc[s2][8 * g + 2 * e], bfhi(res[s2][g][e]) + acc[s2][8 * g + 2 * e + 1]);
  if (L == 1) {
#pragma unroll
    for (int s2 = 0; s2 < 2; ++s2)
#pragma unroll
      for (int g = 0; g < 2; ++g) *(u32x4*)(out + ((size_t)(s0 + 32 * s2 + r) * D + n0 + 16 * hf + 8 * g) * 2) = x[s2][g];
    STAMPW(kid, 6, 4); DRAIN(); STAMPW(kid, 7, 4);
    return;
  }
  f32x2* Red = (f32x2*)(cx.smem + G2_RED);
  auto rowpart = [&]() {
    float v[4] = {0.f, 0.f, 0.f, 0.f};
#pragma unroll
    for (int e = 0; e < 8; ++e)
#pragma unroll
      for (int s2 = 0; s2 < 2; ++s2) { const unsigned u = x[s2][e >> 2][e & 3]; v[2 * s2] = dot2(u, 0x3f803f80u, v[2 * s2]); v[2 * s2 + 1] = dot2(u, u, v[2 * s2 + 1]); }
    return (f32x2){swap32_sum(v[0], v[2]), swap32_sum(v[1], v[3])};
  };
  Red[c * 64 + lane] = rowpart();
  cx.half_sync(2);
  char* gx = cx.ws + OFF_GX2 + (sb * 512 + lane) * 8;
  if (c == 0) {
    const f32x2 t = (Red[lane] + Red[64 + lane]) + (Red[128 + lane] + Red[192 + lane]);
    publish8(gx + nb * 512, t[0], t[1]);
  }
  STAMPW(kid, 4, 4);
  float sm, sq;
#pragma unroll 1
  for (int spin = 0; spin < SPIN_MAX; ++spin) {
    u32x2 g[8];
#pragma unroll
    for (int k = 0; k < 8; ++k) g[k] = poll8(gx + k * 512);
    bool empty = false;
    sm = 0.f; sq = 0.f;
#pragma unroll
    for (int k = 0; k < 8; ++k) { empty |= g[k][1] == ~0u; sm += __uint_as_float(g[k][0]); sq += __uint_as_float(g[k][1]); }
    if (!__any(empty)) break;
    __builtin_amdgcn_s_sleep(1);
  }
  STAMPW(kid, 3, 4);
  const f32x2 st = ln_stats(sm, sq);
  const auto mm = __builtin_amdgcn_permlane32_swap(__float_as_uint(st[0]), __float_as_uint(st[0]), false, false);
  const auto rr = __builtin_amdgcn_permlane32_swap(__float_as_uint(st[1]), __float_as_uint(st[1]), false, false);
#pragma unroll
  for (int s2 = 0; s2 < 2; ++s2) {
    const float mu = __uint_as_float(mm[s2]), rs = __uint_as_float(rr[s2]);
#pragma unroll
    for (int g = 0; g < 2; ++g) {
#pragma unroll
      for (int e = 0; e < 4; ++e) {
        const float a = bflo(x[s2][g][e]), b = bfhi(x[s2][g][e]);
        x[s2][g][e] = pk2(a + (a - mu) * rs * bflo(yv[s2][g][e]), b + (b - mu) * rs * bfhi(yv[s2][g][e]));
      }
      st16(rw, OFF_X3 + (((n0 >> 3) + 2 * hf + g) * S + s0 + 32 * s2 + r) * 16, x[s2][g]);
    }
  }
  Red[256 + c * 64 + lane] = rowpart();
  cx.half_sync(3);
  if (c == 0) {
    const f32x2 t = (Red[256 + lane] + Red[320 + lane]) + (Red[384 + lane] + Red[448 + lane]);
    st8(rw, OFF_ST3 + ((sb * 8 + nb) * 64 + lane) * 8, __builtin_bit_cast(u32x2, t));
  }
  STAMPW(kid, 6, 4);
  wait_vm<0>();
  cx.half_sync(4);
  if (c == 0) cx.signal(FL_X3 + sb * 8 + nb);
  STAMPW(kid, 7, 4);
  BAR();
}

__global__ __launch_bounds__(512, 2) void fused_kernel(const uint16_t* __restrict__ q, const uint16_t* __restrict__ k,
                                                       const uint16_t* __restrict__ v, const uint16_t* __restrict__ shift_k,
                                                       const uint16_t* __restrict__ ssm_k, const uint16_t* __restrict__ ssm_D,
                                                       const uint16_t* __restrict__ wqkv, const uint16_t* __restrict__ wo,
                                                       char* __restrict__ ws, char* __restrict__ out) {
  extern __shared__ __attribute__((aligned(16))) char smem[];
  unsigned long long pc;
  asm volatile("s_getpc_b64 %0" : "=s"(pc));
  STAMP(0, 0);
  Cx c;
  c.ws = ws; c.smem = smem; c.rw = mk_rsrc(ws, WS_USED); c.rc = mk_rsrc((void*)(pc & ~255ull), CODE_BYTES);
  c.lane = threadIdx.x & 63; c.w = __builtin_amdgcn_readfirstlane(threadIdx.x >> 6); c.tid = threadIdx.x & 255; c.bid = blockIdx.x;
  if (c.w < 4) {
    front_m(c, k, v, shift_k, ssm_k, ssm_D);
    c.fresh(); g1_m<0>(c); c.fresh(); g2_m<0>(c);
    c.fresh(); g1_m<1>(c); c.fresh(); g2_m<1>(c);
  } else {
    c.mark(0);
    front_h(c, q, wqkv, wo); c.mark(2);
    c.fresh(); g1_h<0>(c); c.mark(3); c.fresh(); g2_h<0>(c, out); c.mark(4);
    c.fresh(); g1_h<1>(c); c.mark(5); c.fresh(); g2_h<1>(c, out); c.mark(6);
    c.endspin();
  }
}

extern "C" void kernel_launch(void* const* d_in, const int* in_sizes, int n_in, void* d_out, int out_size, void* d_ws,
                              size_t ws_size, hipStream_t stream) {
  (void)hipFuncSetAttribute((const void*)fused_kernel, hipFuncAttributeMaxDynamicSharedMemorySize, LDS_BYTES);
  fused_kernel<<<256, 512, LDS_BYTES, stream>>>((const uint16_t*)d_in[0], (const uint16_t*)d_in[1], (const uint16_t*)d_in[2],
                                                (const uint16_t*)d_in[3], (const uint16_t*)d_in[4], (const uint16_t*)d_in[5],
                                                (const uint16_t*)d_in[7], (const uint16_t*)d_in[8], (char*)d_ws, (char*)d_out);
}
```

```cpp
#include <hip/hip_runtime.h>
#include <stdint.h>

typedef __attribute__((ext_vector_type(8))) short bf16x8;
typedef __attribute__((ext_vector_type(4))) short s16x4;
typedef __attribute__((ext_vector_type(4))) float f32x4;
typedef __attribute__((ext_vector_type(2))) float f32x2;
typedef __attribute__((ext_vector_type(16))) float f32x16;
typedef __attribute__((ext_vector_type(4))) unsigned u32x4;
typedef __attribute__((ext_vector_type(2))) unsigned u32x2;
typedef __attribute__((ext_vector_type(2))) __bf16 bf16x2_t;
typedef __amdgpu_buffer_rsrc_t rsrc_t;
#define DI __device__ __forceinline__
#define LAS __attribute__((address_space(3)))
#define STAMPW(kid, slot, wv) do {} while (0)
#define DRAIN() do {} while (0)
#define STAMP(kid, slot) STAMPW(kid, slot, 0)

constexpr int S = 2048, D = 1024;
constexpr unsigned MB = 1024 * 1024;
constexpr unsigned OFF_UB = 0,
                   OFF_X0 = 4 * MB,
                   OFF_W8 = 8 * MB,
                   OFF_WOF = 14 * MB,
                   OFF_Y = 16 * MB,
                   OFF_X1 = 20 * MB,
                   OFF_PQ = 24 * MB,
                   OFF_X3 = 32 * MB,
                   OFF_SM = 36 * MB, WS_USED = 37 * MB;
constexpr int KV_LAYER = 32 * 1024 + 32 * 32;
constexpr unsigned OFF_ST0 = OFF_SM,
                   OFF_CSP = OFF_ST0 + 16384,
                   OFF_KV = OFF_CSP + 16 * 3072 * 4,
                   OFF_GX = OFF_KV + 2 * KV_LAYER * 4,
                   OFF_GX2 = OFF_GX + 65536,
                   OFF_ST3 = OFF_GX2 + 131072,
                   OFF_FLG = OFF_ST3 + 131072,
                   OFF_EPO = OFF_FLG + 3072 * 32;
constexpr int FL_U = 0, FL_Q = 1024, FL_X1 = 2048, FL_G1 = 2304  , FL_X3 = 2816;
constexpr unsigned CODE_BYTES = 56 * 1024;
constexpr int SPIN_MAX = 1 << 17;
#define WMODE 0

DI float bflo(unsigned u) { return __uint_as_float(u << 16); }
DI float bfhi(unsigned u) { return __uint_as_float(u & 0xffff0000u); }
DI unsigned pk2v(f32x2 v) { return __builtin_bit_cast(unsigned, __builtin_convertvector(v, bf16x2_t)); }
DI unsigned pk2(float a, float b) { return pk2v((f32x2){a, b}); }
DI float rbf(float a) { return bflo(pk2(a, a)); }
DI bf16x8 as_frag(u32x4 v) { return __builtin_bit_cast(bf16x8, v); }
DI f32x2 lo2(f32x4 v) { return (f32x2){v[0], v[1]}; }
DI f32x2 hi2(f32x4 v) { return (f32x2){v[2], v[3]}; }
DI float dot2(unsigned a, unsigned b, float c) {
  return __builtin_amdgcn_fdot2_f32_bf16(__builtin_bit_cast(bf16x2_t, a), __builtin_bit_cast(bf16x2_t, b), c, false);
}
#define MFMA16(a, b, c) __builtin_amdgcn_mfma_f32_16x16x32_bf16((a), (b), (c), 0, 0, 0)
#define MFMA32(a, b, c) __builtin_amdgcn_mfma_f32_32x32x16_bf16((a), (b), (c), 0, 0, 0)
#define WAIT_LGKM0() asm volatile("s_waitcnt lgkmcnt(0)" ::: "memory")
#define BAR() do { __builtin_amdgcn_s_barrier(); asm volatile("" ::: "memory"); } while (0)
#define SB() __builtin_amdgcn_sched_barrier(0)
#define KEEP(x) asm volatile("" ::"v"(x))
template <int N> DI void wait_vm() { asm volatile("s_waitcnt vmcnt(%0)" ::"i"(N) : "memory"); }
DI rsrc_t mk_rsrc(void* p, unsigned bytes) { return __builtin_amdgcn_make_buffer_rsrc(p, 0, bytes, 0x00020000); }
DI void glds16(const void* g, char* l) {
  __builtin_amdgcn_global_load_lds((const __attribute__((address_space(1))) void*)g, (LAS void*)l, 16, 0, 0);
}
DI void dma16(rsrc_t r, char* l, unsigned voff, unsigned soff) { __builtin_amdgcn_raw_ptr_buffer_load_lds(r, (LAS void*)l, 16, voff, soff, 0, 16); }
DI u32x4 ld16(rsrc_t r, unsigned voff, unsigned soff) { return __builtin_amdgcn_raw_buffer_load_b128(r, voff, soff, 16); }
DI u32x2 ld8(rsrc_t r, unsigned voff, unsigned soff) { return __builtin_amdgcn_raw_buffer_load_b64(r, voff, soff, 16); }
DI void st16(rsrc_t r, unsigned off, u32x4 v) { __builtin_amdgcn_raw_buffer_store_b128(v, r, off, 0, 16); }
DI void st8(rsrc_t r, unsigned off, u32x2 v) { __builtin_amdgcn_raw_buffer_store_b64(v, r, off, 0, 16); }
DI void put8(char* p, u32x2 v) { __hip_atomic_store((unsigned long long*)p, __builtin_bit_cast(unsigned long long, v), __ATOMIC_RELAXED, __HIP_MEMORY_SCOPE_AGENT); }
DI u32x2 poll8(const char* p) { return __builtin_bit_cast(u32x2, __hip_atomic_load((const unsigned long long*)p, __ATOMIC_RELAXED, __HIP_MEMORY_SCOPE_AGENT)); }
DI void publish8(char* p, float sm, float sq) {
  unsigned qb = __float_as_uint(sq);
  if (qb == ~0u) qb = 0x7fc00000u;
  put8(p, (u32x2){__float_as_uint(sm), qb});
}
DI float opq(float m) { asm("" : "+v"(m)); return m; }
template <int N> DI float row_ror(float x) {
  return __int_as_float(__builtin_amdgcn_update_dpp(0, __float_as_int(x), 0x120 + N, 0xf, 0xf, false));
}
DI float swap16_sum(float a, float b) { const auto p = __builtin_amdgcn_permlane16_swap(__float_as_uint(a), __float_as_uint(b), false, false); return __uint_as_float(p[0]) + __uint_as_float(p[1]); }
DI float swap32_sum(float a, float b) { const auto p = __builtin_amdgcn_permlane32_swap(__float_as_uint(a), __float_as_uint(b), false, false); return __uint_as_float(p[0]) + __uint_as_float(p[1]); }

DI f32x2 ln_stats(float s, float q) {
  const float m = s * (1.0f / 1024.0f);
  const float v = fmaxf(q * (1.0f / 1024.0f) - m * m, 0.0f);
  const float sd = rbf(sqrtf(rbf(rbf(v) + 1e-5f)));
  return (f32x2){rbf(m), 1.0f / sd};
}
constexpr float LOG2E = 1.44269504088896f;
DI f32x2 phi2(f32x2 t2) {
  const f32x2 a = t2 * LOG2E - 1e-6f * LOG2E;
  f32x2 e, m;
  e[0] = __builtin_amdgcn_fmed3f(__builtin_amdgcn_exp2f(a[0]), 0.0f, 1.0f);
  e[1] = __builtin_amdgcn_fmed3f(__builtin_amdgcn_exp2f(a[1]), 0.0f, 1.0f);
  m[0] = fmaxf(t2[0], 1e-6f);
  m[1] = fmaxf(t2[1], 1e-6f);
  return m + e;
}

constexpr int LDS_BYTES = 163840, LDS_CNT = LDS_BYTES - 64;
constexpr int CV_PAD = 512, CV_HA = 286 * 16, CV_HB = CV_HA + 4160, CV_CH = CV_HB + 4128 + 32;
constexpr int CV_WB = CV_PAD + 4 * CV_CH, CV_X0 = CV_WB + 4 * 8192, CV_ST = CV_X0 + 16384, CV_BN = CV_ST + 16384;
constexpr int G1_AB = 32768, G1_STAGE = G1_AB + 12288, G1_SCR = 3 * G1_STAGE, G1_NP = 11;
constexpr int G2_Z = 131072, G2_RED = G2_Z + 2048;

struct Cx {
  char* ws; char* smem; rsrc_t rw, rc; int lane, w, tid, bid; u32x2 tag;
  DI void mark(int) {}
  DI void endspin() {}
  DI void half_sync(int n) const {
    const unsigned c = (unsigned)(size_t)(LAS char*)(smem + LDS_CNT + (w >> 2) * 16);
    asm volatile("s_waitcnt lgkmcnt(0)" ::: "memory");
    if (lane == 0) asm volatile("ds_add_u32 %0, %1" ::"v"(c), "v"(1u) : "memory");
    unsigned x;
    do { asm volatile("ds_read_b32 %0, %1\n\ts_waitcnt lgkmcnt(0)" : "=v"(x) : "v"(c) : "memory"); } while (__builtin_amdgcn_readfirstlane(x) < 4u * n);
  }
  DI void signal(int idx) const { if (lane == 0) put8(ws + OFF_FLG + idx * 32, tag); }
  DI void await(int idx, bool pend) const {
    const char* fp = ws + OFF_FLG + idx * 32;
#pragma unroll 1
    for (int spin = 0; spin < SPIN_MAX; ++spin) {
      if (pend) { const u32x2 f = poll8(fp); pend = f[0] != tag[0] || f[1] != tag[1]; }
      if (!__any(pend)) break;
      __builtin_amdgcn_s_sleep(1);
    }
    asm volatile("" ::: "memory");
  }
  DI unsigned touch_code() const {
    unsigned x;
    asm volatile("buffer_load_dword %0, %1, %2, 0 offen" : "=v"(x) : "v"(((bid >> 3) * 16 + (lane & 15)) * 128), "s"(rc) : "memory");
    return x;
  }
  DI void fresh() {
    asm volatile("" : "+v"(tid), "+s"(w), "+s"(bid));
    lane = tid & 63;
  }
  DI void set_tag(u32x2 e) {
    tag = (u32x2){__builtin_amdgcn_readfirstlane(e[0]) * 1664525u + 1013904223u, __builtin_amdgcn_readfirstlane(e[1]) * 22695477u + 1u};
  }
};

DI void conv_store(const Cx& c) {
  const int j = (c.bid & 7) * 32 + (c.bid >> 3), t = c.tid + (c.w >> 2) * 256;
  const char* smem = c.smem;
  u32x4 yo[2];
#pragma unroll
  for (int i = 0; i < 2; ++i) {
    const int p = t + 512 * i;
    unsigned yy[4];
#pragma unroll
    for (int ch = 0; ch < 4; ++ch) yy[ch] = *(const unsigned*)(smem + CV_PAD + ch * CV_CH + CV_HA + p * 4);
    yo[i] = (u32x4){__builtin_amdgcn_perm(yy[1], yy[0], 0x05040100u), __builtin_amdgcn_perm(yy[3], yy[2], 0x05040100u),
                    __builtin_amdgcn_perm(yy[1], yy[0], 0x07060302u), __builtin_amdgcn_perm(yy[3], yy[2], 0x07060302u)};
    const u32x4 x0v = *(const u32x4*)(smem + CV_X0 + p * 16);
    const f32x4 stv = *(const f32x4*)(smem + CV_ST + p * 16);
    u32x4 xo;
#pragma unroll
    for (int e = 0; e < 4; ++e) {
      const float m = stv[e & 2], rs = stv[(e & 2) + 1], xa = bflo(x0v[e]), xb = bfhi(x0v[e]);
      xo[e] = pk2(xa + (xa - m) * rs * bflo(yo[i][e]), xb + (xb - m) * rs * bfhi(yo[i][e]));
    }
    st16(c.rw, OFF_X1 + (j * 1024 + p) * 16, xo);
  }
  SB();
#pragma unroll
  for (int i = 0; i < 2; ++i) st16(c.rw, OFF_Y + (j * 1024 + t + 512 * i) * 16, yo[i]);
  wait_vm<2>();
  WAIT_LGKM0();
  BAR();
}

DI void front_m(Cx& c, const uint16_t* k, const uint16_t* v, const uint16_t* shift_k, const uint16_t* ssm_k, const uint16_t* ssm_D) {
  const int lane = c.lane, w = c.w, tid = c.tid, bid = c.bid, s0 = bid * 8, j = (bid & 7) * 32 + (bid >> 3);
  char* smem = c.smem;
  u32x4* bounce = (u32x4*)(smem + CV_BN) + w * 256;
  u32x4 uu[4], hh[4];
  unsigned hn[4];
  float Dc;
  u32x2 e0;
  {
    u32x2 kk[11], vv[8];
#pragma unroll
    for (int r = 0; r < 11; ++r) kk[r] = *(const u32x2*)(k + (size_t)((r < 3) ? max(s0 - 3 + r, 0) : s0 - 3 + r) * D + tid * 4);
#pragma unroll
    for (int r = 0; r < 8; ++r) vv[r] = *(const u32x2*)(v + (size_t)(s0 + r) * D + tid * 4);
    const u32x4 wa = *(const u32x4*)(shift_k + tid * 16), wb = *(const u32x4*)(shift_k + tid * 16 + 8);
    SB();
    const uint16_t* hrow = ssm_k + (size_t)(4 * j + w) * S;
#pragma unroll
    for (int i = 0; i < 4; ++i) {
      const int ch = i * 64 + lane;
      hh[i] = *(const u32x4*)(hrow + ch * 8);
      hn[i] = *(const unsigned*)(hrow + min(ch * 8 + 8, S - 2));
    }
    Dc = bflo((unsigned)ssm_D[4 * j + w]);
    e0 = poll8(c.ws + OFF_EPO);
    SB();
    if (tid < 2) *(unsigned*)(smem + LDS_CNT + tid * 16) = 0u;
    WAIT_LGKM0();
    BAR();
    SB();
    const unsigned hm = bid ? ~0u : 0u;
#pragma unroll
    for (int r = 0; r < 3; ++r) { kk[r][0] &= hm; kk[r][1] &= hm; }
    const int sw = (lane >> 1) & 3;
#pragma unroll
    for (int ch = 0; ch < 4; ++ch) {
      const unsigned w01 = (ch == 0) ? wa[0] : (ch == 1) ? wa[2] : (ch == 2) ? wb[0] : wb[2];
      const unsigned w23 = (ch == 0) ? wa[1] : (ch == 1) ? wa[3] : (ch == 2) ? wb[1] : wb[3];
      const float w0 = bflo(w01), w1 = bfhi(w01), w2 = bflo(w23), w3 = bfhi(w23);
      float kf[11], u[8];
#pragma unroll
      for (int r = 0; r < 11; ++r) kf[r] = (ch & 1) ? bfhi(kk[r][ch >> 1]) : bflo(kk[r][ch >> 1]);
#pragma unroll
      for (int r = 0; r < 8; ++r) {
        const float ks = kf[r] * w3 + kf[r + 1] * w2 + kf[r + 2] * w1 + kf[r + 3] * w0;
        u[r] = ks * ((ch & 1) ? bfhi(vv[r][ch >> 1]) : bflo(vv[r][ch >> 1]));
      }
      bounce[lane * 4 + (ch ^ sw)] = (u32x4){pk2(u[7], u[6]), pk2(u[5], u[4]), pk2(u[3], u[2]), pk2(u[1], u[0])};
    }
#pragma unroll
    for (int i = 0; i < 4; ++i) {
      const int u = 64 * i + lane;
      st16(c.rw, OFF_UB + (bid * 1024 + w * 256 + u) * 16, bounce[(u & ~3) | ((u & 3) ^ ((u >> 3) & 3))]);
    }
    STAMP(0, 2); c.mark(11);
    SB();
  }
  char* my = smem + CV_PAD + w * CV_CH;
  if (lane < 30) *(u32x4*)(my + lane * 16) = (u32x4){0u, 0u, 0u, 0u};
  if (lane >= 32 && lane < 34) *(u32x4*)(my + CV_HA + (lane - 32) * 16) = (u32x4){0u, 0u, 0u, 0u};
  if (lane == 34) *(u32x4*)(my + CV_HB) = (u32x4){0u, 0u, 0u, 0u};
  if (lane == 0) *(u32x4*)(my + CV_HB + 16) = (u32x4){0u, 0u, 0u, hh[0][0] << 16};
#pragma unroll
  for (int i = 0; i < 4; ++i) {
    const int ch = i * 64 + lane;
    *(u32x4*)(my + CV_HA + 32 + ch * 16) = hh[i];
    *(u32x4*)(my + CV_HB + 32 + ch * 16) =
        (u32x4){__builtin_amdgcn_alignbyte(hh[i][1], hh[i][0], 2), __builtin_amdgcn_alignbyte(hh[i][2], hh[i][1], 2),
                __builtin_amdgcn_alignbyte(hh[i][3], hh[i][2], 2), __builtin_amdgcn_alignbyte(hn[i], hh[i][3], 2)};
  }
  asm volatile("" : "+v"(Dc), "+v"(e0));
  c.set_tag(e0);
  wait_vm<0>();
  c.signal(FL_U + w * 256 + bid);
  STAMP(0, 3); c.mark(12);
  c.await(FL_U + (j >> 6) * 256 + w * 64 + lane, true);
  STAMP(0, 4); c.mark(13);
#pragma unroll
  for (int i = 0; i < 4; ++i) uu[i] = ld16(c.rw, ((i * 16 + (lane >> 2)) * 1024 + (lane & 3)) * 16, OFF_UB + (w * 65536 + 4 * j) * 16);
#pragma unroll
  for (int i = 0; i < 4; ++i) *(u32x4*)(smem + CV_PAD + (lane & 3) * CV_CH + (30 + w * 64 + i * 16 + (lane >> 2)) * 16) = uu[i];
  const int t0 = lane & 15, kq = lane >> 4;
  const LAS unsigned* fbk[4];
#pragma unroll
  for (int i = 0; i < 4; ++i) {
    fbk[i] = (const LAS unsigned*)(my + ((t0 & 1) ? CV_HA : CV_HB)) + 4 * kq + ((1 + t0) >> 1) + 256 * i;
    asm volatile("" : "+v"(fbk[i]));
  }
  const char* gb = my + (2 * t0 + 3 - kq) * 16;
  f32x4 acc[8];
#pragma unroll
  for (int b = 0; b < 8; ++b) { acc[b] = (f32x4){0.f, 0.f, 0.f, 0.f}; asm volatile("" : "+v"(acc[b])); }
  c.half_sync(1);
  STAMP(1, 0); STAMP(1, 1); c.mark(14);
  bf16x8 F[2][8], G[2][8];
  u32x2 fh[8];
  auto rdn = [&](int buf, int g0, int n) {
    if (n == 0 || n > 16) { const int g1 = n ? n - 16 : 0; G[buf][g1] = *(const bf16x8*)(gb + (32 * g1 + 4 * (7 - g0)) * 16); }
    else {
      const int e1 = (n - 1) >> 1;
      const LAS unsigned* p = fbk[e1 >> 1] + 128 * (e1 & 1) + 16 * g0;
      if ((n - 1) & 1) F[buf][e1] = as_frag((u32x4){fh[e1][0], fh[e1][1], p[2], p[3]});
      else fh[e1] = (u32x2){p[0], p[1]};
    }
  };
  auto cstep = [&](int cb, int g0n) {
    int n = 0;
#pragma unroll
    for (int g1 = 0; g1 < 8; ++g1)
#pragma unroll
      for (int e1 = 0; e1 + g1 < 8; ++e1) {
        acc[g1 + e1] = MFMA16(F[cb][e1], G[cb][g1], acc[g1 + e1]);
        SB();
        if (n < 24) { rdn(cb ^ 1, g0n, n); SB(); }
        ++n;
      }
  };
#pragma unroll
  for (int n = 0; n < 24; ++n) rdn(0, 0, n);
  for (int gp = 0; gp < 4; ++gp) {
    cstep(0, 1);
    cstep(1, 2);
    gb -= 128;
#pragma unroll
    for (int i = 0; i < 4; ++i) fbk[i] += 32;
  }
  asm volatile("" ::"v"(acc[7]), "v"(acc[0]));
  STAMP(1, 2);
#pragma unroll
  for (int b = 0; b < 8; ++b) {
    const int s = 256 * b + 16 * t0 + 4 * kq;
    const u32x2 ur = *(const u32x2*)(my + (30 + (s >> 3)) * 16 + ((s & 4) ? 0 : 8));
    *(u32x2*)(my + CV_HA + s * 2) = (u32x2){pk2(acc[b][0] + bfhi(ur[1]) * Dc, acc[b][1] + bflo(ur[1]) * Dc),
                                            pk2(acc[b][2] + bfhi(ur[0]) * Dc, acc[b][3] + bflo(ur[0]) * Dc)};
  }
  WAIT_LGKM0();
  BAR();
  STAMP(1, 3); c.mark(15);
  conv_store(c);
  STAMP(1, 6); c.mark(16);
  if (w == 0) c.signal(FL_X1 + bid);
  const unsigned pf = c.touch_code();
  c.await(FL_X1 + w * 64 + lane, true);
  KEEP(pf);
  STAMP(1, 7);
  BAR();
  if (bid == 0 && tid == 0) put8(c.ws + OFF_EPO, c.tag);
}

DI void front_h(Cx& c, const uint16_t* q, const uint16_t* wqkv, const uint16_t* wo) {
  const int lane = c.lane, w4 = c.w - 4, tid = c.tid, bid = c.bid, s0 = bid * 8, j = (bid & 7) * 32 + (bid >> 3);
  char* smem = c.smem;
  const rsrc_t rw = c.rw;
  u32x4* bounce = (u32x4*)(smem + CV_BN) + c.w * 256;
  float* red = (float*)smem;
  const int gw = bid * 4 + w4;
  char* buf = smem + CV_WB + w4 * 8192;
  const int g = lane >> 4, qr = (lane >> 2) & 3, p = lane & 3, rr = lane >> 3;
  const bool isw = gw < 768;
  const int cg = isw ? gw % 48 : (gw - 768) & 15, og = isw ? gw / 48 : (gw - 768) >> 4;
  auto w_loads = [&]() {
    if (isw) {
      const uint16_t* src = wqkv + (size_t)(og * 64 + rr) * 3072 + cg * 64 + (lane & 7) * 8;
#pragma unroll
      for (int i = 0; i < 8; ++i) glds16(src + (size_t)i * 8 * 3072, buf + i * 1024);
    } else {
      const uint16_t* src = wo + (size_t)(og * 64 + (rr & 3) + 16 * (rr >> 2)) * 1024 + cg * 64 + (lane & 7) * 8;
#pragma unroll
      for (int i = 0; i < 8; ++i) glds16(src + (size_t)((i >> 2) * 32 + (i & 3) * 4) * 1024, buf + i * 1024);
    }
  };
  u32x2 qq[8];
#pragma unroll
  for (int r = 0; r < 8; ++r) qq[r] = *(const u32x2*)(q + (size_t)(s0 + r) * D + tid * 4);
  SB();
  u32x2 e0 = poll8(c.ws + OFF_EPO);
  const unsigned pf = c.touch_code();
  if (WMODE == 0) w_loads();
  SB();
  BAR();
  const int sw = (lane >> 1) & 3;
#pragma unroll
  for (int r = 0; r < 4; ++r) bounce[lane * 4 + (r ^ sw)] = (u32x4){qq[2 * r][0], qq[2 * r][1], qq[2 * r + 1][0], qq[2 * r + 1][1]};
#pragma unroll
  for (int i = 0; i < 4; ++i) {
    const int u = 64 * i + lane;
    st16(rw, OFF_X0 + (bid * 1024 + w4 * 256 + u) * 16, bounce[(u & ~3) | ((u & 3) ^ ((u >> 3) & 3))]);
  }
  if (tid < 48) st16(rw, OFF_GX + bid * 768 + tid * 16, (u32x4){~0u, ~0u, ~0u, ~0u});
  SB();
  if (WMODE == 1) w_loads();
  SB();
  float a[16];
#pragma unroll
  for (int r = 0; r < 8; ++r) {
    a[2 * r] = dot2(qq[r][0], 0x3f803f80u, dot2(qq[r][1], 0x3f803f80u, 0.f));
    a[2 * r + 1] = dot2(qq[r][0], qq[r][0], dot2(qq[r][1], qq[r][1], 0.f));
  }
#pragma unroll
  for (int i = 0; i < 8; ++i) a[i] = swap32_sum(a[i], a[i + 8]);
#pragma unroll
  for (int i = 0; i < 4; ++i) a[i] = swap16_sum(a[i], a[i + 4]);
#pragma unroll
  for (int i = 0; i < 4; ++i) { a[i] += row_ror<1>(a[i]); a[i] += row_ror<2>(a[i]); a[i] += row_ror<4>(a[i]); a[i] += row_ror<8>(a[i]); }
  if ((lane & 15) == 0) *(f32x4*)&red[w4 * 16 + (lane >> 4) * 4] = (f32x4){a[0], a[1], a[2], a[3]};
  c.half_sync(1);
  if (tid < 8) {
    const float sm = (red[2 * tid] + red[16 + 2 * tid]) + (red[32 + 2 * tid] + red[48 + 2 * tid]);
    const float sq = (red[2 * tid + 1] + red[17 + 2 * tid]) + (red[33 + 2 * tid] + red[49 + 2 * tid]);
    st8(rw, OFF_ST0 + (s0 + tid) * 8, __builtin_bit_cast(u32x2, ln_stats(sm, sq)));
  }
  asm volatile("" : "+v"(e0));
  c.set_tag(e0);
  wait_vm<0>();
  KEEP(pf);
  c.signal(FL_Q + w4 * 256 + bid);
  STAMPW(0, 5, 4);
  if (WMODE == 2) w_loads();
  {
    const unsigned cm = (unsigned)(size_t)(LAS char*)(smem + LDS_CNT);
    unsigned x;
    do { __builtin_amdgcn_s_sleep(2); asm volatile("ds_read_b32 %0, %1\n\ts_waitcnt lgkmcnt(0)" : "=v"(x) : "v"(cm) : "memory"); } while (__builtin_amdgcn_readfirstlane(x) < 4u);
  }
  c.mark(1);
  if (WMODE == 3) w_loads();
  c.await(FL_Q + w4 * 64 + lane, true);
  c.await(FL_Q + (j >> 6) * 256 + w4 * 64 + lane, true);
  const int hd = (cg & 15) * 2 + (lane >> 5), col = (cg >> 4) * 32 + (lane & 31);
  const unsigned dst = isw ? OFF_W8 + ((hd * 128 + og * 8) * 96 + col) * 16
                           : OFF_WOF + ((((og >> 1) * 32 + cg * 2 + (g >> 1)) * 8 + (og & 1) * 4) * 64 + (lane & 31)) * 16;
  const unsigned dstep = isw ? 1536 : 512;
  const unsigned la = (unsigned)(size_t)(LAS char*)buf + qr * 128 + (isw ? g * 32 + p * 8 : (g >> 1) * 64 + (p & 1) * 32 + ((g & 1) * 2 + (p >> 1)) * 8);
  u32x2 lo[8], hi[8];
#pragma unroll
  for (int i = 0; i < 8; ++i) {
    lo[i] = __builtin_bit_cast(u32x2, __builtin_amdgcn_ds_read_tr16_b64_v4i16((LAS s16x4*)(size_t)(la + i * 1024)));
    hi[i] = __builtin_bit_cast(u32x2, __builtin_amdgcn_ds_read_tr16_b64_v4i16((LAS s16x4*)(size_t)(la + i * 1024 + 512)));
  }
  SB();
#pragma unroll
  for (int i = 0; i < 4; ++i) {
    const int pp = w4 * 256 + i * 64 + lane;
    dma16(rw, smem + CV_X0 + (w4 * 4 + i) * 1024, ((pp >> 2) * 1024 + (pp & 3)) * 16, OFF_X0 + j * 64);
    dma16(rw, smem + CV_ST + (w4 * 4 + i) * 1024, pp * 16, OFF_ST0);
  }
  SB();
#pragma unroll
  for (int i = 0; i < 8; ++i) st16(rw, dst + i * dstep, (u32x4){lo[i][0], lo[i][1], hi[i][0], hi[i][1]});
  if (lane < 16 + 1 - (gw & 1)) st16(rw, OFF_KV + (gw * 33 / 2 + (gw & 1) + lane) * 16, (u32x4){0u, 0u, 0u, 0u});
  if (isw) {
    float c0 = 0.f, c1 = 0.f;
#pragma unroll
    for (int i = 0; i < 8; ++i)
#pragma unroll
      for (int e = 0; e < 2; ++e) {
        c0 = opq(opq(c0 + bflo(lo[i][e])) + bflo(hi[i][e]));
        c1 = opq(opq(c1 + bfhi(lo[i][e])) + bfhi(hi[i][e]));
      }
    __builtin_amdgcn_raw_buffer_store_b32(__float_as_uint(c0 + c1), rw, OFF_CSP + ((hd * 96 + col) * 16 + og) * 4, 0, 16);
  }
  STAMPW(1, 4, 4);
  wait_vm<9>();
  STAMPW(1, 5, 4);
  BAR();
  conv_store(c);
  BAR();
}

template <int L> DI void g1_m(const Cx& c) {
  constexpr bool C4 = L == 0;
  constexpr int kid = 2 + 2 * L;
  const int lane = c.lane, w = c.w, tid = c.tid, xcd = c.bid & 7, idx = c.bid >> 3;
  const int band = (xcd & 1) * 4 + (idx & 3), h = (xcd >> 1) * 8 + (idx >> 2), r = lane & 15, kq = lane >> 4;
  char* smem = c.smem;
  const float* Rs = (const float*)(smem + G1_SCR);
  const float* MRs = Rs + 256;
  const float* CSs = Rs + 512;
  float* P = (float*)(smem + G1_SCR) + 640;
  const char* ab = C4 ? smem + kq * 4096 + ((64 * w + r) ^ (16 * (kq & 1))) * 8 : smem + kq * 4096 + (64 * w + r) * 16;
  const char* ab2 = ab + 128 - (kq & 1) * 256;
  const char* bb = smem + G1_AB + kq * 1536 + r * 16;
  STAMP(kid, 0);
  f32x4 acc[4][6];
#pragma unroll
  for (int m = 0; m < 4; ++m)
#pragma unroll
    for (int p = 0; p < 6; ++p) { acc[m][p] = (f32x4){0.f, 0.f, 0.f, 0.f}; asm volatile("" : "+v"(acc[m][p])); }
  bf16x8 F[2][10];
  auto rd1 = [&](int buf, const char* a, const char* a2, const char* b, int t, int i) {
    if (i < 4) {
      if (C4) {
        const char* p = ((i & 1) ? a2 : a) + t * 16384 + (i >> 1) * 256;
        const u32x2 lo = *(const u32x2*)p, hi = *(const u32x2*)(p + 2048);
        F[buf][i] = as_frag((u32x4){lo[0], lo[1], hi[0], hi[1]});
      } else F[buf][i] = *(const bf16x8*)(a + t * 16384 + i * 256);
    } else F[buf][i] = *(const bf16x8*)(b + t * 6144 + (i - 4) * 256);
  };
  auto step = [&](int cb, const char* a, const char* a2, const char* b, int t, bool rd) {
    const int ord[10] = {0, 4, 5, 6, 7, 8, 9, 1, 2, 3};
#pragma unroll
    for (int i = 0; i < 24; ++i) {
      const int m = i / 6, p = i % 6;
      if (p < 2) acc[m][p] = MFMA16(F[cb][4 + p], F[cb][m], acc[m][p]);
      else acc[m][p] = MFMA16(F[cb][m], F[cb][4 + p], acc[m][p]);
      SB();
      if (rd && (i & 1) && (i >> 1) < 10) { rd1(cb ^ 1, a, a2, b, t, ord[i >> 1]); SB(); }
    }
  };
  BAR();
  STAMP(kid, 1);
#pragma unroll
  for (int i = 0; i < 10; ++i) rd1(0, ab, ab2, bb, 0, i);
  int so = 0;
  for (int it = 0; it < 15; ++it) {
    step(0, ab + so, ab2 + so, bb + so, 1, true);
    WAIT_LGKM0();
    BAR();
    so = (so == 2 * G1_STAGE) ? 0 : so + G1_STAGE;
    step(1, ab + so, ab2 + so, bb + so, 0, true);
  }
  step(0, ab + so, ab2 + so, bb + so, 1, true);
  BAR();
  step(1, ab, ab2, bb, 0, false);
  STAMP(kid, 2);
  f32x4 Z[2][2], Zz[2];
#pragma unroll
  for (int i = 0; i < 2; ++i) { Z[i][0] = (f32x4){0.f, 0.f, 0.f, 0.f}; Z[i][1] = Z[i][0]; Zz[i] = Z[i][0]; }
  {
    float ck[4];
#pragma unroll
    for (int p = 0; p < 4; ++p) ck[p] = CSs[32 + p * 16 + r];
    const bf16x8 ones = as_frag((u32x4){0x3f803f80u, 0x3f803f80u, 0x3f803f80u, 0x3f803f80u});
#pragma unroll
    for (int jj = 0; jj < 2; ++jj) {
      u32x4 fr[4];
#pragma unroll
      for (int mm = 0; mm < 2; ++mm) {
        const int m = 2 * jj + mm;
        const f32x4 rs = *(const f32x4*)(Rs + 64 * w + 16 * m + 4 * kq), mr = *(const f32x4*)(MRs + 64 * w + 16 * m + 4 * kq);
#pragma unroll
        for (int p = 0; p < 2; ++p) {
          fr[p][2 * mm] = pk2v(phi2(lo2(acc[m][2 + p]) * lo2(rs) + (lo2(mr) * ck[p] + 1e-6f)));
          fr[p][2 * mm + 1] = pk2v(phi2(hi2(acc[m][2 + p]) * hi2(rs) + (hi2(mr) * ck[p] + 1e-6f)));
          fr[2 + p][2 * mm] = pk2v(lo2(acc[m][4 + p]) * lo2(rs) + lo2(mr) * ck[2 + p]);
          fr[2 + p][2 * mm + 1] = pk2v(hi2(acc[m][4 + p]) * hi2(rs) + hi2(mr) * ck[2 + p]);
        }
      }
#pragma unroll
      for (int dh = 0; dh < 2; ++dh) {
        Z[0][dh] = MFMA16(as_frag(fr[dh]), as_frag(fr[2]), Z[0][dh]);
        Z[1][dh] = MFMA16(as_frag(fr[dh]), as_frag(fr[3]), Z[1][dh]);
        Zz[dh] = MFMA16(as_frag(fr[dh]), ones, Zz[dh]);
      }
    }
  }
  {
    float* mine = P + w * 1056;
#pragma unroll
    for (int i = 0; i < 4; ++i) *(f32x4*)(mine + i * 256 + lane * 4) = Z[i >> 1][i & 1];
    if (r == 0) { *(f32x4*)(mine + 1024 + 4 * kq) = Zz[0]; *(f32x4*)(mine + 1040 + 4 * kq) = Zz[1]; }
  }
  c.half_sync(2 * L + 2);
  STAMP(kid, 3);
  float* kvacc = (float*)(c.ws + OFF_KV) + L * KV_LAYER;
#pragma unroll
  for (int i = 0; i < 4; ++i) {
    const int e = tid + 256 * i;
    atomicAdd(kvacc + h * 1024 + e, (P[e] + P[1056 + e]) + (P[2112 + e] + P[3168 + e]));
  }
  if (tid < 32) atomicAdd(kvacc + 32768 + h * 32 + tid, (P[1024 + tid] + P[2080 + tid]) + (P[3136 + tid] + P[4192 + tid]));
  SB();
  {
    const f32x4 c0 = *(const f32x4*)(CSs + 4 * kq), c1 = *(const f32x4*)(CSs + 16 + 4 * kq);
#pragma unroll
    for (int m = 0; m < 4; ++m) {
      const float rs = Rs[64 * w + 16 * m + r], mr = MRs[64 * w + 16 * m + r];
      const u32x4 o = {pk2v(phi2(lo2(acc[m][0]) * rs + (lo2(c0) * mr + 1e-6f))), pk2v(phi2(hi2(acc[m][0]) * rs + (hi2(c0) * mr + 1e-6f))),
                       pk2v(phi2(lo2(acc[m][1]) * rs + (lo2(c1) * mr + 1e-6f))), pk2v(phi2(hi2(acc[m][1]) * rs + (hi2(c1) * mr + 1e-6f)))};
      st16(c.rw, OFF_PQ + L * 4 * MB + ((((h >> 2) * 128 + band * 16 + w * 4 + m) * 4 + (h & 3)) * 64 + lane) * 16, o);
    }
  }
  STAMP(kid, 6);
  wait_vm<0>();
  c.half_sync(2 * L + 3);
  STAMP(kid, 7);
  if (w == 0) c.signal(FL_G1 + L * 256 + h * 8 + band);
}

template <int L> DI void g1_h(const Cx& c) {
  constexpr bool C4 = L == 0;
  constexpr unsigned OFF_X = C4 ? OFF_X1 : OFF_X3;
  const int lane = c.lane, lw = c.w - 4, t = c.tid, xcd = c.bid & 7, idx = c.bid >> 3;
  const int band = (xcd & 1) * 4 + (idx & 3), h = (xcd >> 1) * 8 + (idx >> 2);
  char* smem = c.smem;
  char* ws = c.ws;
  const rsrc_t rw = c.rw;
  float* Rs = (float*)(smem + G1_SCR);
  const unsigned l0 = lane * 16, l1 = (lane ^ 8) * 16;
  auto issue = [&](int st) {
    char* base = smem + (st % 3) * G1_STAGE;
#pragma unroll
    for (int p = 0; p < 8; ++p) {
      if (C4) dma16(rw, base + (8 * lw + p) * 1024, (p & 4) ? l1 : l0, OFF_X + ((st * 16 + lw * 4 + (p >> 1)) * 1024 + band * 128 + (p & 1) * 64) * 16);
      else dma16(rw, base + (8 * lw + p) * 1024, l0, OFF_X + ((st * 8 + lw * 2 + (p >> 2)) * S + band * 256 + (p & 3) * 64) * 16);
    }
#pragma unroll
    for (int p = 0; p < 3; ++p) dma16(rw, base + G1_AB + (3 * lw + p) * 1024, l0, OFF_W8 + (h * 128 + st * 8) * 1536 + (3 * lw + p) * 1024);
  };
  issue(0); issue(1);
  wait_vm<G1_NP>();
  BAR();
  issue(2);
  u32x4 xv[4];
  float v[4] = {0.f, 0.f, 0.f, 0.f};
  if (C4) {
    const u32x4* xp = (const u32x4*)(ws + OFF_X) + (size_t)(t >> 2) * 1024 + band * 128 + h * 4 + (t & 3);
    asm volatile("global_load_dwordx4 %0, %4, off sc1\n\tglobal_load_dwordx4 %1, %5, off sc1\n\tglobal_load_dwordx4 %2, %6, off sc1\n\tglobal_load_dwordx4 %3, %7, off sc1"
                 : "=&v"(xv[0]), "=&v"(xv[1]), "=&v"(xv[2]), "=&v"(xv[3]) : "v"(xp), "v"(xp + 65536), "v"(xp + 131072), "v"(xp + 196608) : "memory");
  }
#pragma unroll
  for (int i = 0; i < 13; ++i) {
    if (C4 && i < 2) wait_vm<G1_NP + 4>();
    else if (C4 && i == 2) asm volatile("s_waitcnt vmcnt(%4)" : "+v"(xv[0]), "+v"(xv[1]), "+v"(xv[2]), "+v"(xv[3]) : "i"(G1_NP) : "memory");
    else wait_vm<G1_NP>();
    BAR();
    issue(i + 3);
    if (C4 && i >= 2 && i < 6) {
      const u32x4 x = xv[i - 2];
#pragma unroll
      for (int e = 0; e < 4; ++e) {
        const float lo = bflo(x[e]), hi = bfhi(x[e]);
        v[e & 2] = opq(v[e & 2] + lo); v[(e & 2) + 1] = opq(fmaf(lo, lo, v[(e & 2) + 1]));
        v[e & 2] = opq(v[e & 2] + hi); v[(e & 2) + 1] = opq(fmaf(hi, hi, v[(e & 2) + 1]));
      }
      asm volatile("" : "+v"(v[0]), "+v"(v[1]), "+v"(v[2]), "+v"(v[3]));
    }
    if (C4 && i == 6) {
#pragma unroll
      for (int e = 0; e < 4; ++e) { v[e] += row_ror<4>(v[e]); v[e] += row_ror<8>(v[e]); v[e] = swap16_sum(v[e], v[e]); v[e] = swap32_sum(v[e], v[e]); }
      const bool up = (lane & 4) != 0;
      if (lane < 8) publish8(ws + OFF_GX + ((band * 4 + lw) * 256 + h * 8 + 2 * (lane & 3) + (lane >> 2)) * 8, up ? v[2] : v[0], up ? v[3] : v[1]);
    }
  }
  const char* gp = ws + (C4 ? OFF_GX + (band * 1024 + t) * 8 + 4096 : OFF_ST3 + ((band * 4 + lw) * 512 + lane) * 8);
  const char* cp = ws + OFF_CSP + (h * 96 + (t < 96 ? t : 0)) * 64;
  u32x2 g[8];
  f32x4 c0, c1, c2, c3;
  asm volatile("global_load_dwordx4 %0, %4, off sc1\n\tglobal_load_dwordx4 %1, %4, off offset:16 sc1\n\t"
               "global_load_dwordx4 %2, %4, off offset:32 sc1\n\tglobal_load_dwordx4 %3, %4, off offset:48 sc1"
               : "=&v"(c0), "=&v"(c1), "=&v"(c2), "=&v"(c3) : "v"(cp) : "memory");
  if (C4) {
    asm volatile("global_load_dwordx2 %0, %4, off offset:-4096 sc1\n\tglobal_load_dwordx2 %1, %4, off offset:-2048 sc1\n\t"
                 "global_load_dwordx2 %2, %4, off sc1\n\tglobal_load_dwordx2 %3, %4, off offset:2048 sc1"
                 : "=&v"(g[0]), "=&v"(g[1]), "=&v"(g[2]), "=&v"(g[3]) : "v"(gp) : "memory");
    wait_vm<G1_NP + 8>();
  } else {
    asm volatile("global_load_dwordx2 %0, %8, off sc1\n\tglobal_load_dwordx2 %1, %8, off offset:512 sc1\n\tglobal_load_dwordx2 %2, %8, off offset:1024 sc1\n\t"
                 "global_load_dwordx2 %3, %8, off offset:1536 sc1\n\tglobal_load_dwordx2 %4, %8, off offset:2048 sc1\n\tglobal_load_dwordx2 %5, %8, off offset:2560 sc1\n\t"
                 "global_load_dwordx2 %6, %8, off offset:3072 sc1\n\tglobal_load_dwordx2 %7, %8, off offset:3584 sc1"
                 : "=&v"(g[0]), "=&v"(g[1]), "=&v"(g[2]), "=&v"(g[3]), "=&v"(g[4]), "=&v"(g[5]), "=&v"(g[6]), "=&v"(g[7]) : "v"(gp) : "memory");
    wait_vm<G1_NP + 12>();
  }
  BAR();
  if (C4) asm volatile("s_waitcnt vmcnt(0)" : "+v"(g[0]), "+v"(g[1]), "+v"(g[2]), "+v"(g[3]), "+v"(c0), "+v"(c1), "+v"(c2), "+v"(c3)::"memory");
  else asm volatile("s_waitcnt vmcnt(0)" : "+v"(g[0]), "+v"(g[1]), "+v"(g[2]), "+v"(g[3]), "+v"(g[4]), "+v"(g[5]), "+v"(g[6]), "+v"(g[7]),
                    "+v"(c0), "+v"(c1), "+v"(c2), "+v"(c3)::"memory");
  BAR();
  if (C4) {
#pragma unroll 1
    for (int spin = 0; spin < SPIN_MAX; ++spin) {
      if (!__any(g[0][1] == ~0u || g[1][1] == ~0u || g[2][1] == ~0u || g[3][1] == ~0u)) break;
      __builtin_amdgcn_s_sleep(4);
#pragma unroll
      for (int k = 0; k < 4; ++k) g[k] = poll8(gp + (k - 2) * 2048);
    }
  }
  float sm = 0.f, sq = 0.f, cs = 0.f;
#pragma unroll
  for (int k = 0; k < (C4 ? 4 : 8); ++k) { sm = opq(sm + __uint_as_float(g[k][0])); sq = opq(sq + __uint_as_float(g[k][1])); }
#pragma unroll
  for (int k = 0; k < 4; ++k) cs = opq(opq(opq(opq(cs + c0[k]) + c1[k]) + c2[k]) + c3[k]);
  const f32x2 st = ln_stats(sm, sq);
  Rs[t] = st[1]; Rs[256 + t] = -st[0] * st[1];
  if (t < 96) Rs[512 + t] = cs;
  WAIT_LGKM0();
  BAR();
}

template <int L> DI void g2_m(const Cx& c) {
  constexpr int kid = 3 + 2 * L;
  constexpr unsigned KVO = OFF_KV + L * KV_LAYER * 4;
  const int lane = c.lane, w = c.w, xcd = c.bid & 7, idx = c.bid >> 3, sb = (xcd >> 1) * 8 + (idx & 7), kq = lane >> 4;
  const rsrc_t rw = c.rw;
  const unsigned l16 = lane * 16;
  u32x4* O = (u32x4*)c.smem;
  unsigned pf = c.touch_code();
  c.await(FL_G1 + L * 256 + ((lane >> 3) * 4 + w) * 8 + (lane & 7), true);
  KEEP(pf);
  STAMP(kid, 0);
  constexpr int PL = 2, PD = PL + 1;
  u32x4 pq[PD][4];
  f32x4 kv[PD][4];
  const unsigned spq = OFF_PQ + L * 4 * MB + (sb * 16 + w) * 1024, skv = KVO + w * 4096;
  auto ld = [&](int i, int n) {
    if (n < 4) kv[i % PD][n] = __builtin_bit_cast(f32x4, ld16(rw, l16 + n * 1024, skv + i * 16384));
    else pq[i % PD][n - 4] = ld16(rw, l16, spq + (i * 512 + (n - 4) * 4) * 1024);
    SB();
  };
  const f32x4 zall = __builtin_bit_cast(f32x4, ld16(rw, (lane >> 3) * 512 + (lane & 7) * 16, KVO + 131072 + w * 128));
  SB();
#pragma unroll
  for (int n = 0; n < 8 * PL; ++n) ld(n >> 3, n & 7);
  u32x2* Zs = (u32x2*)(c.smem + G2_Z) + w * 64;
  Zs[lane] = (u32x2){pk2v(lo2(zall)), pk2v(hi2(zall))};
  const f32x4 zero = {0.f, 0.f, 0.f, 0.f}, eps = {1e-6f, 1e-6f, 1e-6f, 1e-6f};
  bf16x8 k0, k1, zb;
  auto prep = [&](int i) {
    const int b = i % PD;
    const u32x2 za = Zs[i * 8 + kq], zc = Zs[i * 8 + 4 + kq];
    k0 = as_frag((u32x4){pk2v(lo2(kv[b][0])), pk2v(hi2(kv[b][0])), pk2v(lo2(kv[b][1])), pk2v(hi2(kv[b][1]))});
    k1 = as_frag((u32x4){pk2v(lo2(kv[b][2])), pk2v(hi2(kv[b][2])), pk2v(lo2(kv[b][3])), pk2v(hi2(kv[b][3]))});
    zb = as_frag((u32x4){za[0], za[1], zc[0], zc[1]});
    asm volatile("" : "+v"(k0), "+v"(k1), "+v"(zb));
  };
  prep(0);
#pragma unroll
  for (int i = 0; i < 8; ++i) {
    const int b = i % PD;
    f32x4 x0[4], x1[4], dn[4];
    __builtin_amdgcn_s_setprio(2);
#pragma unroll
    for (int st = 0; st < 4; ++st) {
      dn[st] = MFMA16(zb, as_frag(pq[b][st]), eps);
      x0[st] = MFMA16(k0, as_frag(pq[b][st]), zero);
      x1[st] = MFMA16(k1, as_frag(pq[b][st]), zero);
    }
    __builtin_amdgcn_s_setprio(0);
    SB();
#pragma unroll
    for (int st = 0; st < 4; ++st) {
      const float inv = __builtin_amdgcn_rcpf(dn[st][0]);
      O[((i * 4 + w) * 4 + st) * 64 + lane] =
          (u32x4){pk2(opq(x0[st][0] * inv), opq(x0[st][1] * inv)), pk2(opq(x0[st][2] * inv), opq(x0[st][3] * inv)),
                  pk2(opq(x1[st][0] * inv), opq(x1[st][1] * inv)), pk2(opq(x1[st][2] * inv), opq(x1[st][3] * inv))};
      SB();
      if (i + PL < 8) { ld(i + PL, 2 * st); ld(i + PL, 2 * st + 1); }
    }
    if (i < 7) prep(i + 1);
    WAIT_LGKM0();
    BAR();
    if (i == 0) STAMP(kid, 1);
  }
  if (L == 0) {
    const int band = (xcd & 1) * 4 + (idx & 3);
    pf = c.touch_code();
    if (w == 0) c.await(FL_X3 + band * 32 + (lane & 31), lane < 32);
    wait_vm<0>();
    KEEP(pf);
    STAMP(kid, 5);
    BAR();
  }
}

template <int L> DI void g2_h(const Cx& cx, char* out) {
  constexpr int kid = 3 + 2 * L;
  const int lane = cx.lane, c = cx.w - 4, xcd = cx.bid & 7, idx = cx.bid >> 3;
  const int sb = (xcd >> 1) * 8 + (idx & 7), nb = (xcd & 1) * 4 + (idx >> 3);
  const int n0 = nb * 128 + c * 32, s0 = sb * 64, r = lane & 31, hf = lane >> 5;
  const rsrc_t rw = cx.rw;
  const unsigned l16 = lane * 16;
  constexpr int CD = 5, LA = 6;
  u32x4 wo[CD][8], of[8];
  const unsigned swo = OFF_WOF + (nb * 4 + c) * 8192;
  auto ldw = [&](int i, int n) { wo[i % CD][n] = ld16(rw, l16 + (n & 3) * 1024, swo + i * 262144 + (n >> 2) * 4096); SB(); };
#pragma unroll
  for (int n = 0; n < 8 * CD; ++n) ldw(n >> 3, n & 7);
  f32x16 acc[2];
#pragma unroll
  for (int i = 0; i < 16; ++i) { acc[0][i] = 0.f; acc[1][i] = 0.f; }
  asm volatile("" : "+v"(acc[0]), "+v"(acc[1]));
  u32x4 res[2][2], yv[2][2];
  auto ldres = [&](int k) {
    const int s2 = (k >> 1) & 1, g = k & 1;
    if (L == 1) { if (k < 4) res[s2][g] = ld16(rw, (2 * hf * S + r) * 16 + s2 * 512, OFF_X3 + (((n0 >> 3) + g) * S + s0) * 16); }
    else {
      const unsigned so = (k < 4 ? OFF_X1 : OFF_Y) + (((n0 >> 2) + 2 * g) * S + s0) * 8, vo = (4 * hf * S + r) * 8 + s2 * 256;
      const u32x2 lo = ld8(rw, vo, so), hi = ld8(rw, vo, so + S * 8);
      if (k < 4) res[s2][g] = (u32x4){lo[0], lo[1], hi[0], hi[1]}; else yv[s2][g] = (u32x4){lo[0], lo[1], hi[0], hi[1]};
    }
    SB();
  };
  const u32x4* Ob = (const u32x4*)cx.smem + (r >> 4) * 64 + hf * 16 + (r & 15);
  auto rdo = [&](int f) { const int u = f & 15; of[f & 7] = Ob[((f >> 4) * 16 + (u >> 2) * 4 + (u & 1) * 2) * 64 + ((u >> 1) & 1) * 32]; SB(); };
  __builtin_amdgcn_s_setprio(1);
  BAR();
#pragma unroll
  for (int f = 0; f < LA; ++f) rdo(f);
#pragma unroll
  for (int i = 0; i < 8; ++i)
#pragma unroll
    for (int n = 0; n < 16; ++n) {
      const int f = i * 16 + n;
      acc[n & 1] = MFMA32(as_frag(wo[i % CD][n >> 1]), as_frag(of[f & 7]), acc[n & 1]);
      SB();
      if (f + LA < 128) { if (((f + LA) & 15) == 0) BAR(); rdo(f + LA); }
      if ((n & 1) && i + CD < 8) ldw(i + CD, n >> 1);
      if (i == 5 && (n & 1)) ldres(n >> 1);
    }
  __builtin_amdgcn_s_setprio(0);
  STAMPW(kid, 2, 4);
  u32x4 x[2][2];
#pragma unroll
  for (int s2 = 0; s2 < 2; ++s2)
#pragma unroll
    for (int g = 0; g < 2; ++g)
#pragma unroll
      for (int e = 0; e < 4; ++e) x[s2][g][e] = pk2(bflo(res[s2][g][e]) + acc[s2][8 * g + 2 * e], bfhi(res[s2][g][e]) + acc[s2][8 * g + 2 * e + 1]);
  if (L == 1) {
#pragma unroll
    for (int s2 = 0; s2 < 2; ++s2)
#pragma unroll
      for (int g = 0; g < 2; ++g) *(u32x4*)(out + ((size_t)(s0 + 32 * s2 + r) * D + n0 + 16 * hf + 8 * g) * 2) = x[s2][g];
    STAMPW(kid, 6, 4); DRAIN(); STAMPW(kid, 7, 4);
    return;
  }
  f32x2* Red = (f32x2*)(cx.smem + G2_RED);
  auto rowpart = [&]() {
    float v[4] = {0.f, 0.f, 0.f, 0.f};
#pragma unroll
    for (int e = 0; e < 8; ++e)
#pragma unroll
      for (int s2 = 0; s2 < 2; ++s2) { const unsigned u = x[s2][e >> 2][e & 3]; v[2 * s2] = dot2(u, 0x3f803f80u, v[2 * s2]); v[2 * s2 + 1] = dot2(u, u, v[2 * s2 + 1]); }
    return (f32x2){swap32_sum(v[0], v[2]), swap32_sum(v[1], v[3])};
  };
  Red[c * 64 + lane] = rowpart();
  cx.half_sync(2);
  char* gx = cx.ws + OFF_GX2 + (sb * 512 + lane) * 8;
  if (c == 0) {
    const f32x2 t = (Red[lane] + Red[64 + lane]) + (Red[128 + lane] + Red[192 + lane]);
    publish8(gx + nb * 512, t[0], t[1]);
  }
  STAMPW(kid, 4, 4);
  float sm, sq;
#pragma unroll 1
  for (int spin = 0; spin < SPIN_MAX; ++spin) {
    u32x2 g[8];
#pragma unroll
    for (int k = 0; k < 8; ++k) g[k] = poll8(gx + k * 512);
    bool empty = false;
    sm = 0.f; sq = 0.f;
#pragma unroll
    for (int k = 0; k < 8; ++k) { empty |= g[k][1] == ~0u; sm += __uint_as_float(g[k][0]); sq += __uint_as_float(g[k][1]); }
    if (!__any(empty)) break;
    __builtin_amdgcn_s_sleep(1);
  }
  STAMPW(kid, 3, 4);
  const f32x2 st = ln_stats(sm, sq);
  const auto mm = __builtin_amdgcn_permlane32_swap(__float_as_uint(st[0]), __float_as_uint(st[0]), false, false);
  const auto rr = __builtin_amdgcn_permlane32_swap(__float_as_uint(st[1]), __float_as_uint(st[1]), false, false);
#pragma unroll
  for (int s2 = 0; s2 < 2; ++s2) {
    const float mu = __uint_as_float(mm[s2]), rs = __uint_as_float(rr[s2]);
#pragma unroll
    for (int g = 0; g < 2; ++g) {
#pragma unroll
      for (int e = 0; e < 4; ++e) {
        const float a = bflo(x[s2][g][e]), b = bfhi(x[s2][g][e]);
        x[s2][g][e] = pk2(a + (a - mu) * rs * bflo(yv[s2][g][e]), b + (b - mu) * rs * bfhi(yv[s2][g][e]));
      }
      st16(rw, OFF_X3 + (((n0 >> 3) + 2 * hf + g) * S + s0 + 32 * s2 + r) * 16, x[s2][g]);
    }
  }
  Red[256 + c * 64 + lane] = rowpart();
  cx.half_sync(3);
  if (c == 0) {
    const f32x2 t = (Red[256 + lane] + Red[320 + lane]) + (Red[384 + lane] + Red[448 + lane]);
    st8(rw, OFF_ST3 + ((sb * 8 + nb) * 64 + lane) * 8, __builtin_bit_cast(u32x2, t));
  }
  STAMPW(kid, 6, 4);
  wait_vm<0>();
  cx.half_sync(4);
  if (c == 0) cx.signal(FL_X3 + sb * 8 + nb);
  STAMPW(kid, 7, 4);
  BAR();
}

__global__ __launch_bounds__(512, 2) void fused_kernel(const uint16_t* __restrict__ q, const uint16_t* __restrict__ k,
                                                       const uint16_t* __restrict__ v, const uint16_t* __restrict__ shift_k,
                                                       const uint16_t* __restrict__ ssm_k, const uint16_t* __restrict__ ssm_D,
                                                       const uint16_t* __restrict__ wqkv, const uint16_t* __restrict__ wo,
                                                       char* __restrict__ ws, char* __restrict__ out) {
  extern __shared__ __attribute__((aligned(16))) char smem[];
  unsigned long long pc;
  asm volatile("s_getpc_b64 %0" : "=s"(pc));
  STAMP(0, 0);
  Cx c;
  c.ws = ws; c.smem = smem; c.rw = mk_rsrc(ws, WS_USED); c.rc = mk_rsrc((void*)(pc & ~255ull), CODE_BYTES);
  c.lane = threadIdx.x & 63; c.w = __builtin_amdgcn_readfirstlane(threadIdx.x >> 6); c.tid = threadIdx.x & 255; c.bid = blockIdx.x;
  if (c.w < 4) {
    c.mark(0);
    front_m(c, k, v, shift_k, ssm_k, ssm_D); c.mark(17);
    c.fresh(); g1_m<0>(c); c.fresh(); g2_m<0>(c);
    c.fresh(); g1_m<1>(c); c.fresh(); g2_m<1>(c);
    c.endspin();
  } else {
    c.mark(0);
    front_h(c, q, wqkv, wo); c.mark(2);
    c.fresh(); g1_h<0>(c); c.mark(3); c.fresh(); g2_h<0>(c, out); c.mark(4);
    c.fresh(); g1_h<1>(c); c.mark(5); c.fresh(); g2_h<1>(c, out); c.mark(6);
    c.endspin();
  }
}

extern "C" void kernel_launch(void* const* d_in, const int* in_sizes, int n_in, void* d_out, int out_size, void* d_ws,
                              size_t ws_size, hipStream_t stream) {
  (void)hipFuncSetAttribute((const void*)fused_kernel, hipFuncAttributeMaxDynamicSharedMemorySize, LDS_BYTES);
  fused_kernel<<<256, 512, LDS_BYTES, stream>>>((const uint16_t*)d_in[0], (const uint16_t*)d_in[1], (const uint16_t*)d_in[2],
                                                (const uint16_t*)d_in[3], (const uint16_t*)d_in[4], (const uint16_t*)d_in[5],
                                                (const uint16_t*)d_in[7], (const uint16_t*)d_in[8], (char*)d_ws, (char*)d_out);
}
```

```cpp
#include <hip/hip_runtime.h>
#include <stdint.h>

typedef __attribute__((ext_vector_type(8))) short bf16x8;
typedef __attribute__((ext_vector_type(4))) short s16x4;
typedef __attribute__((ext_vector_type(4))) float f32x4;
typedef __attribute__((ext_vector_type(2))) float f32x2;
typedef __attribute__((ext_vector_type(16))) float f32x16;
typedef __attribute__((ext_vector_type(4))) unsigned u32x4;
typedef __attribute__((ext_vector_type(2))) unsigned u32x2;
typedef __attribute__((ext_vector_type(2))) __bf16 bf16x2_t;
typedef __amdgpu_buffer_rsrc_t rsrc_t;
#define DI __device__ __forceinline__
#define LAS __attribute__((address_space(3)))
#define STAMPW(kid, slot, wv) do {} while (0)
#define DRAIN() do {} while (0)
#define STAMP(kid, slot) STAMPW(kid, slot, 0)

constexpr int S = 2048, D = 1024;
constexpr unsigned MB = 1024 * 1024;
constexpr unsigned OFF_UB = 0,
                   OFF_X0 = 4 * MB,
                   OFF_W8 = 8 * MB,
                   OFF_WOF = 14 * MB,
                   OFF_Y = 16 * MB,
                   OFF_X1 = 20 * MB,
                   OFF_PQ = 24 * MB,
                   OFF_X3 = 32 * MB,
                   OFF_SM = 36 * MB, WS_USED = 38 * MB;
constexpr int KV_LAYER = 32 * 1024 + 32 * 32;
constexpr unsigned OFF_ST0 = OFF_SM,
                   OFF_CSP = OFF_ST0 + 16384,
                   OFF_KV = OFF_CSP + 16 * 3072 * 4,
                   OFF_GX = OFF_KV + 2 * KV_LAYER * 4,
                   OFF_GX2 = OFF_GX + 65536,
                   OFF_ST3 = OFF_GX2 + 131072,
                   OFF_EPO = OFF_ST3 + 131072,
                   OFF_FLG = 37 * MB;
constexpr int FL_U = 0, FL_Q = 1024, FL_X1 = 2048, FL_G1 = 2304  , FL_X3 = 2816;
constexpr unsigned CODE_BYTES = 56 * 1024;
constexpr int SPIN_MAX = 1 << 17;
#define FREP 8
#define PDELAY 20
#define XDELAY 16
#define PSLEEP 8
#define WMODE 0

DI float bflo(unsigned u) { return __uint_as_float(u << 16); }
DI float bfhi(unsigned u) { return __uint_as_float(u & 0xffff0000u); }
DI unsigned pk2v(f32x2 v) { return __builtin_bit_cast(unsigned, __builtin_convertvector(v, bf16x2_t)); }
DI unsigned pk2(float a, float b) { return pk2v((f32x2){a, b}); }
DI float rbf(float a) { return bflo(pk2(a, a)); }
DI bf16x8 as_frag(u32x4 v) { return __builtin_bit_cast(bf16x8, v); }
DI f32x2 lo2(f32x4 v) { return (f32x2){v[0], v[1]}; }
DI f32x2 hi2(f32x4 v) { return (f32x2){v[2], v[3]}; }
DI float dot2(unsigned a, unsigned b, float c) {
  return __builtin_amdgcn_fdot2_f32_bf16(__builtin_bit_cast(bf16x2_t, a), __builtin_bit_cast(bf16x2_t, b), c, false);
}
#define MFMA16(a, b, c) __builtin_amdgcn_mfma_f32_16x16x32_bf16((a), (b), (c), 0, 0, 0)
#define MFMA32(a, b, c) __builtin_amdgcn_mfma_f32_32x32x16_bf16((a), (b), (c), 0, 0, 0)
#define WAIT_LGKM0() asm volatile("s_waitcnt lgkmcnt(0)" ::: "memory")
#define BAR() do { __builtin_amdgcn_s_barrier(); asm volatile("" ::: "memory"); } while (0)
#define SB() __builtin_amdgcn_sched_barrier(0)
#define KEEP(x) asm volatile("" ::"v"(x))
template <int N> DI void wait_vm() { asm volatile("s_waitcnt vmcnt(%0)" ::"i"(N) : "memory"); }
DI rsrc_t mk_rsrc(void* p, unsigned bytes) { return __builtin_amdgcn_make_buffer_rsrc(p, 0, bytes, 0x00020000); }
DI void glds16(const void* g, char* l) {
  __builtin_amdgcn_global_load_lds((const __attribute__((address_space(1))) void*)g, (LAS void*)l, 16, 0, 0);
}
DI void dma16(rsrc_t r, char* l, unsigned voff, unsigned soff) { __builtin_amdgcn_raw_ptr_buffer_load_lds(r, (LAS void*)l, 16, voff, soff, 0, 16); }
DI u32x4 ld16(rsrc_t r, unsigned voff, unsigned soff) { return __builtin_amdgcn_raw_buffer_load_b128(r, voff, soff, 16); }
DI u32x2 ld8(rsrc_t r, unsigned voff, unsigned soff) { return __builtin_amdgcn_raw_buffer_load_b64(r, voff, soff, 16); }
DI void st16(rsrc_t r, unsigned off, u32x4 v) { __builtin_amdgcn_raw_buffer_store_b128(v, r, off, 0, 16); }
DI void st8(rsrc_t r, unsigned off, u32x2 v) { __builtin_amdgcn_raw_buffer_store_b64(v, r, off, 0, 16); }
DI void put8(char* p, u32x2 v) { __hip_atomic_store((unsigned long long*)p, __builtin_bit_cast(unsigned long long, v), __ATOMIC_RELAXED, __HIP_MEMORY_SCOPE_AGENT); }
DI u32x2 poll8(const char* p) { return __builtin_bit_cast(u32x2, __hip_atomic_load((const unsigned long long*)p, __ATOMIC_RELAXED, __HIP_MEMORY_SCOPE_AGENT)); }
DI void publish8(char* p, float sm, float sq) {
  unsigned qb = __float_as_uint(sq);
  if (qb == ~0u) qb = 0x7fc00000u;
  put8(p, (u32x2){__float_as_uint(sm), qb});
}
DI float opq(float m) { asm("" : "+v"(m)); return m; }
template <int N> DI float row_ror(float x) {
  return __int_as_float(__builtin_amdgcn_update_dpp(0, __float_as_int(x), 0x120 + N, 0xf, 0xf, false));
}
DI float swap16_sum(float a, float b) { const auto p = __builtin_amdgcn_permlane16_swap(__float_as_uint(a), __float_as_uint(b), false, false); return __uint_as_float(p[0]) + __uint_as_float(p[1]); }
DI float swap32_sum(float a, float b) { const auto p = __builtin_amdgcn_permlane32_swap(__float_as_uint(a), __float_as_uint(b), false, false); return __uint_as_float(p[0]) + __uint_as_float(p[1]); }

DI f32x2 ln_stats(float s, float q) {
  const float m = s * (1.0f / 1024.0f);
  const float v = fmaxf(q * (1.0f / 1024.0f) - m * m, 0.0f);
  const float sd = rbf(sqrtf(rbf(rbf(v) + 1e-5f)));
  return (f32x2){rbf(m), 1.0f / sd};
}
constexpr float LOG2E = 1.44269504088896f;
DI f32x2 phi2(f32x2 t2) {
  const f32x2 a = t2 * LOG2E - 1e-6f * LOG2E;
  f32x2 e, m;
  e[0] = __builtin_amdgcn_fmed3f(__builtin_amdgcn_exp2f(a[0]), 0.0f, 1.0f);
  e[1] = __builtin_amdgcn_fmed3f(__builtin_amdgcn_exp2f(a[1]), 0.0f, 1.0f);
  m[0] = fmaxf(t2[0], 1e-6f);
  m[1] = fmaxf(t2[1], 1e-6f);
  return m + e;
}

constexpr int LDS_BYTES = 163840, LDS_CNT = LDS_BYTES - 64;
constexpr int CV_PAD = 512, CV_HA = 286 * 16, CV_HB = CV_HA + 4160, CV_CH = CV_HB + 4128 + 32;
constexpr int CV_WB = CV_PAD + 4 * CV_CH, CV_X0 = CV_WB + 4 * 8192, CV_ST = CV_X0 + 16384, CV_BN = CV_ST + 16384;
constexpr int G1_AB = 32768, G1_STAGE = G1_AB + 12288, G1_SCR = 3 * G1_STAGE, G1_NP = 11;
constexpr int G2_Z = 131072, G2_RED = G2_Z + 2048;

struct Cx {
  char* ws; char* smem; rsrc_t rw, rc; int lane, w, tid, bid; u32x2 tag;
  DI void mark(int) {}
  DI void endspin() {}
  DI void half_sync(int n) const {
    const unsigned c = (unsigned)(size_t)(LAS char*)(smem + LDS_CNT + (w >> 2) * 16);
    asm volatile("s_waitcnt lgkmcnt(0)" ::: "memory");
    if (lane == 0) asm volatile("ds_add_u32 %0, %1" ::"v"(c), "v"(1u) : "memory");
    unsigned x;
    do { asm volatile("ds_read_b32 %0, %1\n\ts_waitcnt lgkmcnt(0)" : "=v"(x) : "v"(c) : "memory"); } while (__builtin_amdgcn_readfirstlane(x) < 4u * n);
  }
  DI void signal(int idx) const { if (lane < FREP) put8(ws + OFF_FLG + (lane * 3072 + idx) * 32, tag); }
  template <int DELAY> DI void await(int idx, bool pend) const {
    const char* fp = ws + OFF_FLG + ((bid & (FREP - 1)) * 3072 + idx) * 32;
    if (DELAY) __builtin_amdgcn_s_sleep(DELAY);
#pragma unroll 1
    for (int spin = 0; spin < SPIN_MAX; ++spin) {
      if (pend) { const u32x2 f = poll8(fp); pend = f[0] != tag[0] || f[1] != tag[1]; }
      if (!__any(pend)) break;
      __builtin_amdgcn_s_sleep(PSLEEP);
    }
    asm volatile("" ::: "memory");
  }
  DI unsigned touch_code() const {
    unsigned x;
    asm volatile("buffer_load_dword %0, %1, %2, 0 offen" : "=v"(x) : "v"(((bid >> 3) * 16 + (lane & 15)) * 128), "s"(rc) : "memory");
    return x;
  }
  DI void fresh() {
    asm volatile("" : "+v"(tid), "+s"(w), "+s"(bid));
    lane = tid & 63;
  }
  DI void set_tag(u32x2 e) {
    tag = (u32x2){__builtin_amdgcn_readfirstlane(e[0]) * 1664525u + 1013904223u, __builtin_amdgcn_readfirstlane(e[1]) * 22695477u + 1u};
  }
};

DI void conv_store(const Cx& c) {
  const int j = (c.bid & 7) * 32 + (c.bid >> 3), t = c.tid + (c.w >> 2) * 256;
  const char* smem = c.smem;
  u32x4 yo[2];
#pragma unroll
  for (int i = 0; i < 2; ++i) {
    const int p = t + 512 * i;
    unsigned yy[4];
#pragma unroll
    for (int ch = 0; ch < 4; ++ch) yy[ch] = *(const unsigned*)(smem + CV_PAD + ch * CV_CH + CV_HA + p * 4);
    yo[i] = (u32x4){__builtin_amdgcn_perm(yy[1], yy[0], 0x05040100u), __builtin_amdgcn_perm(yy[3], yy[2], 0x05040100u),
                    __builtin_amdgcn_perm(yy[1], yy[0], 0x07060302u), __builtin_amdgcn_perm(yy[3], yy[2], 0x07060302u)};
    const u32x4 x0v = *(const u32x4*)(smem + CV_X0 + p * 16);
    const f32x4 stv = *(const f32x4*)(smem + CV_ST + p * 16);
    u32x4 xo;
#pragma unroll
    for (int e = 0; e < 4; ++e) {
      const float m = stv[e & 2], rs = stv[(e & 2) + 1], xa = bflo(x0v[e]), xb = bfhi(x0v[e]);
      xo[e] = pk2(xa + (xa - m) * rs * bflo(yo[i][e]), xb + (xb - m) * rs * bfhi(yo[i][e]));
    }
    st16(c.rw, OFF_X1 + (j * 1024 + p) * 16, xo);
  }
  SB();
#pragma unroll
  for (int i = 0; i < 2; ++i) st16(c.rw, OFF_Y + (j * 1024 + t + 512 * i) * 16, yo[i]);
  wait_vm<2>();
  WAIT_LGKM0();
  BAR();
}

DI void front_m(Cx& c, const uint16_t* k, const uint16_t* v, const uint16_t* shift_k, const uint16_t* ssm_k, const uint16_t* ssm_D) {
  const int lane = c.lane, w = c.w, tid = c.tid, bid = c.bid, s0 = bid * 8, j = (bid & 7) * 32 + (bid >> 3);
  char* smem = c.smem;
  u32x4* bounce = (u32x4*)(smem + CV_BN) + w * 256;
  u32x4 uu[4], hh[4];
  unsigned hn[4];
  float Dc;
  u32x2 e0;
  {
    u32x2 kk[11], vv[8];
#pragma unroll
    for (int r = 0; r < 11; ++r) kk[r] = *(const u32x2*)(k + (size_t)((r < 3) ? max(s0 - 3 + r, 0) : s0 - 3 + r) * D + tid * 4);
#pragma unroll
    for (int r = 0; r < 8; ++r) vv[r] = *(const u32x2*)(v + (size_t)(s0 + r) * D + tid * 4);
    const u32x4 wa = *(const u32x4*)(shift_k + tid * 16), wb = *(const u32x4*)(shift_k + tid * 16 + 8);
    SB();
    const uint16_t* hrow = ssm_k + (size_t)(4 * j + w) * S;
#pragma unroll
    for (int i = 0; i < 4; ++i) {
      const int ch = i * 64 + lane;
      hh[i] = *(const u32x4*)(hrow + ch * 8);
      hn[i] = *(const unsigned*)(hrow + min(ch * 8 + 8, S - 2));
    }
    Dc = bflo((unsigned)ssm_D[4 * j + w]);
    e0 = poll8(c.ws + OFF_EPO);
    SB();
    if (tid < 2) *(unsigned*)(smem + LDS_CNT + tid * 16) = 0u;
    WAIT_LGKM0();
    BAR();
    SB();
    const unsigned hm = bid ? ~0u : 0u;
#pragma unroll
    for (int r = 0; r < 3; ++r) { kk[r][0] &= hm; kk[r][1] &= hm; }
    const int sw = (lane >> 1) & 3;
#pragma unroll
    for (int ch = 0; ch < 4; ++ch) {
      const unsigned w01 = (ch == 0) ? wa[0] : (ch == 1) ? wa[2] : (ch == 2) ? wb[0] : wb[2];
      const unsigned w23 = (ch == 0) ? wa[1] : (ch == 1) ? wa[3] : (ch == 2) ? wb[1] : wb[3];
      const float w0 = bflo(w01), w1 = bfhi(w01), w2 = bflo(w23), w3 = bfhi(w23);
      float kf[11], u[8];
#pragma unroll
      for (int r = 0; r < 11; ++r) kf[r] = (ch & 1) ? bfhi(kk[r][ch >> 1]) : bflo(kk[r][ch >> 1]);
#pragma unroll
      for (int r = 0; r < 8; ++r) {
        const float ks = kf[r] * w3 + kf[r + 1] * w2 + kf[r + 2] * w1 + kf[r + 3] * w0;
        u[r] = ks * ((ch & 1) ? bfhi(vv[r][ch >> 1]) : bflo(vv[r][ch >> 1]));
      }
      bounce[lane * 4 + (ch ^ sw)] = (u32x4){pk2(u[7], u[6]), pk2(u[5], u[4]), pk2(u[3], u[2]), pk2(u[1], u[0])};
    }
#pragma unroll
    for (int i = 0; i < 4; ++i) {
      const int u = 64 * i + lane;
      st16(c.rw, OFF_UB + (bid * 1024 + w * 256 + u) * 16, bounce[(u & ~3) | ((u & 3) ^ ((u >> 3) & 3))]);
    }
    STAMP(0, 2); c.mark(11);
    SB();
  }
  char* my = smem + CV_PAD + w * CV_CH;
  if (lane < 30) *(u32x4*)(my + lane * 16) = (u32x4){0u, 0u, 0u, 0u};
  if (lane >= 32 && lane < 34) *(u32x4*)(my + CV_HA + (lane - 32) * 16) = (u32x4){0u, 0u, 0u, 0u};
  if (lane == 34) *(u32x4*)(my + CV_HB) = (u32x4){0u, 0u, 0u, 0u};
  if (lane == 0) *(u32x4*)(my + CV_HB + 16) = (u32x4){0u, 0u, 0u, hh[0][0] << 16};
#pragma unroll
  for (int i = 0; i < 4; ++i) {
    const int ch = i * 64 + lane;
    *(u32x4*)(my + CV_HA + 32 + ch * 16) = hh[i];
    *(u32x4*)(my + CV_HB + 32 + ch * 16) =
        (u32x4){__builtin_amdgcn_alignbyte(hh[i][1], hh[i][0], 2), __builtin_amdgcn_alignbyte(hh[i][2], hh[i][1], 2),
                __builtin_amdgcn_alignbyte(hh[i][3], hh[i][2], 2), __builtin_amdgcn_alignbyte(hn[i], hh[i][3], 2)};
  }
  asm volatile("" : "+v"(Dc), "+v"(e0));
  c.set_tag(e0);
  wait_vm<0>();
  c.signal(FL_U + w * 256 + bid);
  STAMP(0, 3); c.mark(12);
  c.await<PDELAY>(FL_U + (j >> 6) * 256 + w * 64 + lane, true);
  STAMP(0, 4); c.mark(13);
#pragma unroll
  for (int i = 0; i < 4; ++i) uu[i] = ld16(c.rw, ((i * 16 + (lane >> 2)) * 1024 + (lane & 3)) * 16, OFF_UB + (w * 65536 + 4 * j) * 16);
#pragma unroll
  for (int i = 0; i < 4; ++i) *(u32x4*)(smem + CV_PAD + (lane & 3) * CV_CH + (30 + w * 64 + i * 16 + (lane >> 2)) * 16) = uu[i];
  const int t0 = lane & 15, kq = lane >> 4;
  const LAS unsigned* fbk[4];
#pragma unroll
  for (int i = 0; i < 4; ++i) {
    fbk[i] = (const LAS unsigned*)(my + ((t0 & 1) ? CV_HA : CV_HB)) + 4 * kq + ((1 + t0) >> 1) + 256 * i;
    asm volatile("" : "+v"(fbk[i]));
  }
  const char* gb = my + (2 * t0 + 3 - kq) * 16;
  f32x4 acc[8];
#pragma unroll
  for (int b = 0; b < 8; ++b) { acc[b] = (f32x4){0.f, 0.f, 0.f, 0.f}; asm volatile("" : "+v"(acc[b])); }
  c.half_sync(1);
  STAMP(1, 0); STAMP(1, 1); c.mark(14);
  bf16x8 F[2][8], G[2][8];
  u32x2 fh[8];
  auto rdn = [&](int buf, int g0, int n) {
    if (n == 0 || n > 16) { const int g1 = n ? n - 16 : 0; G[buf][g1] = *(const bf16x8*)(gb + (32 * g1 + 4 * (7 - g0)) * 16); }
    else {
      const int e1 = (n - 1) >> 1;
      const LAS unsigned* p = fbk[e1 >> 1] + 128 * (e1 & 1) + 16 * g0;
      if ((n - 1) & 1) F[buf][e1] = as_frag((u32x4){fh[e1][0], fh[e1][1], p[2], p[3]});
      else fh[e1] = (u32x2){p[0], p[1]};
    }
  };
  auto cstep = [&](int cb, int g0n) {
    int n = 0;
#pragma unroll
    for (int g1 = 0; g1 < 8; ++g1)
#pragma unroll
      for (int e1 = 0; e1 + g1 < 8; ++e1) {
        acc[g1 + e1] = MFMA16(F[cb][e1], G[cb][g1], acc[g1 + e1]);
        SB();
        if (n < 24) { rdn(cb ^ 1, g0n, n); SB(); }
        ++n;
      }
  };
#pragma unroll
  for (int n = 0; n < 24; ++n) rdn(0, 0, n);
  for (int gp = 0; gp < 4; ++gp) {
    cstep(0, 1);
    cstep(1, 2);
    gb -= 128;
#pragma unroll
    for (int i = 0; i < 4; ++i) fbk[i] += 32;
  }
  asm volatile("" ::"v"(acc[7]), "v"(acc[0]));
  STAMP(1, 2);
#pragma unroll
  for (int b = 0; b < 8; ++b) {
    const int s = 256 * b + 16 * t0 + 4 * kq;
    const u32x2 ur = *(const u32x2*)(my + (30 + (s >> 3)) * 16 + ((s & 4) ? 0 : 8));
    *(u32x2*)(my + CV_HA + s * 2) = (u32x2){pk2(acc[b][0] + bfhi(ur[1]) * Dc, acc[b][1] + bflo(ur[1]) * Dc),
                                            pk2(acc[b][2] + bfhi(ur[0]) * Dc, acc[b][3] + bflo(ur[0]) * Dc)};
  }
  WAIT_LGKM0();
  BAR();
  STAMP(1, 3); c.mark(15);
  conv_store(c);
  STAMP(1, 6); c.mark(16);
  if (w == 0) c.signal(FL_X1 + bid);
  const unsigned pf = c.touch_code();
  c.await<PDELAY>(FL_X1 + w * 64 + lane, true);
  KEEP(pf);
  STAMP(1, 7);
  BAR();
  if (bid == 0 && tid == 0) put8(c.ws + OFF_EPO, c.tag);
}

DI void front_h(Cx& c, const uint16_t* q, const uint16_t* wqkv, const uint16_t* wo) {
  const int lane = c.lane, w4 = c.w - 4, tid = c.tid, bid = c.bid, s0 = bid * 8, j = (bid & 7) * 32 + (bid >> 3);
  char* smem = c.smem;
  const rsrc_t rw = c.rw;
  u32x4* bounce = (u32x4*)(smem + CV_BN) + c.w * 256;
  float* red = (float*)smem;
  const int gw = bid * 4 + w4;
  char* buf = smem + CV_WB + w4 * 8192;
  const int g = lane >> 4, qr = (lane >> 2) & 3, p = lane & 3, rr = lane >> 3;
  const bool isw = gw < 768;
  const int cg = isw ? gw % 48 : (gw - 768) & 15, og = isw ? gw / 48 : (gw - 768) >> 4;
  auto w_loads = [&]() {
    if (isw) {
      const uint16_t* src = wqkv + (size_t)(og * 64 + rr) * 3072 + cg * 64 + (lane & 7) * 8;
#pragma unroll
      for (int i = 0; i < 8; ++i) glds16(src + (size_t)i * 8 * 3072, buf + i * 1024);
    } else {
      const uint16_t* src = wo + (size_t)(og * 64 + (rr & 3) + 16 * (rr >> 2)) * 1024 + cg * 64 + (lane & 7) * 8;
#pragma unroll
      for (int i = 0; i < 8; ++i) glds16(src + (size_t)((i >> 2) * 32 + (i & 3) * 4) * 1024, buf + i * 1024);
    }
  };
  u32x2 qq[8];
#pragma unroll
  for (int r = 0; r < 8; ++r) qq[r] = *(const u32x2*)(q + (size_t)(s0 + r) * D + tid * 4);
  SB();
  u32x2 e0 = poll8(c.ws + OFF_EPO);
  const unsigned pf = c.touch_code();
  if (WMODE == 0) w_loads();
  SB();
  BAR();
  const int sw = (lane >> 1) & 3;
#pragma unroll
  for (int r = 0; r < 4; ++r) bounce[lane * 4 + (r ^ sw)] = (u32x4){qq[2 * r][0], qq[2 * r][1], qq[2 * r + 1][0], qq[2 * r + 1][1]};
#pragma unroll
  for (int i = 0; i < 4; ++i) {
    const int u = 64 * i + lane;
    st16(rw, OFF_X0 + (bid * 1024 + w4 * 256 + u) * 16, bounce[(u & ~3) | ((u & 3) ^ ((u >> 3) & 3))]);
  }
  if (tid < 48) st16(rw, OFF_GX + bid * 768 + tid * 16, (u32x4){~0u, ~0u, ~0u, ~0u});
  SB();
  if (WMODE == 1) w_loads();
  SB();
  float a[16];
#pragma unroll
  for (int r = 0; r < 8; ++r) {
    a[2 * r] = dot2(qq[r][0], 0x3f803f80u, dot2(qq[r][1], 0x3f803f80u, 0.f));
    a[2 * r + 1] = dot2(qq[r][0], qq[r][0], dot2(qq[r][1], qq[r][1], 0.f));
  }
#pragma unroll
  for (int i = 0; i < 8; ++i) a[i] = swap32_sum(a[i], a[i + 8]);
#pragma unroll
  for (int i = 0; i < 4; ++i) a[i] = swap16_sum(a[i], a[i + 4]);
#pragma unroll
  for (int i = 0; i < 4; ++i) { a[i] += row_ror<1>(a[i]); a[i] += row_ror<2>(a[i]); a[i] += row_ror<4>(a[i]); a[i] += row_ror<8>(a[i]); }
  if ((lane & 15) == 0) *(f32x4*)&red[w4 * 16 + (lane >> 4) * 4] = (f32x4){a[0], a[1], a[2], a[3]};
  c.half_sync(1);
  if (tid < 8) {
    const float sm = (red[2 * tid] + red[16 + 2 * tid]) + (red[32 + 2 * tid] + red[48 + 2 * tid]);
    const float sq = (red[2 * tid + 1] + red[17 + 2 * tid]) + (red[33 + 2 * tid] + red[49 + 2 * tid]);
    st8(rw, OFF_ST0 + (s0 + tid) * 8, __builtin_bit_cast(u32x2, ln_stats(sm, sq)));
  }
  asm volatile("" : "+v"(e0));
  c.set_tag(e0);
  wait_vm<0>();
  KEEP(pf);
  c.signal(FL_Q + w4 * 256 + bid);
  STAMPW(0, 5, 4);
  if (WMODE == 2) w_loads();
  {
    const unsigned cm = (unsigned)(size_t)(LAS char*)(smem + LDS_CNT);
    unsigned x;
    do { __builtin_amdgcn_s_sleep(2); asm volatile("ds_read_b32 %0, %1\n\ts_waitcnt lgkmcnt(0)" : "=v"(x) : "v"(cm) : "memory"); } while (__builtin_amdgcn_readfirstlane(x) < 4u);
  }
  c.mark(1);
  if (WMODE == 3) w_loads();
  c.await<0>(FL_Q + w4 * 64 + lane, true);
  c.await<0>(FL_Q + (j >> 6) * 256 + w4 * 64 + lane, true);
  const int hd = (cg & 15) * 2 + (lane >> 5), col = (cg >> 4) * 32 + (lane & 31);
  const unsigned dst = isw ? OFF_W8 + ((hd * 128 + og * 8) * 96 + col) * 16
                           : OFF_WOF + ((((og >> 1) * 32 + cg * 2 + (g >> 1)) * 8 + (og & 1) * 4) * 64 + (lane & 31)) * 16;
  const unsigned dstep = isw ? 1536 : 512;
  const unsigned la = (unsigned)(size_t)(LAS char*)buf + qr * 128 + (isw ? g * 32 + p * 8 : (g >> 1) * 64 + (p & 1) * 32 + ((g & 1) * 2 + (p >> 1)) * 8);
  u32x2 lo[8], hi[8];
#pragma unroll
  for (int i = 0; i < 8; ++i) {
    lo[i] = __builtin_bit_cast(u32x2, __builtin_amdgcn_ds_read_tr16_b64_v4i16((LAS s16x4*)(size_t)(la + i * 1024)));
    hi[i] = __builtin_bit_cast(u32x2, __builtin_amdgcn_ds_read_tr16_b64_v4i16((LAS s16x4*)(size_t)(la + i * 1024 + 512)));
  }
  SB();
#pragma unroll
  for (int i = 0; i < 4; ++i) {
    const int pp = w4 * 256 + i * 64 + lane;
    dma16(rw, smem + CV_X0 + (w4 * 4 + i) * 1024, ((pp >> 2) * 1024 + (pp & 3)) * 16, OFF_X0 + j * 64);
    dma16(rw, smem + CV_ST + (w4 * 4 + i) * 1024, pp * 16, OFF_ST0);
  }
  SB();
#pragma unroll
  for (int i = 0; i < 8; ++i) st16(rw, dst + i * dstep, (u32x4){lo[i][0], lo[i][1], hi[i][0], hi[i][1]});
  if (lane < 16 + 1 - (gw & 1)) st16(rw, OFF_KV + (gw * 33 / 2 + (gw & 1) + lane) * 16, (u32x4){0u, 0u, 0u, 0u});
  if (isw) {
    float c0 = 0.f, c1 = 0.f;
#pragma unroll
    for (int i = 0; i < 8; ++i)
#pragma unroll
      for (int e = 0; e < 2; ++e) {
        c0 = opq(opq(c0 + bflo(lo[i][e])) + bflo(hi[i][e]));
        c1 = opq(opq(c1 + bfhi(lo[i][e])) + bfhi(hi[i][e]));
      }
    __builtin_amdgcn_raw_buffer_store_b32(__float_as_uint(c0 + c1), rw, OFF_CSP + ((hd * 96 + col) * 16 + og) * 4, 0, 16);
  }
  STAMPW(1, 4, 4);
  wait_vm<9>();
  STAMPW(1, 5, 4);
  BAR();
  conv_store(c);
  BAR();
}

template <int L> DI void g1_m(const Cx& c) {
  constexpr bool C4 = L == 0;
  constexpr int kid = 2 + 2 * L;
  const int lane = c.lane, w = c.w, tid = c.tid, xcd = c.bid & 7, idx = c.bid >> 3;
  const int band = (xcd & 1) * 4 + (idx & 3), h = (xcd >> 1) * 8 + (idx >> 2), r = lane & 15, kq = lane >> 4;
  char* smem = c.smem;
  const float* Rs = (const float*)(smem + G1_SCR);
  const float* MRs = Rs + 256;
  const float* CSs = Rs + 512;
  float* P = (float*)(smem + G1_SCR) + 640;
  const char* ab = C4 ? smem + kq * 4096 + ((64 * w + r) ^ (16 * (kq & 1))) * 8 : smem + kq * 4096 + (64 * w + r) * 16;
  const char* ab2 = ab + 128 - (kq & 1) * 256;
  const char* bb = smem + G1_AB + kq * 1536 + r * 16;
  STAMP(kid, 0);
  f32x4 acc[4][6];
#pragma unroll
  for (int m = 0; m < 4; ++m)
#pragma unroll
    for (int p = 0; p < 6; ++p) { acc[m][p] = (f32x4){0.f, 0.f, 0.f, 0.f}; asm volatile("" : "+v"(acc[m][p])); }
  bf16x8 F[2][10];
  auto rd1 = [&](int buf, const char* a, const char* a2, const char* b, int t, int i) {
    if (i < 4) {
      if (C4) {
        const char* p = ((i & 1) ? a2 : a) + t * 16384 + (i >> 1) * 256;
        const u32x2 lo = *(const u32x2*)p, hi = *(const u32x2*)(p + 2048);
        F[buf][i] = as_frag((u32x4){lo[0], lo[1], hi[0], hi[1]});
      } else F[buf][i] = *(const bf16x8*)(a + t * 16384 + i * 256);
    } else F[buf][i] = *(const bf16x8*)(b + t * 6144 + (i - 4) * 256);
  };
  auto step = [&](int cb, const char* a, const char* a2, const char* b, int t, bool rd) {
    const int ord[10] = {0, 4, 5, 6, 7, 8, 9, 1, 2, 3};
#pragma unroll
    for (int i = 0; i < 24; ++i) {
      const int m = i / 6, p = i % 6;
      if (p < 2) acc[m][p] = MFMA16(F[cb][4 + p], F[cb][m], acc[m][p]);
      else acc[m][p] = MFMA16(F[cb][m], F[cb][4 + p], acc[m][p]);
      SB();
      if (rd && (i & 1) && (i >> 1) < 10) { rd1(cb ^ 1, a, a2, b, t, ord[i >> 1]); SB(); }
    }
  };
  BAR();
  STAMP(kid, 1);
#pragma unroll
  for (int i = 0; i < 10; ++i) rd1(0, ab, ab2, bb, 0, i);
  int so = 0;
  for (int it = 0; it < 15; ++it) {
    step(0, ab + so, ab2 + so, bb + so, 1, true);
    WAIT_LGKM0();
    BAR();
    so = (so == 2 * G1_STAGE) ? 0 : so + G1_STAGE;
    step(1, ab + so, ab2 + so, bb + so, 0, true);
  }
  step(0, ab + so, ab2 + so, bb + so, 1, true);
  BAR();
  step(1, ab, ab2, bb, 0, false);
  STAMP(kid, 2);
  f32x4 Z[2][2], Zz[2];
#pragma unroll
  for (int i = 0; i < 2; ++i) { Z[i][0] = (f32x4){0.f, 0.f, 0.f, 0.f}; Z[i][1] = Z[i][0]; Zz[i] = Z[i][0]; }
  {
    float ck[4];
#pragma unroll
    for (int p = 0; p < 4; ++p) ck[p] = CSs[32 + p * 16 + r];
    const bf16x8 ones = as_frag((u32x4){0x3f803f80u, 0x3f803f80u, 0x3f803f80u, 0x3f803f80u});
#pragma unroll
    for (int jj = 0; jj < 2; ++jj) {
      u32x4 fr[4];
#pragma unroll
      for (int mm = 0; mm < 2; ++mm) {
        const int m = 2 * jj + mm;
        const f32x4 rs = *(const f32x4*)(Rs + 64 * w + 16 * m + 4 * kq), mr = *(const f32x4*)(MRs + 64 * w + 16 * m + 4 * kq);
#pragma unroll
        for (int p = 0; p < 2; ++p) {
          fr[p][2 * mm] = pk2v(phi2(lo2(acc[m][2 + p]) * lo2(rs) + (lo2(mr) * ck[p] + 1e-6f)));
          fr[p][2 * mm + 1] = pk2v(phi2(hi2(acc[m][2 + p]) * hi2(rs) + (hi2(mr) * ck[p] + 1e-6f)));
          fr[2 + p][2 * mm] = pk2v(lo2(acc[m][4 + p]) * lo2(rs) + lo2(mr) * ck[2 + p]);
          fr[2 + p][2 * mm + 1] = pk2v(hi2(acc[m][4 + p]) * hi2(rs) + hi2(mr) * ck[2 + p]);
        }
      }
#pragma unroll
      for (int dh = 0; dh < 2; ++dh) {
        Z[0][dh] = MFMA16(as_frag(fr[dh]), as_frag(fr[2]), Z[0][dh]);
        Z[1][dh] = MFMA16(as_frag(fr[dh]), as_frag(fr[3]), Z[1][dh]);
        Zz[dh] = MFMA16(as_frag(fr[dh]), ones, Zz[dh]);
      }
    }
  }
  {
    float* mine = P + w * 1056;
#pragma unroll
    for (int i = 0; i < 4; ++i) *(f32x4*)(mine + i * 256 + lane * 4) = Z[i >> 1][i & 1];
    if (r == 0) { *(f32x4*)(mine + 1024 + 4 * kq) = Zz[0]; *(f32x4*)(mine + 1040 + 4 * kq) = Zz[1]; }
  }
  c.half_sync(2 * L + 2);
  STAMP(kid, 3);
  float* kvacc = (float*)(c.ws + OFF_KV) + L * KV_LAYER;
#pragma unroll
  for (int i = 0; i < 4; ++i) {
    const int e = tid + 256 * i;
    atomicAdd(kvacc + h * 1024 + e, (P[e] + P[1056 + e]) + (P[2112 + e] + P[3168 + e]));
  }
  if (tid < 32) atomicAdd(kvacc + 32768 + h * 32 + tid, (P[1024 + tid] + P[2080 + tid]) + (P[3136 + tid] + P[4192 + tid]));
  SB();
  {
    const f32x4 c0 = *(const f32x4*)(CSs + 4 * kq), c1 = *(const f32x4*)(CSs + 16 + 4 * kq);
#pragma unroll
    for (int m = 0; m < 4; ++m) {
      const float rs = Rs[64 * w + 16 * m + r], mr = MRs[64 * w + 16 * m + r];
      const u32x4 o = {pk2v(phi2(lo2(acc[m][0]) * rs + (lo2(c0) * mr + 1e-6f))), pk2v(phi2(hi2(acc[m][0]) * rs + (hi2(c0) * mr + 1e-6f))),
                       pk2v(phi2(lo2(acc[m][1]) * rs + (lo2(c1) * mr + 1e-6f))), pk2v(phi2(hi2(acc[m][1]) * rs + (hi2(c1) * mr + 1e-6f)))};
      st16(c.rw, OFF_PQ + L * 4 * MB + ((((h >> 2) * 128 + band * 16 + w * 4 + m) * 4 + (h & 3)) * 64 + lane) * 16, o);
    }
  }
  STAMP(kid, 6);
  wait_vm<0>();
  c.half_sync(2 * L + 3);
  STAMP(kid, 7);
  if (w == 0) c.signal(FL_G1 + L * 256 + h * 8 + band);
}

template <int L> DI void g1_h(const Cx& c) {
  constexpr bool C4 = L == 0;
  constexpr unsigned OFF_X = C4 ? OFF_X1 : OFF_X3;
  const int lane = c.lane, lw = c.w - 4, t = c.tid, xcd = c.bid & 7, idx = c.bid >> 3;
  const int band = (xcd & 1) * 4 + (idx & 3), h = (xcd >> 1) * 8 + (idx >> 2);
  char* smem = c.smem;
  char* ws = c.ws;
  const rsrc_t rw = c.rw;
  float* Rs = (float*)(smem + G1_SCR);
  const unsigned l0 = lane * 16, l1 = (lane ^ 8) * 16;
  auto issue = [&](int st) {
    char* base = smem + (st % 3) * G1_STAGE;
#pragma unroll
    for (int p = 0; p < 8; ++p) {
      if (C4) dma16(rw, base + (8 * lw + p) * 1024, (p & 4) ? l1 : l0, OFF_X + ((st * 16 + lw * 4 + (p >> 1)) * 1024 + band * 128 + (p & 1) * 64) * 16);
      else dma16(rw, base + (8 * lw + p) * 1024, l0, OFF_X + ((st * 8 + lw * 2 + (p >> 2)) * S + band * 256 + (p & 3) * 64) * 16);
    }
#pragma unroll
    for (int p = 0; p < 3; ++p) dma16(rw, base + G1_AB + (3 * lw + p) * 1024, l0, OFF_W8 + (h * 128 + st * 8) * 1536 + (3 * lw + p) * 1024);
  };
  issue(0); issue(1);
  wait_vm<G1_NP>();
  BAR();
  issue(2);
  u32x4 xv[4];
  float v[4] = {0.f, 0.f, 0.f, 0.f};
  if (C4) {
    const u32x4* xp = (const u32x4*)(ws + OFF_X) + (size_t)(t >> 2) * 1024 + band * 128 + h * 4 + (t & 3);
    asm volatile("global_load_dwordx4 %0, %4, off sc1\n\tglobal_load_dwordx4 %1, %5, off sc1\n\tglobal_load_dwordx4 %2, %6, off sc1\n\tglobal_load_dwordx4 %3, %7, off sc1"
                 : "=&v"(xv[0]), "=&v"(xv[1]), "=&v"(xv[2]), "=&v"(xv[3]) : "v"(xp), "v"(xp + 65536), "v"(xp + 131072), "v"(xp + 196608) : "memory");
  }
#pragma unroll
  for (int i = 0; i < 13; ++i) {
    if (C4 && i < 2) wait_vm<G1_NP + 4>();
    else if (C4 && i == 2) asm volatile("s_waitcnt vmcnt(%4)" : "+v"(xv[0]), "+v"(xv[1]), "+v"(xv[2]), "+v"(xv[3]) : "i"(G1_NP) : "memory");
    else wait_vm<G1_NP>();
    BAR();
    issue(i + 3);
    if (C4 && i >= 2 && i < 6) {
      const u32x4 x = xv[i - 2];
#pragma unroll
      for (int e = 0; e < 4; ++e) {
        const float lo = bflo(x[e]), hi = bfhi(x[e]);
        v[e & 2] = opq(v[e & 2] + lo); v[(e & 2) + 1] = opq(fmaf(lo, lo, v[(e & 2) + 1]));
        v[e & 2] = opq(v[e & 2] + hi); v[(e & 2) + 1] = opq(fmaf(hi, hi, v[(e & 2) + 1]));
      }
      asm volatile("" : "+v"(v[0]), "+v"(v[1]), "+v"(v[2]), "+v"(v[3]));
    }
    if (C4 && i == 6) {
#pragma unroll
      for (int e = 0; e < 4; ++e) { v[e] += row_ror<4>(v[e]); v[e] += row_ror<8>(v[e]); v[e] = swap16_sum(v[e], v[e]); v[e] = swap32_sum(v[e], v[e]); }
      const bool up = (lane & 4) != 0;
      if (lane < 8) publish8(ws + OFF_GX + ((band * 4 + lw) * 256 + h * 8 + 2 * (lane & 3) + (lane >> 2)) * 8, up ? v[2] : v[0], up ? v[3] : v[1]);
    }
  }
  const char* gp = ws + (C4 ? OFF_GX + (band * 1024 + t) * 8 + 4096 : OFF_ST3 + ((band * 4 + lw) * 512 + lane) * 8);
  const char* cp = ws + OFF_CSP + (h * 96 + (t < 96 ? t : 0)) * 64;
  u32x2 g[8];
  f32x4 c0, c1, c2, c3;
  asm volatile("global_load_dwordx4 %0, %4, off sc1\n\tglobal_load_dwordx4 %1, %4, off offset:16 sc1\n\t"
               "global_load_dwordx4 %2, %4, off offset:32 sc1\n\tglobal_load_dwordx4 %3, %4, off offset:48 sc1"
               : "=&v"(c0), "=&v"(c1), "=&v"(c2), "=&v"(c3) : "v"(cp) : "memory");
  if (C4) {
    asm volatile("global_load_dwordx2 %0, %4, off offset:-4096 sc1\n\tglobal_load_dwordx2 %1, %4, off offset:-2048 sc1\n\t"
                 "global_load_dwordx2 %2, %4, off sc1\n\tglobal_load_dwordx2 %3, %4, off offset:2048 sc1"
                 : "=&v"(g[0]), "=&v"(g[1]), "=&v"(g[2]), "=&v"(g[3]) : "v"(gp) : "memory");
    wait_vm<G1_NP + 8>();
  } else {
    asm volatile("global_load_dwordx2 %0, %8, off sc1\n\tglobal_load_dwordx2 %1, %8, off offset:512 sc1\n\tglobal_load_dwordx2 %2, %8, off offset:1024 sc1\n\t"
                 "global_load_dwordx2 %3, %8, off offset:1536 sc1\n\tglobal_load_dwordx2 %4, %8, off offset:2048 sc1\n\tglobal_load_dwordx2 %5, %8, off offset:2560 sc1\n\t"
                 "global_load_dwordx2 %6, %8, off offset:3072 sc1\n\tglobal_load_dwordx2 %7, %8, off offset:3584 sc1"
                 : "=&v"(g[0]), "=&v"(g[1]), "=&v"(g[2]), "=&v"(g[3]), "=&v"(g[4]), "=&v"(g[5]), "=&v"(g[6]), "=&v"(g[7]) : "v"(gp) : "memory");
    wait_vm<G1_NP + 12>();
  }
  BAR();
  if (C4) asm volatile("s_waitcnt vmcnt(0)" : "+v"(g[0]), "+v"(g[1]), "+v"(g[2]), "+v"(g[3]), "+v"(c0), "+v"(c1), "+v"(c2), "+v"(c3)::"memory");
  else asm volatile("s_waitcnt vmcnt(0)" : "+v"(g[0]), "+v"(g[1]), "+v"(g[2]), "+v"(g[3]), "+v"(g[4]), "+v"(g[5]), "+v"(g[6]), "+v"(g[7]),
                    "+v"(c0), "+v"(c1), "+v"(c2), "+v"(c3)::"memory");
  BAR();
  if (C4) {
#pragma unroll 1
    for (int spin = 0; spin < SPIN_MAX; ++spin) {
      if (!__any(g[0][1] == ~0u || g[1][1] == ~0u || g[2][1] == ~0u || g[3][1] == ~0u)) break;
      __builtin_amdgcn_s_sleep(4);
#pragma unroll
      for (int k = 0; k < 4; ++k) g[k] = poll8(gp + (k - 2) * 2048);
    }
  }
  float sm = 0.f, sq = 0.f, cs = 0.f;
#pragma unroll
  for (int k = 0; k < (C4 ? 4 : 8); ++k) { sm = opq(sm + __uint_as_float(g[k][0])); sq = opq(sq + __uint_as_float(g[k][1])); }
#pragma unroll
  for (int k = 0; k < 4; ++k) cs = opq(opq(opq(opq(cs + c0[k]) + c1[k]) + c2[k]) + c3[k]);
  const f32x2 st = ln_stats(sm, sq);
  Rs[t] = st[1]; Rs[256 + t] = -st[0] * st[1];
  if (t < 96) Rs[512 + t] = cs;
  WAIT_LGKM0();
  BAR();
}

template <int L> DI void g2_m(const Cx& c) {
  constexpr int kid = 3 + 2 * L;
  constexpr unsigned KVO = OFF_KV + L * KV_LAYER * 4;
  const int lane = c.lane, w = c.w, xcd = c.bid & 7, idx = c.bid >> 3, sb = (xcd >> 1) * 8 + (idx & 7), kq = lane >> 4;
  const rsrc_t rw = c.rw;
  const unsigned l16 = lane * 16;
  u32x4* O = (u32x4*)c.smem;
  unsigned pf = c.touch_code();
  c.await<PDELAY>(FL_G1 + L * 256 + ((lane >> 3) * 4 + w) * 8 + (lane & 7), true);
  KEEP(pf);
  STAMP(kid, 0);
  constexpr int PL = 2, PD = PL + 1;
  u32x4 pq[PD][4];
  f32x4 kv[PD][4];
  const unsigned spq = OFF_PQ + L * 4 * MB + (sb * 16 + w) * 1024, skv = KVO + w * 4096;
  auto ld = [&](int i, int n) {
    if (n < 4) kv[i % PD][n] = __builtin_bit_cast(f32x4, ld16(rw, l16 + n * 1024, skv + i * 16384));
    else pq[i % PD][n - 4] = ld16(rw, l16, spq + (i * 512 + (n - 4) * 4) * 1024);
    SB();
  };
  const f32x4 zall = __builtin_bit_cast(f32x4, ld16(rw, (lane >> 3) * 512 + (lane & 7) * 16, KVO + 131072 + w * 128));
  SB();
#pragma unroll
  for (int n = 0; n < 8 * PL; ++n) ld(n >> 3, n & 7);
  u32x2* Zs = (u32x2*)(c.smem + G2_Z) + w * 64;
  Zs[lane] = (u32x2){pk2v(lo2(zall)), pk2v(hi2(zall))};
  const f32x4 zero = {0.f, 0.f, 0.f, 0.f}, eps = {1e-6f, 1e-6f, 1e-6f, 1e-6f};
  bf16x8 k0, k1, zb;
  auto prep = [&](int i) {
    const int b = i % PD;
    const u32x2 za = Zs[i * 8 + kq], zc = Zs[i * 8 + 4 + kq];
    k0 = as_frag((u32x4){pk2v(lo2(kv[b][0])), pk2v(hi2(kv[b][0])), pk2v(lo2(kv[b][1])), pk2v(hi2(kv[b][1]))});
    k1 = as_frag((u32x4){pk2v(lo2(kv[b][2])), pk2v(hi2(kv[b][2])), pk2v(lo2(kv[b][3])), pk2v(hi2(kv[b][3]))});
    zb = as_frag((u32x4){za[0], za[1], zc[0], zc[1]});
    asm volatile("" : "+v"(k0), "+v"(k1), "+v"(zb));
  };
  prep(0);
#pragma unroll
  for (int i = 0; i < 8; ++i) {
    const int b = i % PD;
    f32x4 x0[4], x1[4], dn[4];
    __builtin_amdgcn_s_setprio(2);
#pragma unroll
    for (int st = 0; st < 4; ++st) {
      dn[st] = MFMA16(zb, as_frag(pq[b][st]), eps);
      x0[st] = MFMA16(k0, as_frag(pq[b][st]), zero);
      x1[st] = MFMA16(k1, as_frag(pq[b][st]), zero);
    }
    __builtin_amdgcn_s_setprio(0);
    SB();
#pragma unroll
    for (int st = 0; st < 4; ++st) {
      const float inv = __builtin_amdgcn_rcpf(dn[st][0]);
      O[((i * 4 + w) * 4 + st) * 64 + lane] =
          (u32x4){pk2(opq(x0[st][0] * inv), opq(x0[st][1] * inv)), pk2(opq(x0[st][2] * inv), opq(x0[st][3] * inv)),
                  pk2(opq(x1[st][0] * inv), opq(x1[st][1] * inv)), pk2(opq(x1[st][2] * inv), opq(x1[st][3] * inv))};
      SB();
      if (i + PL < 8) { ld(i + PL, 2 * st); ld(i + PL, 2 * st + 1); }
    }
    if (i < 7) prep(i + 1);
    WAIT_LGKM0();
    BAR();
    if (i == 0) STAMP(kid, 1);
  }
  if (L == 0) {
    pf = c.touch_code();
    wait_vm<0>();
    KEEP(pf);
    BAR();
  }
}

template <int L> DI void g2_h(const Cx& cx, char* out) {
  constexpr int kid = 3 + 2 * L;
  const int lane = cx.lane, c = cx.w - 4, xcd = cx.bid & 7, idx = cx.bid >> 3;
  const int sb = (xcd >> 1) * 8 + (idx & 7), nb = (xcd & 1) * 4 + (idx >> 3);
  const int n0 = nb * 128 + c * 32, s0 = sb * 64, r = lane & 31, hf = lane >> 5;
  const rsrc_t rw = cx.rw;
  const unsigned l16 = lane * 16;
  constexpr int CD = 5, LA = 6;
  u32x4 wo[CD][8], of[8];
  const unsigned swo = OFF_WOF + (nb * 4 + c) * 8192;
  auto ldw = [&](int i, int n) { wo[i % CD][n] = ld16(rw, l16 + (n & 3) * 1024, swo + i * 262144 + (n >> 2) * 4096); SB(); };
#pragma unroll
  for (int n = 0; n < 8 * CD; ++n) ldw(n >> 3, n & 7);
  f32x16 acc[2];
#pragma unroll
  for (int i = 0; i < 16; ++i) { acc[0][i] = 0.f; acc[1][i] = 0.f; }
  asm volatile("" : "+v"(acc[0]), "+v"(acc[1]));
  u32x4 res[2][2], yv[2][2];
  auto ldres = [&](int k) {
    const int s2 = (k >> 1) & 1, g = k & 1;
    if (L == 1) { if (k < 4) res[s2][g] = ld16(rw, (2 * hf * S + r) * 16 + s2 * 512, OFF_X3 + (((n0 >> 3) + g) * S + s0) * 16); }
    else {
      const unsigned so = (k < 4 ? OFF_X1 : OFF_Y) + (((n0 >> 2) + 2 * g) * S + s0) * 8, vo = (4 * hf * S + r) * 8 + s2 * 256;
      const u32x2 lo = ld8(rw, vo, so), hi = ld8(rw, vo, so + S * 8);
      if (k < 4) res[s2][g] = (u32x4){lo[0], lo[1], hi[0], hi[1]}; else yv[s2][g] = (u32x4){lo[0], lo[1], hi[0], hi[1]};
    }
    SB();
  };
  const u32x4* Ob = (const u32x4*)cx.smem + (r >> 4) * 64 + hf * 16 + (r & 15);
  auto rdo = [&](int f) { const int u = f & 15; of[f & 7] = Ob[((f >> 4) * 16 + (u >> 2) * 4 + (u & 1) * 2) * 64 + ((u >> 1) & 1) * 32]; SB(); };
  __builtin_amdgcn_s_setprio(1);
  BAR();
#pragma unroll
  for (int f = 0; f < LA; ++f) rdo(f);
#pragma unroll
  for (int i = 0; i < 8; ++i)
#pragma unroll
    for (int n = 0; n < 16; ++n) {
      const int f = i * 16 + n;
      acc[n & 1] = MFMA32(as_frag(wo[i % CD][n >> 1]), as_frag(of[f & 7]), acc[n & 1]);
      SB();
      if (f + LA < 128) { if (((f + LA) & 15) == 0) BAR(); rdo(f + LA); }
      if ((n & 1) && i + CD < 8) ldw(i + CD, n >> 1);
      if (i == 5 && (n & 1)) ldres(n >> 1);
    }
  __builtin_amdgcn_s_setprio(0);
  STAMPW(kid, 2, 4);
  u32x4 x[2][2];
#pragma unroll
  for (int s2 = 0; s2 < 2; ++s2)
#pragma unroll
    for (int g = 0; g < 2; ++g)
#pragma unroll
      for (int e = 0; e < 4; ++e) x[s2][g][e] = pk2(bflo(res[s2][g][e]) + acc[s2][8 * g + 2 * e], bfhi(res[s2][g][e]) + acc[s2][8 * g + 2 * e + 1]);
  if (L == 1) {
#pragma unroll
    for (int s2 = 0; s2 < 2; ++s2)
#pragma unroll
      for (int g = 0; g < 2; ++g) *(u32x4*)(out + ((size_t)(s0 + 32 * s2 + r) * D + n0 + 16 * hf + 8 * g) * 2) = x[s2][g];
    STAMPW(kid, 6, 4); DRAIN(); STAMPW(kid, 7, 4);
    return;
  }
  f32x2* Red = (f32x2*)(cx.smem + G2_RED);
  auto rowpart = [&]() {
    float v[4] = {0.f, 0.f, 0.f, 0.f};
#pragma unroll
    for (int e = 0; e < 8; ++e)
#pragma unroll
      for (int s2 = 0; s2 < 2; ++s2) { const unsigned u = x[s2][e >> 2][e & 3]; v[2 * s2] = dot2(u, 0x3f803f80u, v[2 * s2]); v[2 * s2 + 1] = dot2(u, u, v[2 * s2 + 1]); }
    return (f32x2){swap32_sum(v[0], v[2]), swap32_sum(v[1], v[3])};
  };
  Red[c * 64 + lane] = rowpart();
  cx.half_sync(2);
  if (c == 0) {
    char* gx = cx.ws + OFF_GX2 + (sb * 512 + lane) * 8;
    const f32x2 t = (Red[lane] + Red[64 + lane]) + (Red[128 + lane] + Red[192 + lane]);
    publish8(gx + nb * 512, t[0], t[1]);
    STAMPW(kid, 4, 4);
    __builtin_amdgcn_s_sleep(XDELAY);
    float sm, sq;
#pragma unroll 1
    for (int spin = 0; spin < SPIN_MAX; ++spin) {
      u32x2 g[8];
#pragma unroll
      for (int k = 0; k < 8; ++k) g[k] = poll8(gx + k * 512);
      bool empty = false;
      sm = 0.f; sq = 0.f;
#pragma unroll
      for (int k = 0; k < 8; ++k) { empty |= g[k][1] == ~0u; sm += __uint_as_float(g[k][0]); sq += __uint_as_float(g[k][1]); }
      if (!__any(empty)) break;
      __builtin_amdgcn_s_sleep(4);
    }
    Red[lane] = ln_stats(sm, sq);
  }
  cx.half_sync(3);
  STAMPW(kid, 3, 4);
  const f32x2 st = Red[lane];
  const auto mm = __builtin_amdgcn_permlane32_swap(__float_as_uint(st[0]), __float_as_uint(st[0]), false, false);
  const auto rr = __builtin_amdgcn_permlane32_swap(__float_as_uint(st[1]), __float_as_uint(st[1]), false, false);
#pragma unroll
  for (int s2 = 0; s2 < 2; ++s2) {
    const float mu = __uint_as_float(mm[s2]), rs = __uint_as_float(rr[s2]);
#pragma unroll
    for (int g = 0; g < 2; ++g) {
#pragma unroll
      for (int e = 0; e < 4; ++e) {
        const float a = bflo(x[s2][g][e]), b = bfhi(x[s2][g][e]);
        x[s2][g][e] = pk2(a + (a - mu) * rs * bflo(yv[s2][g][e]), b + (b - mu) * rs * bfhi(yv[s2][g][e]));
      }
      st16(rw, OFF_X3 + (((n0 >> 3) + 2 * hf + g) * S + s0 + 32 * s2 + r) * 16, x[s2][g]);
    }
  }
  Red[256 + c * 64 + lane] = rowpart();
  cx.half_sync(4);
  if (c == 0) {
    const f32x2 t = (Red[256 + lane] + Red[320 + lane]) + (Red[384 + lane] + Red[448 + lane]);
    st8(rw, OFF_ST3 + ((sb * 8 + nb) * 64 + lane) * 8, __builtin_bit_cast(u32x2, t));
  }
  STAMPW(kid, 6, 4);
  wait_vm<0>();
  cx.half_sync(5);
  STAMPW(kid, 7, 4);
  if (c == 0) {
    cx.signal(FL_X3 + sb * 8 + nb);
    cx.await<PDELAY>(FL_X3 + ((xcd & 1) * 4 + (idx & 3)) * 32 + (lane & 31), lane < 32);
  }
  STAMPW(kid, 5, 4);
  BAR();
}

__global__ __launch_bounds__(512, 2) void fused_kernel(const uint16_t* __restrict__ q, const uint16_t* __restrict__ k,
                                                       const uint16_t* __restrict__ v, const uint16_t* __restrict__ shift_k,
                                                       const uint16_t* __restrict__ ssm_k, const uint16_t* __restrict__ ssm_D,
                                                       const uint16_t* __restrict__ wqkv, const uint16_t* __restrict__ wo,
                                                       char* __restrict__ ws, char* __restrict__ out) {
  extern __shared__ __attribute__((aligned(16))) char smem[];
  unsigned long long pc;
  asm volatile("s_getpc_b64 %0" : "=s"(pc));
  STAMP(0, 0);
  Cx c;
  c.ws = ws; c.smem = smem; c.rw = mk_rsrc(ws, WS_USED); c.rc = mk_rsrc((void*)(pc & ~255ull), CODE_BYTES);
  c.lane = threadIdx.x & 63; c.w = __builtin_amdgcn_readfirstlane(threadIdx.x >> 6); c.tid = threadIdx.x & 255; c.bid = blockIdx.x;
  if (c.w < 4) {
    c.mark(0);
    front_m(c, k, v, shift_k, ssm_k, ssm_D); c.mark(17);
    c.fresh(); g1_m<0>(c); c.fresh(); g2_m<0>(c);
    c.fresh(); g1_m<1>(c); c.fresh(); g2_m<1>(c);
    c.endspin();
  } else {
    c.mark(0);
    front_h(c, q, wqkv, wo); c.mark(2);
    c.fresh(); g1_h<0>(c); c.mark(3); c.fresh(); g2_h<0>(c, out); c.mark(4);
    c.fresh(); g1_h<1>(c); c.mark(5); c.fresh(); g2_h<1>(c, out); c.mark(6);
    c.endspin();
  }
}

extern "C" void kernel_launch(void* const* d_in, const int* in_sizes, int n_in, void* d_out, int out_size, void* d_ws,
                              size_t ws_size, hipStream_t stream) {
  (void)hipFuncSetAttribute((const void*)fused_kernel, hipFuncAttributeMaxDynamicSharedMemorySize, LDS_BYTES);
  fused_kernel<<<256, 512, LDS_BYTES, stream>>>((const uint16_t*)d_in[0], (const uint16_t*)d_in[1], (const uint16_t*)d_in[2],
                                                (const uint16_t*)d_in[3], (const uint16_t*)d_in[4], (const uint16_t*)d_in[5],
                                                (const uint16_t*)d_in[7], (const uint16_t*)d_in[8], (char*)d_ws, (char*)d_out);
}
```

```cpp
#include <hip/hip_runtime.h>
#include <stdint.h>

typedef __attribute__((ext_vector_type(8))) short bf16x8;
typedef __attribute__((ext_vector_type(4))) short s16x4;
typedef __attribute__((ext_vector_type(4))) float f32x4;
typedef __attribute__((ext_vector_type(2))) float f32x2;
typedef __attribute__((ext_vector_type(16))) float f32x16;
typedef __attribute__((ext_vector_type(4))) unsigned u32x4;
typedef __attribute__((ext_vector_type(2))) unsigned u32x2;
typedef __attribute__((ext_vector_type(2))) __bf16 bf16x2_t;
typedef __amdgpu_buffer_rsrc_t rsrc_t;
#define DI __device__ __forceinline__
#define LAS __attribute__((address_space(3)))
#define STAMPW(kid, slot, wv) do {} while (0)
#define DRAIN() do {} while (0)
#define STAMP(kid, slot) STAMPW(kid, slot, 0)

constexpr int S = 2048, D = 1024;
constexpr unsigned MB = 1024 * 1024;
constexpr unsigned OFF_UB = 0,
                   OFF_X0 = 4 * MB,
                   OFF_W8 = 8 * MB,
                   OFF_WOF = 14 * MB,
                   OFF_Y = 16 * MB,
                   OFF_X1 = 20 * MB,
                   OFF_PQ = 24 * MB,
                   OFF_X3 = 32 * MB,
                   OFF_SM = 36 * MB, WS_USED = 38 * MB;
constexpr int KV_LAYER = 32 * 1024 + 32 * 32;
constexpr unsigned OFF_ST0 = OFF_SM,
                   OFF_CSP = OFF_ST0 + 16384,
                   OFF_KV = OFF_CSP + 16 * 3072 * 4,
                   OFF_GX = OFF_KV + 2 * KV_LAYER * 4,
                   OFF_GX2 = OFF_GX + 65536,
                   OFF_ST3 = OFF_GX2 + 131072,
                   OFF_EPO = OFF_ST3 + 131072,
                   OFF_FLG = 37 * MB;
constexpr int FL_U = 0, FL_Q = 1024, FL_X1 = 2048, FL_G1 = 2304  , FL_X3 = 2816;
constexpr unsigned CODE_BYTES = 56 * 1024;
constexpr int SPIN_MAX = 1 << 17;
#define FREP 8
#define PDELAY 20
#define XDELAY 10
#define PSLEEP 8
#define YLATE 1
#define KVW 8
#define NTW 0
#define NTA 0
#define WMODE 0

DI float bflo(unsigned u) { return __uint_as_float(u << 16); }
DI float bfhi(unsigned u) { return __uint_as_float(u & 0xffff0000u); }
DI unsigned pk2v(f32x2 v) { return __builtin_bit_cast(unsigned, __builtin_convertvector(v, bf16x2_t)); }
DI unsigned pk2(float a, float b) { return pk2v((f32x2){a, b}); }
DI float rbf(float a) { return bflo(pk2(a, a)); }
DI bf16x8 as_frag(u32x4 v) { return __builtin_bit_cast(bf16x8, v); }
DI f32x2 lo2(f32x4 v) { return (f32x2){v[0], v[1]}; }
DI f32x2 hi2(f32x4 v) { return (f32x2){v[2], v[3]}; }
DI float dot2(unsigned a, unsigned b, float c) {
  return __builtin_amdgcn_fdot2_f32_bf16(__builtin_bit_cast(bf16x2_t, a), __builtin_bit_cast(bf16x2_t, b), c, false);
}
#define MFMA16(a, b, c) __builtin_amdgcn_mfma_f32_16x16x32_bf16((a), (b), (c), 0, 0, 0)
#define MFMA32(a, b, c) __builtin_amdgcn_mfma_f32_32x32x16_bf16((a), (b), (c), 0, 0, 0)
#define WAIT_LGKM0() asm volatile("s_waitcnt lgkmcnt(0)" ::: "memory")
#define BAR() do { __builtin_amdgcn_s_barrier(); asm volatile("" ::: "memory"); } while (0)
#define SB() __builtin_amdgcn_sched_barrier(0)
#define KEEP(x) asm volatile("" ::"v"(x))
template <int N> DI void wait_vm() { asm volatile("s_waitcnt vmcnt(%0)" ::"i"(N) : "memory"); }
DI rsrc_t mk_rsrc(void* p, unsigned bytes) { return __builtin_amdgcn_make_buffer_rsrc(p, 0, bytes, 0x00020000); }
DI void glds16(const void* g, char* l) {
  __builtin_amdgcn_global_load_lds((const __attribute__((address_space(1))) void*)g, (LAS void*)l, 16, 0, NTW ? 2 : 0);
}
template <class T> DI T ldin(const void* p) { return NTA ? __builtin_nontemporal_load((const T*)p) : *(const T*)p; }
DI void dma16(rsrc_t r, char* l, unsigned voff, unsigned soff) { __builtin_amdgcn_raw_ptr_buffer_load_lds(r, (LAS void*)l, 16, voff, soff, 0, 16); }
DI u32x4 ld16(rsrc_t r, unsigned voff, unsigned soff) { return __builtin_amdgcn_raw_buffer_load_b128(r, voff, soff, 16); }
DI u32x2 ld8(rsrc_t r, unsigned voff, unsigned soff) { return __builtin_amdgcn_raw_buffer_load_b64(r, voff, soff, 16); }
DI void st16(rsrc_t r, unsigned off, u32x4 v) { __builtin_amdgcn_raw_buffer_store_b128(v, r, off, 0, 16); }
DI void st8(rsrc_t r, unsigned off, u32x2 v) { __builtin_amdgcn_raw_buffer_store_b64(v, r, off, 0, 16); }
DI void put8(char* p, u32x2 v) { __hip_atomic_store((unsigned long long*)p, __builtin_bit_cast(unsigned long long, v), __ATOMIC_RELAXED, __HIP_MEMORY_SCOPE_AGENT); }
DI u32x2 poll8(const char* p) { return __builtin_bit_cast(u32x2, __hip_atomic_load((const unsigned long long*)p, __ATOMIC_RELAXED, __HIP_MEMORY_SCOPE_AGENT)); }
DI void publish8(char* p, float sm, float sq) {
  unsigned qb = __float_as_uint(sq);
  if (qb == ~0u) qb = 0x7fc00000u;
  put8(p, (u32x2){__float_as_uint(sm), qb});
}
DI float opq(float m) { asm("" : "+v"(m)); return m; }
template <int N> DI float row_ror(float x) {
  return __int_as_float(__builtin_amdgcn_update_dpp(0, __float_as_int(x), 0x120 + N, 0xf, 0xf, false));
}
DI float swap16_sum(float a, float b) { const auto p = __builtin_amdgcn_permlane16_swap(__float_as_uint(a), __float_as_uint(b), false, false); return __uint_as_float(p[0]) + __uint_as_float(p[1]); }
DI float swap32_sum(float a, float b) { const auto p = __builtin_amdgcn_permlane32_swap(__float_as_uint(a), __float_as_uint(b), false, false); return __uint_as_float(p[0]) + __uint_as_float(p[1]); }

DI f32x2 ln_stats(float s, float q) {
  const float m = s * (1.0f / 1024.0f);
  const float v = fmaxf(q * (1.0f / 1024.0f) - m * m, 0.0f);
  const float sd = rbf(sqrtf(rbf(rbf(v) + 1e-5f)));
  return (f32x2){rbf(m), 1.0f / sd};
}
constexpr float LOG2E = 1.44269504088896f;
DI f32x2 phi2(f32x2 t2) {
  const f32x2 a = t2 * LOG2E - 1e-6f * LOG2E;
  f32x2 e, m;
  e[0] = __builtin_amdgcn_fmed3f(__builtin_amdgcn_exp2f(a[0]), 0.0f, 1.0f);
  e[1] = __builtin_amdgcn_fmed3f(__builtin_amdgcn_exp2f(a[1]), 0.0f, 1.0f);
  m[0] = fmaxf(t2[0], 1e-6f);
  m[1] = fmaxf(t2[1], 1e-6f);
  return m + e;
}

constexpr int LDS_BYTES = 163840, LDS_CNT = LDS_BYTES - 64;
constexpr int CV_PAD = 512, CV_HA = 286 * 16, CV_HB = CV_HA + 4160, CV_CH = CV_HB + 4128 + 32;
constexpr int CV_WB = CV_PAD + 4 * CV_CH, CV_X0 = CV_WB + 4 * 8192, CV_ST = CV_X0 + 16384, CV_BN = CV_ST + 16384;
constexpr int G1_AB = 32768, G1_STAGE = G1_AB + 12288, G1_SCR = 3 * G1_STAGE, G1_NP = 11;
constexpr int G2_Z = 131072, G2_RED = G2_Z + 2048;

struct Cx {
  char* ws; char* smem; rsrc_t rw, rc; int lane, w, tid, bid; u32x2 tag;
  DI void mark(int) {}
  DI void endspin() {}
  DI void half_sync(int n) const {
    const unsigned c = (unsigned)(size_t)(LAS char*)(smem + LDS_CNT + (w >> 2) * 16);
    asm volatile("s_waitcnt lgkmcnt(0)" ::: "memory");
    if (lane == 0) asm volatile("ds_add_u32 %0, %1" ::"v"(c), "v"(1u) : "memory");
    unsigned x;
    do { asm volatile("ds_read_b32 %0, %1\n\ts_waitcnt lgkmcnt(0)" : "=v"(x) : "v"(c) : "memory"); } while (__builtin_amdgcn_readfirstlane(x) < 4u * n);
  }
  DI void signal(int idx) const { if (lane < FREP) put8(ws + OFF_FLG + (lane * 3072 + idx) * 32, tag); }
  template <int DELAY> DI void await(int idx, bool pend) const {
    const char* fp = ws + OFF_FLG + ((bid & (FREP - 1)) * 3072 + idx) * 32;
    if (DELAY) __builtin_amdgcn_s_sleep(DELAY);
#pragma unroll 1
    for (int spin = 0; spin < SPIN_MAX; ++spin) {
      if (pend) { const u32x2 f = poll8(fp); pend = f[0] != tag[0] || f[1] != tag[1]; }
      if (!__any(pend)) break;
      __builtin_amdgcn_s_sleep(PSLEEP);
    }
    asm volatile("" ::: "memory");
  }
  DI unsigned touch_code() const {
    unsigned x;
    asm volatile("buffer_load_dword %0, %1, %2, 0 offen" : "=v"(x) : "v"(((bid >> 3) * 16 + (lane & 15)) * 128), "s"(rc) : "memory");
    return x;
  }
  DI void fresh() {
    asm volatile("" : "+v"(tid), "+s"(w), "+s"(bid));
    lane = tid & 63;
  }
  DI void set_tag(u32x2 e) {
    tag = (u32x2){__builtin_amdgcn_readfirstlane(e[0]) * 1664525u + 1013904223u, __builtin_amdgcn_readfirstlane(e[1]) * 22695477u + 1u};
  }
};

DI void conv_store(const Cx& c) {
  const int j = (c.bid & 7) * 32 + (c.bid >> 3), t = c.tid + (c.w >> 2) * 256;
  const char* smem = c.smem;
  u32x4 yo[2];
#pragma unroll
  for (int i = 0; i < 2; ++i) {
    const int p = t + 512 * i;
    unsigned yy[4];
#pragma unroll
    for (int ch = 0; ch < 4; ++ch) yy[ch] = *(const unsigned*)(smem + CV_PAD + ch * CV_CH + CV_HA + p * 4);
    yo[i] = (u32x4){__builtin_amdgcn_perm(yy[1], yy[0], 0x05040100u), __builtin_amdgcn_perm(yy[3], yy[2], 0x05040100u),
                    __builtin_amdgcn_perm(yy[1], yy[0], 0x07060302u), __builtin_amdgcn_perm(yy[3], yy[2], 0x07060302u)};
    const u32x4 x0v = *(const u32x4*)(smem + CV_X0 + p * 16);
    const f32x4 stv = *(const f32x4*)(smem + CV_ST + p * 16);
    u32x4 xo;
#pragma unroll
    for (int e = 0; e < 4; ++e) {
      const float m = stv[e & 2], rs = stv[(e & 2) + 1], xa = bflo(x0v[e]), xb = bfhi(x0v[e]);
      xo[e] = pk2(xa + (xa - m) * rs * bflo(yo[i][e]), xb + (xb - m) * rs * bfhi(yo[i][e]));
    }
    st16(c.rw, OFF_X1 + (j * 1024 + p) * 16, xo);
  }
  if (!YLATE) {
    SB();
#pragma unroll
    for (int i = 0; i < 2; ++i) st16(c.rw, OFF_Y + (j * 1024 + t + 512 * i) * 16, yo[i]);
    wait_vm<2>();
  } else wait_vm<0>();
  WAIT_LGKM0();
  BAR();
  if (c.w == 0) c.signal(FL_X1 + c.bid);
  if (YLATE) {
    SB();
#pragma unroll
    for (int i = 0; i < 2; ++i) st16(c.rw, OFF_Y + (j * 1024 + t + 512 * i) * 16, yo[i]);
  }
}

DI void front_m(Cx& c, const uint16_t* k, const uint16_t* v, const uint16_t* shift_k, const uint16_t* ssm_k, const uint16_t* ssm_D) {
  const int lane = c.lane, w = c.w, tid = c.tid, bid = c.bid, s0 = bid * 8, j = (bid & 7) * 32 + (bid >> 3);
  char* smem = c.smem;
  u32x4* bounce = (u32x4*)(smem + CV_BN) + w * 256;
  u32x4 uu[4], hh[4];
  unsigned hn[4];
  float Dc;
  u32x2 e0;
  {
    u32x2 kk[11], vv[8];
#pragma unroll
    for (int r = 0; r < 11; ++r) kk[r] = ldin<u32x2>(k + (size_t)((r < 3) ? max(s0 - 3 + r, 0) : s0 - 3 + r) * D + tid * 4);
#pragma unroll
    for (int r = 0; r < 8; ++r) vv[r] = ldin<u32x2>(v + (size_t)(s0 + r) * D + tid * 4);
    const u32x4 wa = *(const u32x4*)(shift_k + tid * 16), wb = *(const u32x4*)(shift_k + tid * 16 + 8);
    SB();
    if (tid < 2) *(unsigned*)(smem + LDS_CNT + tid * 16) = 0u;
    WAIT_LGKM0();
    wait_vm<KVW>();
    BAR();
    STAMP(0, 1);
    SB();
    const uint16_t* hrow = ssm_k + (size_t)(4 * j + w) * S;
#pragma unroll
    for (int i = 0; i < 4; ++i) {
      const int ch = i * 64 + lane;
      hh[i] = ldin<u32x4>(hrow + ch * 8);
      hn[i] = ldin<unsigned>(hrow + min(ch * 8 + 8, S - 2));
    }
    Dc = bflo((unsigned)ssm_D[4 * j + w]);
    e0 = poll8(c.ws + OFF_EPO);
    SB();
    const unsigned hm = bid ? ~0u : 0u;
#pragma unroll
    for (int r = 0; r < 3; ++r) { kk[r][0] &= hm; kk[r][1] &= hm; }
    const int sw = (lane >> 1) & 3;
#pragma unroll
    for (int ch = 0; ch < 4; ++ch) {
      const unsigned w01 = (ch == 0) ? wa[0] : (ch == 1) ? wa[2] : (ch == 2) ? wb[0] : wb[2];
      const unsigned w23 = (ch == 0) ? wa[1] : (ch == 1) ? wa[3] : (ch == 2) ? wb[1] : wb[3];
      const float w0 = bflo(w01), w1 = bfhi(w01), w2 = bflo(w23), w3 = bfhi(w23);
      float kf[11], u[8];
#pragma unroll
      for (int r = 0; r < 11; ++r) kf[r] = (ch & 1) ? bfhi(kk[r][ch >> 1]) : bflo(kk[r][ch >> 1]);
#pragma unroll
      for (int r = 0; r < 8; ++r) {
        const float ks = kf[r] * w3 + kf[r + 1] * w2 + kf[r + 2] * w1 + kf[r + 3] * w0;
        u[r] = ks * ((ch & 1) ? bfhi(vv[r][ch >> 1]) : bflo(vv[r][ch >> 1]));
      }
      bounce[lane * 4 + (ch ^ sw)] = (u32x4){pk2(u[7], u[6]), pk2(u[5], u[4]), pk2(u[3], u[2]), pk2(u[1], u[0])};
    }
#pragma unroll
    for (int i = 0; i < 4; ++i) {
      const int u = 64 * i + lane;
      st16(c.rw, OFF_UB + (bid * 1024 + w * 256 + u) * 16, bounce[(u & ~3) | ((u & 3) ^ ((u >> 3) & 3))]);
    }
    STAMP(0, 2); c.mark(11);
    SB();
  }
  char* my = smem + CV_PAD + w * CV_CH;
  if (lane < 30) *(u32x4*)(my + lane * 16) = (u32x4){0u, 0u, 0u, 0u};
  if (lane >= 32 && lane < 34) *(u32x4*)(my + CV_HA + (lane - 32) * 16) = (u32x4){0u, 0u, 0u, 0u};
  if (lane == 34) *(u32x4*)(my + CV_HB) = (u32x4){0u, 0u, 0u, 0u};
  if (lane == 0) *(u32x4*)(my + CV_HB + 16) = (u32x4){0u, 0u, 0u, hh[0][0] << 16};
#pragma unroll
  for (int i = 0; i < 4; ++i) {
    const int ch = i * 64 + lane;
    *(u32x4*)(my + CV_HA + 32 + ch * 16) = hh[i];
    *(u32x4*)(my + CV_HB + 32 + ch * 16) =
        (u32x4){__builtin_amdgcn_alignbyte(hh[i][1], hh[i][0], 2), __builtin_amdgcn_alignbyte(hh[i][2], hh[i][1], 2),
                __builtin_amdgcn_alignbyte(hh[i][3], hh[i][2], 2), __builtin_amdgcn_alignbyte(hn[i], hh[i][3], 2)};
  }
  asm volatile("" : "+v"(Dc), "+v"(e0));
  c.set_tag(e0);
  wait_vm<0>();
  c.signal(FL_U + w * 256 + bid);
  STAMP(0, 3); c.mark(12);
  c.await<PDELAY>(FL_U + (j >> 6) * 256 + w * 64 + lane, true);
  STAMP(0, 4); c.mark(13);
#pragma unroll
  for (int i = 0; i < 4; ++i) uu[i] = ld16(c.rw, ((i * 16 + (lane >> 2)) * 1024 + (lane & 3)) * 16, OFF_UB + (w * 65536 + 4 * j) * 16);
#pragma unroll
  for (int i = 0; i < 4; ++i) *(u32x4*)(smem + CV_PAD + (lane & 3) * CV_CH + (30 + w * 64 + i * 16 + (lane >> 2)) * 16) = uu[i];
  const int t0 = lane & 15, kq = lane >> 4;
  const LAS unsigned* fbk[4];
#pragma unroll
  for (int i = 0; i < 4; ++i) {
    fbk[i] = (const LAS unsigned*)(my + ((t0 & 1) ? CV_HA : CV_HB)) + 4 * kq + ((1 + t0) >> 1) + 256 * i;
    asm volatile("" : "+v"(fbk[i]));
  }
  const char* gb = my + (2 * t0 + 3 - kq) * 16;
  f32x4 acc[8];
#pragma unroll
  for (int b = 0; b < 8; ++b) { acc[b] = (f32x4){0.f, 0.f, 0.f, 0.f}; asm volatile("" : "+v"(acc[b])); }
  c.half_sync(1);
  STAMP(1, 0); STAMP(1, 1); c.mark(14);
  bf16x8 F[2][8], G[2][8];
  u32x2 fh[8];
  auto rdn = [&](int buf, int g0, int n) {
    if (n == 0 || n > 16) { const int g1 = n ? n - 16 : 0; G[buf][g1] = *(const bf16x8*)(gb + (32 * g1 + 4 * (7 - g0)) * 16); }
    else {
      const int e1 = (n - 1) >> 1;
      const LAS unsigned* p = fbk[e1 >> 1] + 128 * (e1 & 1) + 16 * g0;
      if ((n - 1) & 1) F[buf][e1] = as_frag((u32x4){fh[e1][0], fh[e1][1], p[2], p[3]});
      else fh[e1] = (u32x2){p[0], p[1]};
    }
  };
  auto cstep = [&](int cb, int g0n) {
    int n = 0;
#pragma unroll
    for (int g1 = 0; g1 < 8; ++g1)
#pragma unroll
      for (int e1 = 0; e1 + g1 < 8; ++e1) {
        acc[g1 + e1] = MFMA16(F[cb][e1], G[cb][g1], acc[g1 + e1]);
        SB();
        if (n < 24) { rdn(cb ^ 1, g0n, n); SB(); }
        ++n;
      }
  };
#pragma unroll
  for (int n = 0; n < 24; ++n) rdn(0, 0, n);
  for (int gp = 0; gp < 4; ++gp) {
    cstep(0, 1);
    cstep(1, 2);
    gb -= 128;
#pragma unroll
    for (int i = 0; i < 4; ++i) fbk[i] += 32;
  }
  asm volatile("" ::"v"(acc[7]), "v"(acc[0]));
  STAMP(1, 2);
#pragma unroll
  for (int b = 0; b < 8; ++b) {
    const int s = 256 * b + 16 * t0 + 4 * kq;
    const u32x2 ur = *(const u32x2*)(my + (30 + (s >> 3)) * 16 + ((s & 4) ? 0 : 8));
    *(u32x2*)(my + CV_HA + s * 2) = (u32x2){pk2(acc[b][0] + bfhi(ur[1]) * Dc, acc[b][1] + bflo(ur[1]) * Dc),
                                            pk2(acc[b][2] + bfhi(ur[0]) * Dc, acc[b][3] + bflo(ur[0]) * Dc)};
  }
  WAIT_LGKM0();
  BAR();
  STAMP(1, 3); c.mark(15);
  conv_store(c);
  STAMP(1, 6); c.mark(16);
  const unsigned pf = c.touch_code();
  c.await<PDELAY>(FL_X1 + w * 64 + lane, true);
  KEEP(pf);
  STAMP(1, 7);
  BAR();
  if (bid == 0 && tid == 0) put8(c.ws + OFF_EPO, c.tag);
}

DI void front_h(Cx& c, const uint16_t* q, const uint16_t* wqkv, const uint16_t* wo) {
  const int lane = c.lane, w4 = c.w - 4, tid = c.tid, bid = c.bid, s0 = bid * 8, j = (bid & 7) * 32 + (bid >> 3);
  char* smem = c.smem;
  const rsrc_t rw = c.rw;
  u32x4* bounce = (u32x4*)(smem + CV_BN) + c.w * 256;
  float* red = (float*)smem;
  const int gw = bid * 4 + w4;
  char* buf = smem + CV_WB + w4 * 8192;
  const int g = lane >> 4, qr = (lane >> 2) & 3, p = lane & 3, rr = lane >> 3;
  const bool isw = gw < 768;
  const int cg = isw ? gw % 48 : (gw - 768) & 15, og = isw ? gw / 48 : (gw - 768) >> 4;
  auto w_loads = [&]() {
    if (isw) {
      const uint16_t* src = wqkv + (size_t)(og * 64 + rr) * 3072 + cg * 64 + (lane & 7) * 8;
#pragma unroll
      for (int i = 0; i < 8; ++i) glds16(src + (size_t)i * 8 * 3072, buf + i * 1024);
    } else {
      const uint16_t* src = wo + (size_t)(og * 64 + (rr & 3) + 16 * (rr >> 2)) * 1024 + cg * 64 + (lane & 7) * 8;
#pragma unroll
      for (int i = 0; i < 8; ++i) glds16(src + (size_t)((i >> 2) * 32 + (i & 3) * 4) * 1024, buf + i * 1024);
    }
  };
  u32x2 e0 = poll8(c.ws + OFF_EPO);
  const unsigned pf = c.touch_code();
  SB();
  BAR();
  u32x2 qq[8];
#pragma unroll
  for (int r = 0; r < 8; ++r) qq[r] = ldin<u32x2>(q + (size_t)(s0 + r) * D + tid * 4);
  SB();
  if (WMODE == 0) w_loads();
  SB();
  const int sw = (lane >> 1) & 3;
#pragma unroll
  for (int r = 0; r < 4; ++r) bounce[lane * 4 + (r ^ sw)] = (u32x4){qq[2 * r][0], qq[2 * r][1], qq[2 * r + 1][0], qq[2 * r + 1][1]};
#pragma unroll
  for (int i = 0; i < 4; ++i) {
    const int u = 64 * i + lane;
    st16(rw, OFF_X0 + (bid * 1024 + w4 * 256 + u) * 16, bounce[(u & ~3) | ((u & 3) ^ ((u >> 3) & 3))]);
  }
  if (tid < 80) st16(rw, OFF_GX + bid * 1280 + tid * 16, (u32x4){~0u, ~0u, ~0u, ~0u});
  SB();
  if (WMODE == 1) w_loads();
  SB();
  float a[16];
#pragma unroll
  for (int r = 0; r < 8; ++r) {
    a[2 * r] = dot2(qq[r][0], 0x3f803f80u, dot2(qq[r][1], 0x3f803f80u, 0.f));
    a[2 * r + 1] = dot2(qq[r][0], qq[r][0], dot2(qq[r][1], qq[r][1], 0.f));
  }
#pragma unroll
  for (int i = 0; i < 8; ++i) a[i] = swap32_sum(a[i], a[i + 8]);
#pragma unroll
  for (int i = 0; i < 4; ++i) a[i] = swap16_sum(a[i], a[i + 4]);
#pragma unroll
  for (int i = 0; i < 4; ++i) { a[i] += row_ror<1>(a[i]); a[i] += row_ror<2>(a[i]); a[i] += row_ror<4>(a[i]); a[i] += row_ror<8>(a[i]); }
  if ((lane & 15) == 0) *(f32x4*)&red[w4 * 16 + (lane >> 4) * 4] = (f32x4){a[0], a[1], a[2], a[3]};
  c.half_sync(1);
  if (tid < 8) {
    const float sm = (red[2 * tid] + red[16 + 2 * tid]) + (red[32 + 2 * tid] + red[48 + 2 * tid]);
    const float sq = (red[2 * tid + 1] + red[17 + 2 * tid]) + (red[33 + 2 * tid] + red[49 + 2 * tid]);
    st8(rw, OFF_ST0 + (s0 + tid) * 8, __builtin_bit_cast(u32x2, ln_stats(sm, sq)));
  }
  asm volatile("" : "+v"(e0));
  c.set_tag(e0);
  wait_vm<0>();
  KEEP(pf);
  c.signal(FL_Q + w4 * 256 + bid);
  STAMPW(0, 5, 4);
  if (WMODE == 2) w_loads();
  {
    const unsigned cm = (unsigned)(size_t)(LAS char*)(smem + LDS_CNT);
    unsigned x;
    do { __builtin_amdgcn_s_sleep(2); asm volatile("ds_read_b32 %0, %1\n\ts_waitcnt lgkmcnt(0)" : "=v"(x) : "v"(cm) : "memory"); } while (__builtin_amdgcn_readfirstlane(x) < 4u);
  }
  c.mark(1);
  if (WMODE == 3) w_loads();
  wait_vm<0>();
  const int hd = (cg & 15) * 2 + (lane >> 5), col = (cg >> 4) * 32 + (lane & 31);
  const unsigned dst = isw ? OFF_W8 + ((hd * 128 + og * 8) * 96 + col) * 16
                           : OFF_WOF + ((((og >> 1) * 32 + cg * 2 + (g >> 1)) * 8 + (og & 1) * 4) * 64 + (lane & 31)) * 16;
  const unsigned dstep = isw ? 1536 : 512;
  const unsigned la = (unsigned)(size_t)(LAS char*)buf + qr * 128 + (isw ? g * 32 + p * 8 : (g >> 1) * 64 + (p & 1) * 32 + ((g & 1) * 2 + (p >> 1)) * 8);
  u32x2 lo[8], hi[8];
#pragma unroll
  for (int i = 0; i < 8; ++i) {
    lo[i] = __builtin_bit_cast(u32x2, __builtin_amdgcn_ds_read_tr16_b64_v4i16((LAS s16x4*)(size_t)(la + i * 1024)));
    hi[i] = __builtin_bit_cast(u32x2, __builtin_amdgcn_ds_read_tr16_b64_v4i16((LAS s16x4*)(size_t)(la + i * 1024 + 512)));
  }
#pragma unroll
  for (int i = 0; i < 8; ++i) st16(rw, dst + i * dstep, (u32x4){lo[i][0], lo[i][1], hi[i][0], hi[i][1]});
  if (lane < 16 + 1 - (gw & 1)) st16(rw, OFF_KV + (gw * 33 / 2 + (gw & 1) + lane) * 16, (u32x4){0u, 0u, 0u, 0u});
  if (isw) {
    float c0 = 0.f, c1 = 0.f;
#pragma unroll
    for (int i = 0; i < 8; ++i)
#pragma unroll
      for (int e = 0; e < 2; ++e) {
        c0 = opq(opq(c0 + bflo(lo[i][e])) + bflo(hi[i][e]));
        c1 = opq(opq(c1 + bfhi(lo[i][e])) + bfhi(hi[i][e]));
      }
    __builtin_amdgcn_raw_buffer_store_b32(__float_as_uint(c0 + c1), rw, OFF_CSP + ((hd * 96 + col) * 16 + og) * 4, 0, 16);
  }
  STAMPW(1, 4, 4);
  c.await<0>(FL_Q + w4 * 64 + lane, true);
  c.await<0>(FL_Q + (j >> 6) * 256 + w4 * 64 + lane, j >= 64);
  SB();
#pragma unroll
  for (int i = 0; i < 4; ++i) {
    const int pp = w4 * 256 + i * 64 + lane;
    dma16(rw, smem + CV_X0 + (w4 * 4 + i) * 1024, ((pp >> 2) * 1024 + (pp & 3)) * 16, OFF_X0 + j * 64);
    dma16(rw, smem + CV_ST + (w4 * 4 + i) * 1024, pp * 16, OFF_ST0);
  }
  SB();
  wait_vm<0>();
  STAMPW(1, 5, 4);
  BAR();
  conv_store(c);
  BAR();
}

template <int L> DI void g1_m(const Cx& c) {
  constexpr bool C4 = L == 0;
  constexpr int kid = 2 + 2 * L;
  const int lane = c.lane, w = c.w, tid = c.tid, xcd = c.bid & 7, idx = c.bid >> 3;
  const int band = (xcd & 1) * 4 + (idx & 3), h = (xcd >> 1) * 8 + (idx >> 2), r = lane & 15, kq = lane >> 4;
  char* smem = c.smem;
  const float* Rs = (const float*)(smem + G1_SCR);
  const float* MRs = Rs + 256;
  const float* CSs = Rs + 512;
  float* P = (float*)(smem + G1_SCR) + 640;
  const char* ab = smem + kq * 4096 + ((C4 ? 32 : 64) * w + r) * 16;
  const char* bb = smem + G1_AB + kq * 1536 + r * 16;
  STAMP(kid, 0);
  f32x4 acc[4][6];
#pragma unroll
  for (int m = 0; m < 4; ++m)
#pragma unroll
    for (int p = 0; p < 6; ++p) { acc[m][p] = (f32x4){0.f, 0.f, 0.f, 0.f}; asm volatile("" : "+v"(acc[m][p])); }
  bf16x8 F[2][10];
  auto rd1 = [&](int buf, const char* a, const char* b, int t, int i) {
    if (i < 4) F[buf][i] = *(const bf16x8*)(a + t * 16384 + (C4 ? (i >> 1) * 256 + (i & 1) * 2048 : i * 256));
    else F[buf][i] = *(const bf16x8*)(b + t * 6144 + (i - 4) * 256);
  };
  auto fix = [&](int buf, int n) {
    if (C4) {
      u32x4 x = __builtin_bit_cast(u32x4, F[buf][2 * n]), y = __builtin_bit_cast(u32x4, F[buf][2 * n + 1]);
      asm("v_swap_b32 %0, %1" : "+v"(x[2]), "+v"(y[0]));
      asm("v_swap_b32 %0, %1" : "+v"(x[3]), "+v"(y[1]));
      F[buf][2 * n] = as_frag(x); F[buf][2 * n + 1] = as_frag(y);
      SB();
    }
  };
  auto step = [&](int cb, const char* a, const char* b, int t, bool rd) {
    const int ord[10] = {0, 1, 4, 5, 6, 7, 8, 9, 2, 3};
#pragma unroll
    for (int i = 0; i < 24; ++i) {
      const int m = i / 6, p = i % 6;
      if (p < 2) acc[m][p] = MFMA16(F[cb][4 + p], F[cb][m], acc[m][p]);
      else acc[m][p] = MFMA16(F[cb][m], F[cb][4 + p], acc[m][p]);
      SB();
      if (rd && (i & 1) && (i >> 1) < 10) { rd1(cb ^ 1, a, b, t, ord[i >> 1]); SB(); }
      if (i == 6) fix(cb, 1);
      if (rd && i == 20) fix(cb ^ 1, 0);
    }
  };
  BAR();
  STAMP(kid, 1);
#pragma unroll
  for (int i = 0; i < 10; ++i) rd1(0, ab, bb, 0, i);
  fix(0, 0);
  asm volatile("s_nop 1");
  SB();
  int so = 0;
  for (int it = 0; it < 15; ++it) {
    step(0, ab + so, bb + so, 1, true);
    WAIT_LGKM0();
    BAR();
    so = (so == 2 * G1_STAGE) ? 0 : so + G1_STAGE;
    step(1, ab + so, bb + so, 0, true);
  }
  step(0, ab + so, bb + so, 1, true);
  BAR();
  step(1, ab, bb, 0, false);
  STAMP(kid, 2);
  f32x4 Z[2][2], Zz[2];
#pragma unroll
  for (int i = 0; i < 2; ++i) { Z[i][0] = (f32x4){0.f, 0.f, 0.f, 0.f}; Z[i][1] = Z[i][0]; Zz[i] = Z[i][0]; }
  {
    float ck[4];
#pragma unroll
    for (int p = 0; p < 4; ++p) ck[p] = CSs[32 + p * 16 + r];
    const bf16x8 ones = as_frag((u32x4){0x3f803f80u, 0x3f803f80u, 0x3f803f80u, 0x3f803f80u});
#pragma unroll
    for (int jj = 0; jj < 2; ++jj) {
      u32x4 fr[4];
#pragma unroll
      for (int mm = 0; mm < 2; ++mm) {
        const int m = 2 * jj + mm;
        const f32x4 rs = *(const f32x4*)(Rs + 64 * w + 16 * m + 4 * kq), mr = *(const f32x4*)(MRs + 64 * w + 16 * m + 4 * kq);
#pragma unroll
        for (int p = 0; p < 2; ++p) {
          fr[p][2 * mm] = pk2v(phi2(lo2(acc[m][2 + p]) * lo2(rs) + (lo2(mr) * ck[p] + 1e-6f)));
          fr[p][2 * mm + 1] = pk2v(phi2(hi2(acc[m][2 + p]) * hi2(rs) + (hi2(mr) * ck[p] + 1e-6f)));
          fr[2 + p][2 * mm] = pk2v(lo2(acc[m][4 + p]) * lo2(rs) + lo2(mr) * ck[2 + p]);
          fr[2 + p][2 * mm + 1] = pk2v(hi2(acc[m][4 + p]) * hi2(rs) + hi2(mr) * ck[2 + p]);
        }
      }
#pragma unroll
      for (int dh = 0; dh < 2; ++dh) {
        Z[0][dh] = MFMA16(as_frag(fr[dh]), as_frag(fr[2]), Z[0][dh]);
        Z[1][dh] = MFMA16(as_frag(fr[dh]), as_frag(fr[3]), Z[1][dh]);
        Zz[dh] = MFMA16(as_frag(fr[dh]), ones, Zz[dh]);
      }
    }
  }
  {
    float* mine = P + w * 1056;
#pragma unroll
    for (int i = 0; i < 4; ++i) *(f32x4*)(mine + i * 256 + lane * 4) = Z[i >> 1][i & 1];
    if (r == 0) { *(f32x4*)(mine + 1024 + 4 * kq) = Zz[0]; *(f32x4*)(mine + 1040 + 4 * kq) = Zz[1]; }
  }
  c.half_sync(2 * L + 2);
  STAMP(kid, 3);
  float* kvacc = (float*)(c.ws + OFF_KV) + L * KV_LAYER;
#pragma unroll
  for (int i = 0; i < 4; ++i) {
    const int e = tid + 256 * i;
    atomicAdd(kvacc + h * 1024 + e, (P[e] + P[1056 + e]) + (P[2112 + e] + P[3168 + e]));
  }
  if (tid < 32) atomicAdd(kvacc + 32768 + h * 32 + tid, (P[1024 + tid] + P[2080 + tid]) + (P[3136 + tid] + P[4192 + tid]));
  SB();
  {
    const f32x4 c0 = *(const f32x4*)(CSs + 4 * kq), c1 = *(const f32x4*)(CSs + 16 + 4 * kq);
#pragma unroll
    for (int m = 0; m < 4; ++m) {
      const float rs = Rs[64 * w + 16 * m + r], mr = MRs[64 * w + 16 * m + r];
      const u32x4 o = {pk2v(phi2(lo2(acc[m][0]) * rs + (lo2(c0) * mr + 1e-6f))), pk2v(phi2(hi2(acc[m][0]) * rs + (hi2(c0) * mr + 1e-6f))),
                       pk2v(phi2(lo2(acc[m][1]) * rs + (lo2(c1) * mr + 1e-6f))), pk2v(phi2(hi2(acc[m][1]) * rs + (hi2(c1) * mr + 1e-6f)))};
      st16(c.rw, OFF_PQ + L * 4 * MB + ((((h >> 2) * 128 + band * 16 + w * 4 + m) * 4 + (h & 3)) * 64 + lane) * 16, o);
    }
  }
  STAMP(kid, 6);
  wait_vm<0>();
  c.half_sync(2 * L + 3);
  STAMP(kid, 7);
  if (w == 0) c.signal(FL_G1 + L * 256 + h * 8 + band);
}

template <int L> DI void g1_h(const Cx& c) {
  constexpr bool C4 = L == 0;
  constexpr unsigned OFF_X = C4 ? OFF_X1 : OFF_X3;
  const int lane = c.lane, lw = c.w - 4, t = c.tid, xcd = c.bid & 7, idx = c.bid >> 3;
  const int band = (xcd & 1) * 4 + (idx & 3), h = (xcd >> 1) * 8 + (idx >> 2);
  char* smem = c.smem;
  char* ws = c.ws;
  const rsrc_t rw = c.rw;
  float* Rs = (float*)(smem + G1_SCR);
  const unsigned l0 = lane * 16;
  auto issue = [&](int st) {
    char* base = smem + (st % 3) * G1_STAGE;
#pragma unroll
    for (int p = 0; p < 8; ++p) {
      if (C4) dma16(rw, base + (8 * lw + p) * 1024, l0, OFF_X + ((st * 16 + lw * 4 + (p >> 1)) * 1024 + band * 128 + (p & 1) * 64) * 16);
      else dma16(rw, base + (8 * lw + p) * 1024, l0, OFF_X + ((st * 8 + lw * 2 + (p >> 2)) * S + band * 256 + (p & 3) * 64) * 16);
    }
#pragma unroll
    for (int p = 0; p < 3; ++p) dma16(rw, base + G1_AB + (3 * lw + p) * 1024, l0, OFF_W8 + (h * 128 + st * 8) * 1536 + (3 * lw + p) * 1024);
  };
  issue(0); issue(1);
  wait_vm<G1_NP>();
  BAR();
  issue(2);
  u32x4 xv[4];
  float v[4] = {0.f, 0.f, 0.f, 0.f};
  if (C4) {
    const u32x4* xp = (const u32x4*)(ws + OFF_X) + (size_t)(t >> 2) * 1024 + band * 128 + h * 4 + (t & 3);
    asm volatile("global_load_dwordx4 %0, %4, off sc1\n\tglobal_load_dwordx4 %1, %5, off sc1\n\tglobal_load_dwordx4 %2, %6, off sc1\n\tglobal_load_dwordx4 %3, %7, off sc1"
                 : "=&v"(xv[0]), "=&v"(xv[1]), "=&v"(xv[2]), "=&v"(xv[3]) : "v"(xp), "v"(xp + 65536), "v"(xp + 131072), "v"(xp + 196608) : "memory");
  }
#pragma unroll
  for (int i = 0; i < 13; ++i) {
    if (C4 && i < 2) wait_vm<G1_NP + 4>();
    else if (C4 && i == 2) asm volatile("s_waitcnt vmcnt(%4)" : "+v"(xv[0]), "+v"(xv[1]), "+v"(xv[2]), "+v"(xv[3]) : "i"(G1_NP) : "memory");
    else wait_vm<G1_NP>();
    BAR();
    issue(i + 3);
    if (C4 && i >= 2 && i < 6) {
      const u32x4 x = xv[i - 2];
#pragma unroll
      for (int e = 0; e < 4; ++e) {
        const float lo = bflo(x[e]), hi = bfhi(x[e]);
        v[e & 2] = opq(v[e & 2] + lo); v[(e & 2) + 1] = opq(fmaf(lo, lo, v[(e & 2) + 1]));
        v[e & 2] = opq(v[e & 2] + hi); v[(e & 2) + 1] = opq(fmaf(hi, hi, v[(e & 2) + 1]));
      }
      asm volatile("" : "+v"(v[0]), "+v"(v[1]), "+v"(v[2]), "+v"(v[3]));
    }
    if (C4 && i == 6) {
#pragma unroll
      for (int e = 0; e < 4; ++e) { v[e] += row_ror<4>(v[e]); v[e] += row_ror<8>(v[e]); v[e] = swap16_sum(v[e], v[e]); v[e] = swap32_sum(v[e], v[e]); }
      const bool up = (lane & 4) != 0;
      if (lane < 8) publish8(ws + OFF_GX + ((band * 4 + lw) * 256 + h * 8 + 2 * (lane & 3) + (lane >> 2)) * 8, up ? v[2] : v[0], up ? v[3] : v[1]);
    }
  }
  const char* gp = ws + (C4 ? OFF_GX + (band * 1024 + t) * 8 + 4096 : OFF_ST3 + ((band * 4 + lw) * 512 + lane) * 8);
  const char* cp = ws + OFF_CSP + (h * 96 + (t < 96 ? t : 0)) * 64;
  u32x2 g[8];
  f32x4 c0, c1, c2, c3;
  asm volatile("global_load_dwordx4 %0, %4, off sc1\n\tglobal_load_dwordx4 %1, %4, off offset:16 sc1\n\t"
               "global_load_dwordx4 %2, %4, off offset:32 sc1\n\tglobal_load_dwordx4 %3, %4, off offset:48 sc1"
               : "=&v"(c0), "=&v"(c1), "=&v"(c2), "=&v"(c3) : "v"(cp) : "memory");
  if (C4) {
    asm volatile("global_load_dwordx2 %0, %4, off offset:-4096 sc1\n\tglobal_load_dwordx2 %1, %4, off offset:-2048 sc1\n\t"
                 "global_load_dwordx2 %2, %4, off sc1\n\tglobal_load_dwordx2 %3, %4, off offset:2048 sc1"
                 : "=&v"(g[0]), "=&v"(g[1]), "=&v"(g[2]), "=&v"(g[3]) : "v"(gp) : "memory");
    wait_vm<G1_NP + 8>();
  } else {
    asm volatile("global_load_dwordx2 %0, %8, off sc1\n\tglobal_load_dwordx2 %1, %8, off offset:512 sc1\n\tglobal_load_dwordx2 %2, %8, off offset:1024 sc1\n\t"
                 "global_load_dwordx2 %3, %8, off offset:1536 sc1\n\tglobal_load_dwordx2 %4, %8, off offset:2048 sc1\n\tglobal_load_dwordx2 %5, %8, off offset:2560 sc1\n\t"
                 "global_load_dwordx2 %6, %8, off offset:3072 sc1\n\tglobal_load_dwordx2 %7, %8, off offset:3584 sc1"
                 : "=&v"(g[0]), "=&v"(g[1]), "=&v"(g[2]), "=&v"(g[3]), "=&v"(g[4]), "=&v"(g[5]), "=&v"(g[6]), "=&v"(g[7]) : "v"(gp) : "memory");
    wait_vm<G1_NP + 12>();
  }
  BAR();
  if (C4) asm volatile("s_waitcnt vmcnt(0)" : "+v"(g[0]), "+v"(g[1]), "+v"(g[2]), "+v"(g[3]), "+v"(c0), "+v"(c1), "+v"(c2), "+v"(c3)::"memory");
  else asm volatile("s_waitcnt vmcnt(0)" : "+v"(g[0]), "+v"(g[1]), "+v"(g[2]), "+v"(g[3]), "+v"(g[4]), "+v"(g[5]), "+v"(g[6]), "+v"(g[7]),
                    "+v"(c0), "+v"(c1), "+v"(c2), "+v"(c3)::"memory");
  BAR();
#pragma unroll 1
  for (int spin = 0; spin < SPIN_MAX; ++spin) {
    bool empty = false;
#pragma unroll
    for (int k = 0; k < (C4 ? 4 : 8); ++k) empty |= g[k][1] == ~0u;
    if (!__any(empty)) break;
    __builtin_amdgcn_s_sleep(4);
#pragma unroll
    for (int k = 0; k < (C4 ? 4 : 8); ++k) g[k] = poll8(gp + (C4 ? (k - 2) * 2048 : k * 512));
  }
  float sm = 0.f, sq = 0.f, cs = 0.f;
#pragma unroll
  for (int k = 0; k < (C4 ? 4 : 8); ++k) { sm = opq(sm + __uint_as_float(g[k][0])); sq = opq(sq + __uint_as_float(g[k][1])); }
#pragma unroll
  for (int k = 0; k < 4; ++k) cs = opq(opq(opq(opq(cs + c0[k]) + c1[k]) + c2[k]) + c3[k]);
  const f32x2 st = ln_stats(sm, sq);
  const int ts = C4 ? (t & ~31) + (t & 1) * 16 + ((t & 31) >> 1) : t;
  Rs[ts] = st[1]; Rs[256 + ts] = -st[0] * st[1];
  if (t < 96) Rs[512 + t] = cs;
  WAIT_LGKM0();
  BAR();
}

template <int L> DI void g2_m(const Cx& c) {
  constexpr int kid = 3 + 2 * L;
  constexpr unsigned KVO = OFF_KV + L * KV_LAYER * 4;
  const int lane = c.lane, w = c.w, xcd = c.bid & 7, idx = c.bid >> 3, sb = (xcd >> 1) * 8 + (idx & 7), kq = lane >> 4;
  const rsrc_t rw = c.rw;
  const unsigned l16 = lane * 16;
  u32x4* O = (u32x4*)c.smem + (L == 0 ? ((lane >> 3) & 1) * 64 + kq * 16 + (lane & 7) * 2 : lane);
  unsigned pf = c.touch_code();
  c.await<PDELAY>(FL_G1 + L * 256 + ((lane >> 3) * 4 + w) * 8 + (lane & 7), true);
  KEEP(pf);
  STAMP(kid, 0);
  constexpr int PL = 2, PD = PL + 1;
  u32x4 pq[PD][4];
  f32x4 kv[PD][4];
  const unsigned spq = OFF_PQ + L * 4 * MB + (sb * 16 + w) * 1024, skv = KVO + w * 4096;
  auto ld = [&](int i, int n) {
    if (n < 4) kv[i % PD][n] = __builtin_bit_cast(f32x4, ld16(rw, l16 + n * 1024, skv + i * 16384));
    else pq[i % PD][n - 4] = ld16(rw, l16, spq + (i * 512 + (n - 4) * 4) * 1024);
    SB();
  };
  const f32x4 zall = __builtin_bit_cast(f32x4, ld16(rw, (lane >> 3) * 512 + (lane & 7) * 16, KVO + 131072 + w * 128));
  SB();
#pragma unroll
  for (int n = 0; n < 8 * PL; ++n) ld(n >> 3, n & 7);
  u32x2* Zs = (u32x2*)(c.smem + G2_Z) + w * 64;
  Zs[lane] = (u32x2){pk2v(lo2(zall)), pk2v(hi2(zall))};
  const f32x4 zero = {0.f, 0.f, 0.f, 0.f}, eps = {1e-6f, 1e-6f, 1e-6f, 1e-6f};
  bf16x8 k0, k1, zb;
  auto prep = [&](int i) {
    const int b = i % PD;
    const u32x2 za = Zs[i * 8 + kq], zc = Zs[i * 8 + 4 + kq];
    k0 = as_frag((u32x4){pk2v(lo2(kv[b][0])), pk2v(hi2(kv[b][0])), pk2v(lo2(kv[b][1])), pk2v(hi2(kv[b][1]))});
    k1 = as_frag((u32x4){pk2v(lo2(kv[b][2])), pk2v(hi2(kv[b][2])), pk2v(lo2(kv[b][3])), pk2v(hi2(kv[b][3]))});
    zb = as_frag((u32x4){za[0], za[1], zc[0], zc[1]});
    asm volatile("" : "+v"(k0), "+v"(k1), "+v"(zb));
  };
  prep(0);
#pragma unroll
  for (int i = 0; i < 8; ++i) {
    const int b = i % PD;
    f32x4 x0[4], x1[4], dn[4];
    __builtin_amdgcn_s_setprio(2);
#pragma unroll
    for (int st = 0; st < 4; ++st) {
      dn[st] = MFMA16(zb, as_frag(pq[b][st]), eps);
      x0[st] = MFMA16(k0, as_frag(pq[b][st]), zero);
      x1[st] = MFMA16(k1, as_frag(pq[b][st]), zero);
    }
    __builtin_amdgcn_s_setprio(0);
    SB();
#pragma unroll
    for (int st = 0; st < 4; ++st) {
      const float inv = __builtin_amdgcn_rcpf(dn[st][0]);
      O[L == 0 ? ((i * 4 + w) * 4 + (st & 2)) * 64 + (st & 1) : ((i * 4 + w) * 4 + st) * 64] =
          (u32x4){pk2(opq(x0[st][0] * inv), opq(x0[st][1] * inv)), pk2(opq(x0[st][2] * inv), opq(x0[st][3] * inv)),
                  pk2(opq(x1[st][0] * inv), opq(x1[st][1] * inv)), pk2(opq(x1[st][2] * inv), opq(x1[st][3] * inv))};
      SB();
      if (i + PL < 8) { ld(i + PL, 2 * st); ld(i + PL, 2 * st + 1); }
    }
    if (i < 7) prep(i + 1);
    WAIT_LGKM0();
    BAR();
    if (i == 0) STAMP(kid, 1);
  }
  if (L == 0) {
    pf = c.touch_code();
    wait_vm<0>();
    KEEP(pf);
    BAR();
  }
}

template <int L> DI void g2_h(const Cx& cx, char* out) {
  constexpr int kid = 3 + 2 * L;
  const int lane = cx.lane, c = cx.w - 4, xcd = cx.bid & 7, idx = cx.bid >> 3;
  const int sb = (xcd >> 1) * 8 + (idx & 7), nb = (xcd & 1) * 4 + (idx >> 3);
  const int n0 = nb * 128 + c * 32, s0 = sb * 64, r = lane & 31, hf = lane >> 5;
  const rsrc_t rw = cx.rw;
  const unsigned l16 = lane * 16;
  constexpr int CD = 5, LA = 6;
  u32x4 wo[CD][8], of[8];
  const unsigned swo = OFF_WOF + (nb * 4 + c) * 8192;
  auto ldw = [&](int i, int n) { wo[i % CD][n] = ld16(rw, l16 + (n & 3) * 1024, swo + i * 262144 + (n >> 2) * 4096); SB(); };
#pragma unroll
  for (int n = 0; n < 8 * CD; ++n) ldw(n >> 3, n & 7);
  f32x16 acc[2];
#pragma unroll
  for (int i = 0; i < 16; ++i) { acc[0][i] = 0.f; acc[1][i] = 0.f; }
  asm volatile("" : "+v"(acc[0]), "+v"(acc[1]));
  u32x4 res[2][2], yv[2][2];
  auto ldres = [&](int k) {
    const int s2 = (k >> 1) & 1, g = k & 1;
    if (L == 1) { if (k < 4) res[s2][g] = ld16(rw, (2 * hf * S + r) * 16 + s2 * 512, OFF_X3 + (((n0 >> 3) + g) * S + s0) * 16); }
    else {
      const unsigned so = (k < 4 ? OFF_X1 : OFF_Y) + (((n0 >> 2) + 2 * g) * S + s0) * 8, vo = (4 * hf * S + r) * 8 + s2 * 256;
      const u32x2 lo = ld8(rw, vo, so), hi = ld8(rw, vo, so + S * 8);
      if (k < 4) res[s2][g] = (u32x4){lo[0], lo[1], hi[0], hi[1]}; else yv[s2][g] = (u32x4){lo[0], lo[1], hi[0], hi[1]};
    }
    SB();
  };
  const u32x4* Ob = (const u32x4*)cx.smem + (r >> 4) * 64 + hf * 16 + (r & 15);
  auto rdo = [&](int f) { const int u = f & 15; of[f & 7] = Ob[((f >> 4) * 16 + (u >> 2) * 4 + (u & 1) * 2) * 64 + ((u >> 1) & 1) * 32]; SB(); };
  __builtin_amdgcn_s_setprio(1);
  BAR();
#pragma unroll
  for (int f = 0; f < LA; ++f) rdo(f);
#pragma unroll
  for (int i = 0; i < 8; ++i)
#pragma unroll
    for (int n = 0; n < 16; ++n) {
      const int f = i * 16 + n;
      acc[n & 1] = MFMA32(as_frag(wo[i % CD][n >> 1]), as_frag(of[f & 7]), acc[n & 1]);
      SB();
      if (f + LA < 128) { if (((f + LA) & 15) == 0) BAR(); rdo(f + LA); }
      if ((n & 3) == 3 && i < 6) ldw(CD + (i >> 1), (i & 1) * 4 + (n >> 2));
      if (i == 4 && (n & 3) == 1) ldres(n >> 2);
      if (i == 5 && (n & 3) == 1) ldres(4 + (n >> 2));
    }
  __builtin_amdgcn_s_setprio(0);
  STAMPW(kid, 2, 4);
  u32x4 x[2][2];
#pragma unroll
  for (int s2 = 0; s2 < 2; ++s2)
#pragma unroll
    for (int g = 0; g < 2; ++g)
#pragma unroll
      for (int e = 0; e < 4; ++e) x[s2][g][e] = pk2(bflo(res[s2][g][e]) + acc[s2][8 * g + 2 * e], bfhi(res[s2][g][e]) + acc[s2][8 * g + 2 * e + 1]);
  if (L == 1) {
#pragma unroll
    for (int s2 = 0; s2 < 2; ++s2)
#pragma unroll
      for (int g = 0; g < 2; ++g) *(u32x4*)(out + ((size_t)(s0 + 32 * s2 + r) * D + n0 + 16 * hf + 8 * g) * 2) = x[s2][g];
    STAMPW(kid, 6, 4); DRAIN(); STAMPW(kid, 7, 4);
    return;
  }
  f32x2* Red = (f32x2*)(cx.smem + G2_RED);
  auto rowpart = [&]() {
    float v[4] = {0.f, 0.f, 0.f, 0.f};
#pragma unroll
    for (int e = 0; e < 8; ++e)
#pragma unroll
      for (int s2 = 0; s2 < 2; ++s2) { const unsigned u = x[s2][e >> 2][e & 3]; v[2 * s2] = dot2(u, 0x3f803f80u, v[2 * s2]); v[2 * s2 + 1] = dot2(u, u, v[2 * s2 + 1]); }
    return (f32x2){swap32_sum(v[0], v[2]), swap32_sum(v[1], v[3])};
  };
  Red[c * 64 + lane] = rowpart();
  cx.half_sync(2);
  if (c == 0) {
    char* gx = cx.ws + OFF_GX2 + (sb * 512 + lane) * 8;
    const f32x2 t = (Red[lane] + Red[64 + lane]) + (Red[128 + lane] + Red[192 + lane]);
    publish8(gx + nb * 512, t[0], t[1]);
    STAMPW(kid, 4, 4);
  }
  f32x2 xa[16], xy[16], yf[16];
#pragma unroll
  for (int e = 0; e < 16; ++e) {
    const unsigned xu = x[e >> 3][(e >> 2) & 1][e & 3], yu = yv[e >> 3][(e >> 2) & 1][e & 3];
    xa[e] = (f32x2){bflo(xu), bfhi(xu)}; yf[e] = (f32x2){bflo(yu), bfhi(yu)}; xy[e] = xa[e] * yf[e];
    asm volatile("" : "+v"(xa[e]), "+v"(yf[e]), "+v"(xy[e]));
  }
  if (c == 0) {
    const char* gx = cx.ws + OFF_GX2 + (sb * 512 + lane) * 8;
    __builtin_amdgcn_s_sleep(XDELAY);
    float sm, sq;
#pragma unroll 1
    for (int spin = 0; spin < SPIN_MAX; ++spin) {
      u32x2 g[8];
#pragma unroll
      for (int k = 0; k < 8; ++k) g[k] = poll8(gx + k * 512);
      bool empty = false;
      sm = 0.f; sq = 0.f;
#pragma unroll
      for (int k = 0; k < 8; ++k) { empty |= g[k][1] == ~0u; sm += __uint_as_float(g[k][0]); sq += __uint_as_float(g[k][1]); }
      if (!__any(empty)) break;
      __builtin_amdgcn_s_sleep(4);
    }
    const f32x2 t = ln_stats(sm, sq);
    Red[lane] = (f32x2){-t[0] * t[1], t[1]};
  }
  cx.half_sync(3);
  STAMPW(kid, 3, 4);
#pragma unroll
  for (int s2 = 0; s2 < 2; ++s2) {
    const f32x2 st = Red[32 * s2 + r];
#pragma unroll
    for (int g = 0; g < 2; ++g) {
#pragma unroll
      for (int e = 0; e < 4; ++e) { const int n = s2 * 8 + g * 4 + e; x[s2][g][e] = pk2v(xy[n] * st[1] + (yf[n] * st[0] + xa[n])); }
      st16(rw, OFF_X3 + (((n0 >> 3) + 2 * hf + g) * S + s0 + 32 * s2 + r) * 16, x[s2][g]);
    }
  }
  STAMPW(kid, 6, 4);
  Red[256 + c * 64 + lane] = rowpart();
  wait_vm<0>();
  cx.half_sync(4);
  if (c == 1) {
    const f32x2 t = (Red[256 + lane] + Red[320 + lane]) + (Red[384 + lane] + Red[448 + lane]);
    publish8(cx.ws + OFF_ST3 + ((sb * 8 + nb) * 64 + lane) * 8, t[0], t[1]);
  }
  STAMPW(kid, 7, 4);
  if (c == 0) {
    cx.signal(FL_X3 + sb * 8 + nb);
    cx.await<PDELAY>(FL_X3 + ((xcd & 1) * 4 + (idx & 3)) * 32 + (lane & 31), lane < 32);
  }
  STAMPW(kid, 5, 4);
  BAR();
}

__global__ __launch_bounds__(512, 2) void fused_kernel(const uint16_t* __restrict__ q, const uint16_t* __restrict__ k,
                                                       const uint16_t* __restrict__ v, const uint16_t* __restrict__ shift_k,
                                                       const uint16_t* __restrict__ ssm_k, const uint16_t* __restrict__ ssm_D,
                                                       const uint16_t* __restrict__ wqkv, const uint16_t* __restrict__ wo,
                                                       char* __restrict__ ws, char* __restrict__ out) {
  extern __shared__ __attribute__((aligned(16))) char smem[];
  unsigned long long pc;
  asm volatile("s_getpc_b64 %0" : "=s"(pc));
  STAMP(0, 0);
  Cx c;
  c.ws = ws; c.smem = smem; c.rw = mk_rsrc(ws, WS_USED); c.rc = mk_rsrc((void*)(pc & ~255ull), CODE_BYTES);
  c.lane = threadIdx.x & 63; c.w = __builtin_amdgcn_readfirstlane(threadIdx.x >> 6); c.tid = threadIdx.x & 255; c.bid = blockIdx.x;
  if (c.w < 4) {
    c.mark(0);
    front_m(c, k, v, shift_k, ssm_k, ssm_D); c.mark(17);
    c.fresh(); g1_m<0>(c); c.fresh(); g2_m<0>(c);
    c.fresh(); g1_m<1>(c); c.fresh(); g2_m<1>(c);
    c.endspin();
  } else {
    c.mark(0);
    front_h(c, q, wqkv, wo); c.mark(2);
    c.fresh(); g1_h<0>(c); c.mark(3); c.fresh(); g2_h<0>(c, out); c.mark(4);
    c.fresh(); g1_h<1>(c); c.mark(5); c.fresh(); g2_h<1>(c, out); c.mark(6);
    c.endspin();
  }
}

extern "C" void kernel_launch(void* const* d_in, const int* in_sizes, int n_in, void* d_out, int out_size, void* d_ws,
                              size_t ws_size, hipStream_t stream) {
  (void)hipFuncSetAttribute((const void*)fused_kernel, hipFuncAttributeMaxDynamicSharedMemorySize, LDS_BYTES);
  fused_kernel<<<256, 512, LDS_BYTES, stream>>>((const uint16_t*)d_in[0], (const uint16_t*)d_in[1], (const uint16_t*)d_in[2],
                                                (const uint16_t*)d_in[3], (const uint16_t*)d_in[4], (const uint16_t*)d_in[5],
                                                (const uint16_t*)d_in[7], (const uint16_t*)d_in[8], (char*)d_ws, (char*)d_out);
}
```

```cpp
#define OMODE 2
#include <hip/hip_runtime.h>
#include <stdint.h>

typedef __attribute__((ext_vector_type(8))) short bf16x8;
typedef __attribute__((ext_vector_type(4))) short s16x4;
typedef __attribute__((ext_vector_type(4))) float f32x4;
typedef __attribute__((ext_vector_type(2))) float f32x2;
typedef __attribute__((ext_vector_type(16))) float f32x16;
typedef __attribute__((ext_vector_type(4))) unsigned u32x4;
typedef __attribute__((ext_vector_type(2))) unsigned u32x2;
typedef __attribute__((ext_vector_type(2))) __bf16 bf16x2_t;
typedef __amdgpu_buffer_rsrc_t rsrc_t;
#define DI __device__ __forceinline__
#define LAS __attribute__((address_space(3)))
#define STAMPW(kid, slot, wv) do {} while (0)
#define DRAIN() do {} while (0)
#define STAMP(kid, slot) STAMPW(kid, slot, 0)

constexpr int S = 2048, D = 1024;
constexpr unsigned MB = 1024 * 1024;
constexpr unsigned OFF_UB = 0,
                   OFF_X0 = 4 * MB,
                   OFF_W8 = 8 * MB,
                   OFF_WOF = 14 * MB,
                   OFF_Y = 16 * MB,
                   OFF_X1 = 20 * MB,
                   OFF_PQ = 24 * MB,
                   OFF_X3 = 32 * MB,
                   OFF_SM = 36 * MB, WS_USED = 38 * MB;
constexpr int KV_LAYER = 32 * 1024 + 32 * 32;
constexpr unsigned OFF_ST0 = OFF_SM,
                   OFF_CSP = OFF_ST0 + 16384,
                   OFF_KV = OFF_CSP + 16 * 3072 * 4,
                   OFF_GX = OFF_KV + 2 * KV_LAYER * 4,
                   OFF_GX2 = OFF_GX + 65536,
                   OFF_ST3 = OFF_GX2 + 131072,
                   OFF_EPO = OFF_ST3 + 131072,
                   OFF_FLG = 37 * MB;
constexpr int FL_U = 0, FL_Q = 1024, FL_X1 = 2048, FL_G1 = 2304  , FL_X3 = 2816;
constexpr unsigned CODE_BYTES = 56 * 1024;
constexpr int SPIN_MAX = 1 << 17;
#define FREP 8
#define PDELAY 20
#define XDELAY 10
#define PSLEEP 8
#define YLATE 1
#define KVW 8
#define NTW 0
#define NTA 0
#define WMODE 0

DI float bflo(unsigned u) { return __uint_as_float(u << 16); }
DI float bfhi(unsigned u) { return __uint_as_float(u & 0xffff0000u); }
DI unsigned pk2v(f32x2 v) { return __builtin_bit_cast(unsigned, __builtin_convertvector(v, bf16x2_t)); }
DI unsigned pk2(float a, float b) { return pk2v((f32x2){a, b}); }
DI float rbf(float a) { return bflo(pk2(a, a)); }
DI bf16x8 as_frag(u32x4 v) { return __builtin_bit_cast(bf16x8, v); }
DI f32x2 lo2(f32x4 v) { return (f32x2){v[0], v[1]}; }
DI f32x2 hi2(f32x4 v) { return (f32x2){v[2], v[3]}; }
DI float dot2(unsigned a, unsigned b, float c) {
  return __builtin_amdgcn_fdot2_f32_bf16(__builtin_bit_cast(bf16x2_t, a), __builtin_bit_cast(bf16x2_t, b), c, false);
}
#define MFMA16(a, b, c) __builtin_amdgcn_mfma_f32_16x16x32_bf16((a), (b), (c), 0, 0, 0)
#define MFMA32(a, b, c) __builtin_amdgcn_mfma_f32_32x32x16_bf16((a), (b), (c), 0, 0, 0)
#define WAIT_LGKM0() asm volatile("s_waitcnt lgkmcnt(0)" ::: "memory")
#define BAR() do { __builtin_amdgcn_s_barrier(); asm volatile("" ::: "memory"); } while (0)
#define SB() __builtin_amdgcn_sched_barrier(0)
#define KEEP(x) asm volatile("" ::"v"(x))
template <int N> DI void wait_vm() { asm volatile("s_waitcnt vmcnt(%0)" ::"i"(N) : "memory"); }
DI rsrc_t mk_rsrc(void* p, unsigned bytes) { return __builtin_amdgcn_make_buffer_rsrc(p, 0, bytes, 0x00020000); }
DI void glds16(const void* g, char* l) {
  __builtin_amdgcn_global_load_lds((const __attribute__((address_space(1))) void*)g, (LAS void*)l, 16, 0, NTW ? 2 : 0);
}
template <class T> DI T ldin(const void* p) { return NTA ? __builtin_nontemporal_load((const T*)p) : *(const T*)p; }
DI void dma16(rsrc_t r, char* l, unsigned voff, unsigned soff) { __builtin_amdgcn_raw_ptr_buffer_load_lds(r, (LAS void*)l, 16, voff, soff, 0, 16); }
DI u32x4 ld16(rsrc_t r, unsigned voff, unsigned soff) { return __builtin_amdgcn_raw_buffer_load_b128(r, voff, soff, 16); }
DI u32x2 ld8(rsrc_t r, unsigned voff, unsigned soff) { return __builtin_amdgcn_raw_buffer_load_b64(r, voff, soff, 16); }
DI void st16(rsrc_t r, unsigned off, u32x4 v) { __builtin_amdgcn_raw_buffer_store_b128(v, r, off, 0, 16); }
DI void st8(rsrc_t r, unsigned off, u32x2 v) { __builtin_amdgcn_raw_buffer_store_b64(v, r, off, 0, 16); }
DI void put8(char* p, u32x2 v) { __hip_atomic_store((unsigned long long*)p, __builtin_bit_cast(unsigned long long, v), __ATOMIC_RELAXED, __HIP_MEMORY_SCOPE_AGENT); }
DI u32x2 poll8(const char* p) { return __builtin_bit_cast(u32x2, __hip_atomic_load((const unsigned long long*)p, __ATOMIC_RELAXED, __HIP_MEMORY_SCOPE_AGENT)); }
DI void publish8(char* p, float sm, float sq) {
  unsigned qb = __float_as_uint(sq);
  if (qb == ~0u) qb = 0x7fc00000u;
  put8(p, (u32x2){__float_as_uint(sm), qb});
}
DI float opq(float m) { asm("" : "+v"(m)); return m; }
template <int N> DI float row_ror(float x) {
  return __int_as_float(__builtin_amdgcn_update_dpp(0, __float_as_int(x), 0x120 + N, 0xf, 0xf, false));
}
DI float swap16_sum(float a, float b) { const auto p = __builtin_amdgcn_permlane16_swap(__float_as_uint(a), __float_as_uint(b), false, false); return __uint_as_float(p[0]) + __uint_as_float(p[1]); }
DI float swap32_sum(float a, float b) { const auto p = __builtin_amdgcn_permlane32_swap(__float_as_uint(a), __float_as_uint(b), false, false); return __uint_as_float(p[0]) + __uint_as_float(p[1]); }

DI f32x2 ln_stats(float s, float q) {
  const float m = s * (1.0f / 1024.0f);
  const float v = fmaxf(q * (1.0f / 1024.0f) - m * m, 0.0f);
  const float sd = rbf(sqrtf(rbf(rbf(v) + 1e-5f)));
  return (f32x2){rbf(m), 1.0f / sd};
}
constexpr float LOG2E = 1.44269504088896f;
DI f32x2 phi2(f32x2 t2) {
  const f32x2 a = t2 * LOG2E - 1e-6f * LOG2E;
  f32x2 e, m;
  e[0] = __builtin_amdgcn_fmed3f(__builtin_amdgcn_exp2f(a[0]), 0.0f, 1.0f);
  e[1] = __builtin_amdgcn_fmed3f(__builtin_amdgcn_exp2f(a[1]), 0.0f, 1.0f);
  m[0] = fmaxf(t2[0], 1e-6f);
  m[1] = fmaxf(t2[1], 1e-6f);
  return m + e;
}

constexpr int LDS_BYTES = 163840, LDS_CNT = LDS_BYTES - 64;
constexpr int CV_PAD = 512, CV_HA = 286 * 16, CV_HB = CV_HA + 4160, CV_CH = CV_HB + 4128 + 32;
constexpr int CV_WB = CV_PAD + 4 * CV_CH, CV_X0 = CV_WB + 4 * 8192, CV_ST = CV_X0 + 16384, CV_BN = CV_ST + 16384;
constexpr int G1_AB = 32768, G1_STAGE = G1_AB + 12288, G1_SCR = 3 * G1_STAGE, G1_NP = 11;
constexpr int G2_Z = 131072, G2_RED = G2_Z + 2048;

struct Cx {
  char* ws; char* smem; rsrc_t rw, rc; int lane, w, tid, bid; u32x2 tag;
  DI void mark(int) {}
  DI void endspin() {}
  DI void half_sync(int n) const {
    const unsigned c = (unsigned)(size_t)(LAS char*)(smem + LDS_CNT + (w >> 2) * 16);
    asm volatile("s_waitcnt lgkmcnt(0)" ::: "memory");
    if (lane == 0) asm volatile("ds_add_u32 %0, %1" ::"v"(c), "v"(1u) : "memory");
    unsigned x;
    do { asm volatile("ds_read_b32 %0, %1\n\ts_waitcnt lgkmcnt(0)" : "=v"(x) : "v"(c) : "memory"); } while (__builtin_amdgcn_readfirstlane(x) < 4u * n);
  }
  DI void signal(int idx) const { if (lane < FREP) put8(ws + OFF_FLG + (lane * 3072 + idx) * 32, tag); }
  template <int DELAY> DI void await(int idx, bool pend) const {
    const char* fp = ws + OFF_FLG + ((bid & (FREP - 1)) * 3072 + idx) * 32;
    if (DELAY) __builtin_amdgcn_s_sleep(DELAY);
#pragma unroll 1
    for (int spin = 0; spin < SPIN_MAX; ++spin) {
      if (pend) { const u32x2 f = poll8(fp); pend = f[0] != tag[0] || f[1] != tag[1]; }
      if (!__any(pend)) break;
      __builtin_amdgcn_s_sleep(PSLEEP);
    }
    asm volatile("" ::: "memory");
  }
  DI unsigned touch_code() const {
    unsigned x;
    asm volatile("buffer_load_dword %0, %1, %2, 0 offen" : "=v"(x) : "v"(((bid >> 3) * 16 + (lane & 15)) * 128), "s"(rc) : "memory");
    return x;
  }
  DI void fresh() {
    asm volatile("" : "+v"(tid), "+s"(w), "+s"(bid));
    lane = tid & 63;
  }
  DI void set_tag(u32x2 e) {
    tag = (u32x2){__builtin_amdgcn_readfirstlane(e[0]) * 1664525u + 1013904223u, __builtin_amdgcn_readfirstlane(e[1]) * 22695477u + 1u};
  }
};

DI void conv_store(const Cx& c) {
  const int j = (c.bid & 7) * 32 + (c.bid >> 3), t = c.tid + (c.w >> 2) * 256;
  const char* smem = c.smem;
  u32x4 yo[2];
#pragma unroll
  for (int i = 0; i < 2; ++i) {
    const int p = t + 512 * i;
    unsigned yy[4];
#pragma unroll
    for (int ch = 0; ch < 4; ++ch) yy[ch] = *(const unsigned*)(smem + CV_PAD + ch * CV_CH + CV_HA + p * 4);
    yo[i] = (u32x4){__builtin_amdgcn_perm(yy[1], yy[0], 0x05040100u), __builtin_amdgcn_perm(yy[3], yy[2], 0x05040100u),
                    __builtin_amdgcn_perm(yy[1], yy[0], 0x07060302u), __builtin_amdgcn_perm(yy[3], yy[2], 0x07060302u)};
    const u32x4 x0v = *(const u32x4*)(smem + CV_X0 + p * 16);
    const f32x4 stv = *(const f32x4*)(smem + CV_ST + p * 16);
    u32x4 xo;
#pragma unroll
    for (int e = 0; e < 4; ++e) {
      const float m = stv[e & 2], rs = stv[(e & 2) + 1], xa = bflo(x0v[e]), xb = bfhi(x0v[e]);
      xo[e] = pk2(xa + (xa - m) * rs * bflo(yo[i][e]), xb + (xb - m) * rs * bfhi(yo[i][e]));
    }
    st16(c.rw, OFF_X1 + (j * 1024 + p) * 16, xo);
  }
  if (!YLATE) {
    SB();
#pragma unroll
    for (int i = 0; i < 2; ++i) st16(c.rw, OFF_Y + (j * 1024 + t + 512 * i) * 16, yo[i]);
    wait_vm<2>();
  } else wait_vm<0>();
  WAIT_LGKM0();
  BAR();
  if (c.w == 0) c.signal(FL_X1 + c.bid);
  if (YLATE) {
    SB();
#pragma unroll
    for (int i = 0; i < 2; ++i) st16(c.rw, OFF_Y + (j * 1024 + t + 512 * i) * 16, yo[i]);
  }
}

DI void front_m(Cx& c, const uint16_t* k, const uint16_t* v, const uint16_t* shift_k, const uint16_t* ssm_k, const uint16_t* ssm_D) {
  const int lane = c.lane, w = c.w, tid = c.tid, bid = c.bid, s0 = bid * 8, j = (bid & 7) * 32 + (bid >> 3);
  char* smem = c.smem;
  u32x4* bounce = (u32x4*)(smem + CV_BN) + w * 256;
  u32x4 uu[4], hh[4];
  unsigned hn[4];
  float Dc;
  u32x2 e0;
  {
    u32x2 kk[11], vv[8];
#pragma unroll
    for (int r = 0; r < 11; ++r) kk[r] = ldin<u32x2>(k + (size_t)((r < 3) ? max(s0 - 3 + r, 0) : s0 - 3 + r) * D + tid * 4);
#pragma unroll
    for (int r = 0; r < 8; ++r) vv[r] = ldin<u32x2>(v + (size_t)(s0 + r) * D + tid * 4);
    const u32x4 wa = *(const u32x4*)(shift_k + tid * 16), wb = *(const u32x4*)(shift_k + tid * 16 + 8);
    SB();
    if (tid < 2) *(unsigned*)(smem + LDS_CNT + tid * 16) = 0u;
    WAIT_LGKM0();
    wait_vm<KVW>();
    BAR();
    STAMP(0, 1);
    SB();
    const uint16_t* hrow = ssm_k + (size_t)(4 * j + w) * S;
#pragma unroll
    for (int i = 0; i < 4; ++i) {
      const int ch = i * 64 + lane;
      hh[i] = ldin<u32x4>(hrow + ch * 8);
      hn[i] = ldin<unsigned>(hrow + min(ch * 8 + 8, S - 2));
    }
    Dc = bflo((unsigned)ssm_D[4 * j + w]);
    e0 = poll8(c.ws + OFF_EPO);
    SB();
    const unsigned hm = bid ? ~0u : 0u;
#pragma unroll
    for (int r = 0; r < 3; ++r) { kk[r][0] &= hm; kk[r][1] &= hm; }
    const int sw = (lane >> 1) & 3;
#pragma unroll
    for (int ch = 0; ch < 4; ++ch) {
      const unsigned w01 = (ch == 0) ? wa[0] : (ch == 1) ? wa[2] : (ch == 2) ? wb[0] : wb[2];
      const unsigned w23 = (ch == 0) ? wa[1] : (ch == 1) ? wa[3] : (ch == 2) ? wb[1] : wb[3];
      const float w0 = bflo(w01), w1 = bfhi(w01), w2 = bflo(w23), w3 = bfhi(w23);
      float kf[11], u[8];
#pragma unroll
      for (int r = 0; r < 11; ++r) kf[r] = (ch & 1) ? bfhi(kk[r][ch >> 1]) : bflo(kk[r][ch >> 1]);
#pragma unroll
      for (int r = 0; r < 8; ++r) {
        const float ks = kf[r] * w3 + kf[r + 1] * w2 + kf[r + 2] * w1 + kf[r + 3] * w0;
        u[r] = ks * ((ch & 1) ? bfhi(vv[r][ch >> 1]) : bflo(vv[r][ch >> 1]));
      }
      bounce[lane * 4 + (ch ^ sw)] = (u32x4){pk2(u[7], u[6]), pk2(u[5], u[4]), pk2(u[3], u[2]), pk2(u[1], u[0])};
    }
#pragma unroll
    for (int i = 0; i < 4; ++i) {
      const int u = 64 * i + lane;
      st16(c.rw, OFF_UB + (bid * 1024 + w * 256 + u) * 16, bounce[(u & ~3) | ((u & 3) ^ ((u >> 3) & 3))]);
    }
    STAMP(0, 2); c.mark(11);
    SB();
  }
  char* my = smem + CV_PAD + w * CV_CH;
  if (lane < 30) *(u32x4*)(my + lane * 16) = (u32x4){0u, 0u, 0u, 0u};
  if (lane >= 32 && lane < 34) *(u32x4*)(my + CV_HA + (lane - 32) * 16) = (u32x4){0u, 0u, 0u, 0u};
  if (lane == 34) *(u32x4*)(my + CV_HB) = (u32x4){0u, 0u, 0u, 0u};
  if (lane == 0) *(u32x4*)(my + CV_HB + 16) = (u32x4){0u, 0u, 0u, hh[0][0] << 16};
#pragma unroll
  for (int i = 0; i < 4; ++i) {
    const int ch = i * 64 + lane;
    *(u32x4*)(my + CV_HA + 32 + ch * 16) = hh[i];
    *(u32x4*)(my + CV_HB + 32 + ch * 16) =
        (u32x4){__builtin_amdgcn_alignbyte(hh[i][1], hh[i][0], 2), __builtin_amdgcn_alignbyte(hh[i][2], hh[i][1], 2),
                __builtin_amdgcn_alignbyte(hh[i][3], hh[i][2], 2), __builtin_amdgcn_alignbyte(hn[i], hh[i][3], 2)};
  }
  asm volatile("" : "+v"(Dc), "+v"(e0));
  c.set_tag(e0);
  wait_vm<0>();
  c.signal(FL_U + w * 256 + bid);
  STAMP(0, 3); c.mark(12);
  c.await<PDELAY>(FL_U + (j >> 6) * 256 + w * 64 + lane, true);
  STAMP(0, 4); c.mark(13);
#pragma unroll
  for (int i = 0; i < 4; ++i) uu[i] = ld16(c.rw, ((i * 16 + (lane >> 2)) * 1024 + (lane & 3)) * 16, OFF_UB + (w * 65536 + 4 * j) * 16);
#pragma unroll
  for (int i = 0; i < 4; ++i) *(u32x4*)(smem + CV_PAD + (lane & 3) * CV_CH + (30 + w * 64 + i * 16 + (lane >> 2)) * 16) = uu[i];
  const int t0 = lane & 15, kq = lane >> 4;
  const LAS unsigned* fbk[4];
#pragma unroll
  for (int i = 0; i < 4; ++i) {
    fbk[i] = (const LAS unsigned*)(my + ((t0 & 1) ? CV_HA : CV_HB)) + 4 * kq + ((1 + t0) >> 1) + 256 * i;
    asm volatile("" : "+v"(fbk[i]));
  }
  const char* gb = my + (2 * t0 + 3 - kq) * 16;
  f32x4 acc[8];
#pragma unroll
  for (int b = 0; b < 8; ++b) { acc[b] = (f32x4){0.f, 0.f, 0.f, 0.f}; asm volatile("" : "+v"(acc[b])); }
  c.half_sync(1);
  STAMP(1, 0); STAMP(1, 1); c.mark(14);
  bf16x8 F[2][8], G[2][8];
  u32x2 fh[8];
  auto rdn = [&](int buf, int g0, int n) {
    if (n == 0 || n > 16) { const int g1 = n ? n - 16 : 0; G[buf][g1] = *(const bf16x8*)(gb + (32 * g1 + 4 * (7 - g0)) * 16); }
    else {
      const int e1 = (n - 1) >> 1;
      const LAS unsigned* p = fbk[e1 >> 1] + 128 * (e1 & 1) + 16 * g0;
      if ((n - 1) & 1) F[buf][e1] = as_frag((u32x4){fh[e1][0], fh[e1][1], p[2], p[3]});
      else fh[e1] = (u32x2){p[0], p[1]};
    }
  };
  auto cstep = [&](int cb, int g0n) {
    int n = 0;
#pragma unroll
    for (int g1 = 0; g1 < 8; ++g1)
#pragma unroll
      for (int e1 = 0; e1 + g1 < 8; ++e1) {
        acc[g1 + e1] = MFMA16(F[cb][e1], G[cb][g1], acc[g1 + e1]);
        SB();
        if (n < 24) { rdn(cb ^ 1, g0n, n); SB(); }
        ++n;
      }
  };
#pragma unroll
  for (int n = 0; n < 24; ++n) rdn(0, 0, n);
  for (int gp = 0; gp < 4; ++gp) {
    cstep(0, 1);
    cstep(1, 2);
    gb -= 128;
#pragma unroll
    for (int i = 0; i < 4; ++i) fbk[i] += 32;
  }
  asm volatile("" ::"v"(acc[7]), "v"(acc[0]));
  STAMP(1, 2);
#pragma unroll
  for (int b = 0; b < 8; ++b) {
    const int s = 256 * b + 16 * t0 + 4 * kq;
    const u32x2 ur = *(const u32x2*)(my + (30 + (s >> 3)) * 16 + ((s & 4) ? 0 : 8));
    *(u32x2*)(my + CV_HA + s * 2) = (u32x2){pk2(acc[b][0] + bfhi(ur[1]) * Dc, acc[b][1] + bflo(ur[1]) * Dc),
                                            pk2(acc[b][2] + bfhi(ur[0]) * Dc, acc[b][3] + bflo(ur[0]) * Dc)};
  }
  WAIT_LGKM0();
  BAR();
  STAMP(1, 3); c.mark(15);
  conv_store(c);
  STAMP(1, 6); c.mark(16);
  const unsigned pf = c.touch_code();
  c.await<PDELAY>(FL_X1 + w * 64 + lane, true);
  KEEP(pf);
  STAMP(1, 7);
  BAR();
  if (bid == 0 && tid == 0) put8(c.ws + OFF_EPO, c.tag);
}

DI void front_h(Cx& c, const uint16_t* q, const uint16_t* wqkv, const uint16_t* wo) {
  const int lane = c.lane, w4 = c.w - 4, tid = c.tid, bid = c.bid, s0 = bid * 8, j = (bid & 7) * 32 + (bid >> 3);
  char* smem = c.smem;
  const rsrc_t rw = c.rw;
  u32x4* bounce = (u32x4*)(smem + CV_BN) + c.w * 256;
  float* red = (float*)smem;
  const int gw = bid * 4 + w4;
  char* buf = smem + CV_WB + w4 * 8192;
  const int g = lane >> 4, qr = (lane >> 2) & 3, p = lane & 3, rr = lane >> 3;
  const bool isw = gw < 768;
  const int cg = isw ? gw % 48 : (gw - 768) & 15, og = isw ? gw / 48 : (gw - 768) >> 4;
  auto w_loads = [&]() {
    if (isw) {
      const uint16_t* src = wqkv + (size_t)(og * 64 + rr) * 3072 + cg * 64 + (lane & 7) * 8;
#pragma unroll
      for (int i = 0; i < 8; ++i) glds16(src + (size_t)i * 8 * 3072, buf + i * 1024);
    } else {
      const uint16_t* src = wo + (size_t)(og * 64 + (rr & 3) + 16 * (rr >> 2)) * 1024 + cg * 64 + (lane & 7) * 8;
#pragma unroll
      for (int i = 0; i < 8; ++i) glds16(src + (size_t)((i >> 2) * 32 + (i & 3) * 4) * 1024, buf + i * 1024);
    }
  };
  u32x2 e0 = poll8(c.ws + OFF_EPO);
  const unsigned pf = c.touch_code();
  SB();
  BAR();
  u32x2 qq[8];
#pragma unroll
  for (int r = 0; r < 8; ++r) qq[r] = ldin<u32x2>(q + (size_t)(s0 + r) * D + tid * 4);
  SB();
  if (WMODE == 0) w_loads();
  SB();
  const int sw = (lane >> 1) & 3;
#pragma unroll
  for (int r = 0; r < 4; ++r) bounce[lane * 4 + (r ^ sw)] = (u32x4){qq[2 * r][0], qq[2 * r][1], qq[2 * r + 1][0], qq[2 * r + 1][1]};
#pragma unroll
  for (int i = 0; i < 4; ++i) {
    const int u = 64 * i + lane;
    st16(rw, OFF_X0 + (bid * 1024 + w4 * 256 + u) * 16, bounce[(u & ~3) | ((u & 3) ^ ((u >> 3) & 3))]);
  }
  if (tid < 80) st16(rw, OFF_GX + bid * 1280 + tid * 16, (u32x4){~0u, ~0u, ~0u, ~0u});
  SB();
  if (WMODE == 1) w_loads();
  SB();
  float a[16];
#pragma unroll
  for (int r = 0; r < 8; ++r) {
    a[2 * r] = dot2(qq[r][0], 0x3f803f80u, dot2(qq[r][1], 0x3f803f80u, 0.f));
    a[2 * r + 1] = dot2(qq[r][0], qq[r][0], dot2(qq[r][1], qq[r][1], 0.f));
  }
#pragma unroll
  for (int i = 0; i < 8; ++i) a[i] = swap32_sum(a[i], a[i + 8]);
#pragma unroll
  for (int i = 0; i < 4; ++i) a[i] = swap16_sum(a[i], a[i + 4]);
#pragma unroll
  for (int i = 0; i < 4; ++i) { a[i] += row_ror<1>(a[i]); a[i] += row_ror<2>(a[i]); a[i] += row_ror<4>(a[i]); a[i] += row_ror<8>(a[i]); }
  if ((lane & 15) == 0) *(f32x4*)&red[w4 * 16 + (lane >> 4) * 4] = (f32x4){a[0], a[1], a[2], a[3]};
  c.half_sync(1);
  if (tid < 8) {
    const float sm = (red[2 * tid] + red[16 + 2 * tid]) + (red[32 + 2 * tid] + red[48 + 2 * tid]);
    const float sq = (red[2 * tid + 1] + red[17 + 2 * tid]) + (red[33 + 2 * tid] + red[49 + 2 * tid]);
    st8(rw, OFF_ST0 + (s0 + tid) * 8, __builtin_bit_cast(u32x2, ln_stats(sm, sq)));
  }
  asm volatile("" : "+v"(e0));
  c.set_tag(e0);
  wait_vm<0>();
  KEEP(pf);
  c.signal(FL_Q + w4 * 256 + bid);
  STAMPW(0, 5, 4);
  if (WMODE == 2) w_loads();
  {
    const unsigned cm = (unsigned)(size_t)(LAS char*)(smem + LDS_CNT);
    unsigned x;
    do { __builtin_amdgcn_s_sleep(2); asm volatile("ds_read_b32 %0, %1\n\ts_waitcnt lgkmcnt(0)" : "=v"(x) : "v"(cm) : "memory"); } while (__builtin_amdgcn_readfirstlane(x) < 4u);
  }
  c.mark(1);
  if (WMODE == 3) w_loads();
  wait_vm<0>();
  const int hd = (cg & 15) * 2 + (lane >> 5), col = (cg >> 4) * 32 + (lane & 31);
  const unsigned dst = isw ? OFF_W8 + ((hd * 128 + og * 8) * 96 + col) * 16
                           : OFF_WOF + ((((og >> 1) * 32 + cg * 2 + (g >> 1)) * 8 + (og & 1) * 4) * 64 + (lane & 31)) * 16;
  const unsigned dstep = isw ? 1536 : 512;
  const unsigned la = (unsigned)(size_t)(LAS char*)buf + qr * 128 + (isw ? g * 32 + p * 8 : (g >> 1) * 64 + (p & 1) * 32 + ((g & 1) * 2 + (p >> 1)) * 8);
  u32x2 lo[8], hi[8];
#pragma unroll
  for (int i = 0; i < 8; ++i) {
    lo[i] = __builtin_bit_cast(u32x2, __builtin_amdgcn_ds_read_tr16_b64_v4i16((LAS s16x4*)(size_t)(la + i * 1024)));
    hi[i] = __builtin_bit_cast(u32x2, __builtin_amdgcn_ds_read_tr16_b64_v4i16((LAS s16x4*)(size_t)(la + i * 1024 + 512)));
  }
#pragma unroll
  for (int i = 0; i < 8; ++i) st16(rw, dst + i * dstep, (u32x4){lo[i][0], lo[i][1], hi[i][0], hi[i][1]});
  if (lane < 16 + 1 - (gw & 1)) st16(rw, OFF_KV + (gw * 33 / 2 + (gw & 1) + lane) * 16, (u32x4){0u, 0u, 0u, 0u});
  if (isw) {
    float c0 = 0.f, c1 = 0.f;
#pragma unroll
    for (int i = 0; i < 8; ++i)
#pragma unroll
      for (int e = 0; e < 2; ++e) {
        c0 = opq(opq(c0 + bflo(lo[i][e])) + bflo(hi[i][e]));
        c1 = opq(opq(c1 + bfhi(lo[i][e])) + bfhi(hi[i][e]));
      }
    __builtin_amdgcn_raw_buffer_store_b32(__float_as_uint(c0 + c1), rw, OFF_CSP + ((hd * 96 + col) * 16 + og) * 4, 0, 16);
  }
  STAMPW(1, 4, 4);
  c.await<0>(FL_Q + w4 * 64 + lane, true);
  c.await<0>(FL_Q + (j >> 6) * 256 + w4 * 64 + lane, j >= 64);
  SB();
#pragma unroll
  for (int i = 0; i < 4; ++i) {
    const int pp = w4 * 256 + i * 64 + lane;
    dma16(rw, smem + CV_X0 + (w4 * 4 + i) * 1024, ((pp >> 2) * 1024 + (pp & 3)) * 16, OFF_X0 + j * 64);
    dma16(rw, smem + CV_ST + (w4 * 4 + i) * 1024, pp * 16, OFF_ST0);
  }
  SB();
  wait_vm<0>();
  STAMPW(1, 5, 4);
  BAR();
  conv_store(c);
  BAR();
}

template <int L> DI void g1_m(const Cx& c) {
  constexpr bool C4 = L == 0;
  constexpr int kid = 2 + 2 * L;
  const int lane = c.lane, w = c.w, tid = c.tid, xcd = c.bid & 7, idx = c.bid >> 3;
  const int band = (xcd & 1) * 4 + (idx & 3), h = (xcd >> 1) * 8 + (idx >> 2), r = lane & 15, kq = lane >> 4;
  char* smem = c.smem;
  const float* Rs = (const float*)(smem + G1_SCR);
  const float* MRs = Rs + 256;
  const float* CSs = Rs + 512;
  float* P = (float*)(smem + G1_SCR) + 640;
  const char* ab = smem + kq * 4096 + ((C4 ? 32 : 64) * w + r) * 16;
  const char* bb = smem + G1_AB + kq * 1536 + r * 16;
  STAMP(kid, 0);
  f32x4 acc[4][6];
#pragma unroll
  for (int m = 0; m < 4; ++m)
#pragma unroll
    for (int p = 0; p < 6; ++p) { acc[m][p] = (f32x4){0.f, 0.f, 0.f, 0.f}; asm volatile("" : "+v"(acc[m][p])); }
  bf16x8 F[2][10];
  auto rd1 = [&](int buf, const char* a, const char* b, int t, int i) {
    if (i < 4) F[buf][i] = *(const bf16x8*)(a + t * 16384 + (C4 ? (i >> 1) * 256 + (i & 1) * 2048 : i * 256));
    else F[buf][i] = *(const bf16x8*)(b + t * 6144 + (i - 4) * 256);
  };
  auto fix = [&](int buf, int n) {
    if (C4) {
      u32x4 x = __builtin_bit_cast(u32x4, F[buf][2 * n]), y = __builtin_bit_cast(u32x4, F[buf][2 * n + 1]);
      asm("v_swap_b32 %0, %1" : "+v"(x[2]), "+v"(y[0]));
      asm("v_swap_b32 %0, %1" : "+v"(x[3]), "+v"(y[1]));
      F[buf][2 * n] = as_frag(x); F[buf][2 * n + 1] = as_frag(y);
      SB();
    }
  };
  auto step = [&](int cb, const char* a, const char* b, int t, bool rd) {
    const int ord[10] = {0, 1, 4, 5, 6, 7, 8, 9, 2, 3};
#pragma unroll
    for (int i = 0; i < 24; ++i) {
      const int m = i / 6, p = i % 6;
      if (p < 2) acc[m][p] = MFMA16(F[cb][4 + p], F[cb][m], acc[m][p]);
      else acc[m][p] = MFMA16(F[cb][m], F[cb][4 + p], acc[m][p]);
      SB();
      if (rd && (i & 1) && (i >> 1) < 10) { rd1(cb ^ 1, a, b, t, ord[i >> 1]); SB(); }
      if (i == 6) fix(cb, 1);
      if (rd && i == 20) fix(cb ^ 1, 0);
    }
  };
  BAR();
  STAMP(kid, 1);
#pragma unroll
  for (int i = 0; i < 10; ++i) rd1(0, ab, bb, 0, i);
  fix(0, 0);
  asm volatile("s_nop 1");
  SB();
  int so = 0;
  for (int it = 0; it < 15; ++it) {
    step(0, ab + so, bb + so, 1, true);
    WAIT_LGKM0();
    BAR();
    so = (so == 2 * G1_STAGE) ? 0 : so + G1_STAGE;
    step(1, ab + so, bb + so, 0, true);
  }
  step(0, ab + so, bb + so, 1, true);
  BAR();
  step(1, ab, bb, 0, false);
  STAMP(kid, 2);
  f32x4 Z[2][2], Zz[2];
#pragma unroll
  for (int i = 0; i < 2; ++i) { Z[i][0] = (f32x4){0.f, 0.f, 0.f, 0.f}; Z[i][1] = Z[i][0]; Zz[i] = Z[i][0]; }
  {
    float ck[4];
#pragma unroll
    for (int p = 0; p < 4; ++p) ck[p] = CSs[32 + p * 16 + r];
    const bf16x8 ones = as_frag((u32x4){0x3f803f80u, 0x3f803f80u, 0x3f803f80u, 0x3f803f80u});
#pragma unroll
    for (int jj = 0; jj < 2; ++jj) {
      u32x4 fr[4];
#pragma unroll
      for (int mm = 0; mm < 2; ++mm) {
        const int m = 2 * jj + mm;
        const f32x4 rs = *(const f32x4*)(Rs + 64 * w + 16 * m + 4 * kq), mr = *(const f32x4*)(MRs + 64 * w + 16 * m + 4 * kq);
#pragma unroll
        for (int p = 0; p < 2; ++p) {
          fr[p][2 * mm] = pk2v(phi2(lo2(acc[m][2 + p]) * lo2(rs) + (lo2(mr) * ck[p] + 1e-6f)));
          fr[p][2 * mm + 1] = pk2v(phi2(hi2(acc[m][2 + p]) * hi2(rs) + (hi2(mr) * ck[p] + 1e-6f)));
          fr[2 + p][2 * mm] = pk2v(lo2(acc[m][4 + p]) * lo2(rs) + lo2(mr) * ck[2 + p]);
          fr[2 + p][2 * mm + 1] = pk2v(hi2(acc[m][4 + p]) * hi2(rs) + hi2(mr) * ck[2 + p]);
        }
      }
#pragma unroll
      for (int dh = 0; dh < 2; ++dh) {
        Z[0][dh] = MFMA16(as_frag(fr[dh]), as_frag(fr[2]), Z[0][dh]);
        Z[1][dh] = MFMA16(as_frag(fr[dh]), as_frag(fr[3]), Z[1][dh]);
        Zz[dh] = MFMA16(as_frag(fr[dh]), ones, Zz[dh]);
      }
    }
  }
  {
    float* mine = P + w * 1056;
#pragma unroll
    for (int i = 0; i < 4; ++i) *(f32x4*)(mine + i * 256 + lane * 4) = Z[i >> 1][i & 1];
    if (r == 0) { *(f32x4*)(mine + 1024 + 4 * kq) = Zz[0]; *(f32x4*)(mine + 1040 + 4 * kq) = Zz[1]; }
  }
  c.half_sync(2 * L + 2);
  STAMP(kid, 3);
  float* kvacc = (float*)(c.ws + OFF_KV) + L * KV_LAYER;
#pragma unroll
  for (int i = 0; i < 4; ++i) {
    const int e = tid + 256 * i;
    atomicAdd(kvacc + h * 1024 + e, (P[e] + P[1056 + e]) + (P[2112 + e] + P[3168 + e]));
  }
  if (tid < 32) atomicAdd(kvacc + 32768 + h * 32 + tid, (P[1024 + tid] + P[2080 + tid]) + (P[3136 + tid] + P[4192 + tid]));
  SB();
  {
    const f32x4 c0 = *(const f32x4*)(CSs + 4 * kq), c1 = *(const f32x4*)(CSs + 16 + 4 * kq);
#pragma unroll
    for (int m = 0; m < 4; ++m) {
      const float rs = Rs[64 * w + 16 * m + r], mr = MRs[64 * w + 16 * m + r];
      const u32x4 o = {pk2v(phi2(lo2(acc[m][0]) * rs + (lo2(c0) * mr + 1e-6f))), pk2v(phi2(hi2(acc[m][0]) * rs + (hi2(c0) * mr + 1e-6f))),
                       pk2v(phi2(lo2(acc[m][1]) * rs + (lo2(c1) * mr + 1e-6f))), pk2v(phi2(hi2(acc[m][1]) * rs + (hi2(c1) * mr + 1e-6f)))};
      st16(c.rw, OFF_PQ + L * 4 * MB + ((((h >> 2) * 128 + band * 16 + w * 4 + m) * 4 + (h & 3)) * 64 + lane) * 16, o);
    }
  }
  STAMP(kid, 6);
  wait_vm<0>();
  c.half_sync(2 * L + 3);
  STAMP(kid, 7);
  if (w == 0) c.signal(FL_G1 + L * 256 + h * 8 + band);
}

template <int L> DI void g1_h(const Cx& c) {
  constexpr bool C4 = L == 0;
  constexpr unsigned OFF_X = C4 ? OFF_X1 : OFF_X3;
  const int lane = c.lane, lw = c.w - 4, t = c.tid, xcd = c.bid & 7, idx = c.bid >> 3;
  const int band = (xcd & 1) * 4 + (idx & 3), h = (xcd >> 1) * 8 + (idx >> 2);
  char* smem = c.smem;
  char* ws = c.ws;
  const rsrc_t rw = c.rw;
  float* Rs = (float*)(smem + G1_SCR);
  const unsigned l0 = lane * 16;
  auto issue = [&](int st) {
    char* base = smem + (st % 3) * G1_STAGE;
#pragma unroll
    for (int p = 0; p < 8; ++p) {
      if (C4) dma16(rw, base + (8 * lw + p) * 1024, l0, OFF_X + ((st * 16 + lw * 4 + (p >> 1)) * 1024 + band * 128 + (p & 1) * 64) * 16);
      else dma16(rw, base + (8 * lw + p) * 1024, l0, OFF_X + ((st * 8 + lw * 2 + (p >> 2)) * S + band * 256 + (p & 3) * 64) * 16);
    }
#pragma unroll
    for (int p = 0; p < 3; ++p) dma16(rw, base + G1_AB + (3 * lw + p) * 1024, l0, OFF_W8 + (h * 128 + st * 8) * 1536 + (3 * lw + p) * 1024);
  };
  issue(0); issue(1);
  wait_vm<G1_NP>();
  BAR();
  issue(2);
  u32x4 xv[4];
  float v[4] = {0.f, 0.f, 0.f, 0.f};
  if (C4) {
    const u32x4* xp = (const u32x4*)(ws + OFF_X) + (size_t)(t >> 2) * 1024 + band * 128 + h * 4 + (t & 3);
    asm volatile("global_load_dwordx4 %0, %4, off sc1\n\tglobal_load_dwordx4 %1, %5, off sc1\n\tglobal_load_dwordx4 %2, %6, off sc1\n\tglobal_load_dwordx4 %3, %7, off sc1"
                 : "=&v"(xv[0]), "=&v"(xv[1]), "=&v"(xv[2]), "=&v"(xv[3]) : "v"(xp), "v"(xp + 65536), "v"(xp + 131072), "v"(xp + 196608) : "memory");
  }
#pragma unroll
  for (int i = 0; i < 13; ++i) {
    if (C4 && i < 2) wait_vm<G1_NP + 4>();
    else if (C4 && i == 2) asm volatile("s_waitcnt vmcnt(%4)" : "+v"(xv[0]), "+v"(xv[1]), "+v"(xv[2]), "+v"(xv[3]) : "i"(G1_NP) : "memory");
    else wait_vm<G1_NP>();
    BAR();
    issue(i + 3);
    if (C4 && i >= 2 && i < 6) {
      const u32x4 x = xv[i - 2];
#pragma unroll
      for (int e = 0; e < 4; ++e) {
        const float lo = bflo(x[e]), hi = bfhi(x[e]);
        v[e & 2] = opq(v[e & 2] + lo); v[(e & 2) + 1] = opq(fmaf(lo, lo, v[(e & 2) + 1]));
        v[e & 2] = opq(v[e & 2] + hi); v[(e & 2) + 1] = opq(fmaf(hi, hi, v[(e & 2) + 1]));
      }
      asm volatile("" : "+v"(v[0]), "+v"(v[1]), "+v"(v[2]), "+v"(v[3]));
    }
    if (C4 && i == 6) {
#pragma unroll
      for (int e = 0; e < 4; ++e) { v[e] += row_ror<4>(v[e]); v[e] += row_ror<8>(v[e]); v[e] = swap16_sum(v[e], v[e]); v[e] = swap32_sum(v[e], v[e]); }
      const bool up = (lane & 4) != 0;
      if (lane < 8) publish8(ws + OFF_GX + ((band * 4 + lw) * 256 + h * 8 + 2 * (lane & 3) + (lane >> 2)) * 8, up ? v[2] : v[0], up ? v[3] : v[1]);
    }
  }
  const char* gp = ws + (C4 ? OFF_GX + (band * 1024 + t) * 8 + 4096 : OFF_ST3 + ((band * 4 + lw) * 512 + lane) * 8);
  const char* cp = ws + OFF_CSP + (h * 96 + (t < 96 ? t : 0)) * 64;
  u32x2 g[8];
  f32x4 c0, c1, c2, c3;
  asm volatile("global_load_dwordx4 %0, %4, off sc1\n\tglobal_load_dwordx4 %1, %4, off offset:16 sc1\n\t"
               "global_load_dwordx4 %2, %4, off offset:32 sc1\n\tglobal_load_dwordx4 %3, %4, off offset:48 sc1"
               : "=&v"(c0), "=&v"(c1), "=&v"(c2), "=&v"(c3) : "v"(cp) : "memory");
  if (C4) {
    asm volatile("global_load_dwordx2 %0, %4, off offset:-4096 sc1\n\tglobal_load_dwordx2 %1, %4, off offset:-2048 sc1\n\t"
                 "global_load_dwordx2 %2, %4, off sc1\n\tglobal_load_dwordx2 %3, %4, off offset:2048 sc1"
                 : "=&v"(g[0]), "=&v"(g[1]), "=&v"(g[2]), "=&v"(g[3]) : "v"(gp) : "memory");
    wait_vm<G1_NP + 8>();
  } else {
    asm volatile("global_load_dwordx2 %0, %8, off sc1\n\tglobal_load_dwordx2 %1, %8, off offset:512 sc1\n\tglobal_load_dwordx2 %2, %8, off offset:1024 sc1\n\t"
                 "global_load_dwordx2 %3, %8, off offset:1536 sc1\n\tglobal_load_dwordx2 %4, %8, off offset:2048 sc1\n\tglobal_load_dwordx2 %5, %8, off offset:2560 sc1\n\t"
                 "global_load_dwordx2 %6, %8, off offset:3072 sc1\n\tglobal_load_dwordx2 %7, %8, off offset:3584 sc1"
                 : "=&v"(g[0]), "=&v"(g[1]), "=&v"(g[2]), "=&v"(g[3]), "=&v"(g[4]), "=&v"(g[5]), "=&v"(g[6]), "=&v"(g[7]) : "v"(gp) : "memory");
    wait_vm<G1_NP + 12>();
  }
  BAR();
  if (C4) asm volatile("s_waitcnt vmcnt(0)" : "+v"(g[0]), "+v"(g[1]), "+v"(g[2]), "+v"(g[3]), "+v"(c0), "+v"(c1), "+v"(c2), "+v"(c3)::"memory");
  else asm volatile("s_waitcnt vmcnt(0)" : "+v"(g[0]), "+v"(g[1]), "+v"(g[2]), "+v"(g[3]), "+v"(g[4]), "+v"(g[5]), "+v"(g[6]), "+v"(g[7]),
                    "+v"(c0), "+v"(c1), "+v"(c2), "+v"(c3)::"memory");
  BAR();
#pragma unroll 1
  for (int spin = 0; spin < SPIN_MAX; ++spin) {
    bool empty = false;
#pragma unroll
    for (int k = 0; k < (C4 ? 4 : 8); ++k) empty |= g[k][1] == ~0u;
    if (!__any(empty)) break;
    __builtin_amdgcn_s_sleep(4);
#pragma unroll
    for (int k = 0; k < (C4 ? 4 : 8); ++k) g[k] = poll8(gp + (C4 ? (k - 2) * 2048 : k * 512));
  }
  float sm = 0.f, sq = 0.f, cs = 0.f;
#pragma unroll
  for (int k = 0; k < (C4 ? 4 : 8); ++k) { sm = opq(sm + __uint_as_float(g[k][0])); sq = opq(sq + __uint_as_float(g[k][1])); }
#pragma unroll
  for (int k = 0; k < 4; ++k) cs = opq(opq(opq(opq(cs + c0[k]) + c1[k]) + c2[k]) + c3[k]);
  const f32x2 st = ln_stats(sm, sq);
  const int ts = C4 ? (t & ~31) + (t & 1) * 16 + ((t & 31) >> 1) : t;
  Rs[ts] = st[1]; Rs[256 + ts] = -st[0] * st[1];
  if (t < 96) Rs[512 + t] = cs;
  WAIT_LGKM0();
  BAR();
}

template <int L> DI void g2_m(const Cx& c) {
  constexpr int kid = 3 + 2 * L;
  constexpr unsigned KVO = OFF_KV + L * KV_LAYER * 4;
  const int lane = c.lane, w = c.w, xcd = c.bid & 7, idx = c.bid >> 3, sb = (xcd >> 1) * 8 + (idx & 7), kq = lane >> 4;
  const rsrc_t rw = c.rw;
  const unsigned l16 = lane * 16;
  u32x4* O = (u32x4*)c.smem + (L == 0 ? ((lane >> 3) & 1) * 64 + kq * 16 + (lane & 7) * 2 : lane);
  unsigned pf = c.touch_code();
  c.await<PDELAY>(FL_G1 + L * 256 + ((lane >> 3) * 4 + w) * 8 + (lane & 7), true);
  KEEP(pf);
  STAMP(kid, 0);
  constexpr int PL = 2, PD = PL + 1;
  u32x4 pq[PD][4];
  f32x4 kv[PD][4];
  const unsigned spq = OFF_PQ + L * 4 * MB + (sb * 16 + w) * 1024, skv = KVO + w * 4096;
  auto ld = [&](int i, int n) {
    if (n < 4) kv[i % PD][n] = __builtin_bit_cast(f32x4, ld16(rw, l16 + n * 1024, skv + i * 16384));
    else pq[i % PD][n - 4] = ld16(rw, l16, spq + (i * 512 + (n - 4) * 4) * 1024);
    SB();
  };
  const f32x4 zall = __builtin_bit_cast(f32x4, ld16(rw, (lane >> 3) * 512 + (lane & 7) * 16, KVO + 131072 + w * 128));
  SB();
#pragma unroll
  for (int n = 0; n < 8 * PL; ++n) ld(n >> 3, n & 7);
  u32x2* Zs = (u32x2*)(c.smem + G2_Z) + w * 64;
  Zs[lane] = (u32x2){pk2v(lo2(zall)), pk2v(hi2(zall))};
  const f32x4 zero = {0.f, 0.f, 0.f, 0.f}, eps = {1e-6f, 1e-6f, 1e-6f, 1e-6f};
  bf16x8 k0, k1, zb;
  auto prep = [&](int i) {
    const int b = i % PD;
    const u32x2 za = Zs[i * 8 + kq], zc = Zs[i * 8 + 4 + kq];
    k0 = as_frag((u32x4){pk2v(lo2(kv[b][0])), pk2v(hi2(kv[b][0])), pk2v(lo2(kv[b][1])), pk2v(hi2(kv[b][1]))});
    k1 = as_frag((u32x4){pk2v(lo2(kv[b][2])), pk2v(hi2(kv[b][2])), pk2v(lo2(kv[b][3])), pk2v(hi2(kv[b][3]))});
    zb = as_frag((u32x4){za[0], za[1], zc[0], zc[1]});
    asm volatile("" : "+v"(k0), "+v"(k1), "+v"(zb));
  };
  prep(0);
#pragma unroll
  for (int i = 0; i < 8; ++i) {
    const int b = i % PD;
    f32x4 x0[4], x1[4], dn[4];
    __builtin_amdgcn_s_setprio(2);
#pragma unroll
    for (int st = 0; st < 4; ++st) {
      dn[st] = MFMA16(zb, as_frag(pq[b][st]), eps);
      x0[st] = MFMA16(k0, as_frag(pq[b][st]), zero);
      x1[st] = MFMA16(k1, as_frag(pq[b][st]), zero);
    }
    __builtin_amdgcn_s_setprio(0);
    SB();
#pragma unroll
    for (int st = 0; st < 4; ++st) {
      const float inv = __builtin_amdgcn_rcpf(dn[st][0]);
      O[L == 0 ? ((i * 4 + w) * 4 + (st & 2)) * 64 + (st & 1) : ((i * 4 + w) * 4 + st) * 64] =
          (u32x4){pk2(opq(x0[st][0] * inv), opq(x0[st][1] * inv)), pk2(opq(x0[st][2] * inv), opq(x0[st][3] * inv)),
                  pk2(opq(x1[st][0] * inv), opq(x1[st][1] * inv)), pk2(opq(x1[st][2] * inv), opq(x1[st][3] * inv))};
      SB();
      if (i + PL < 8) { ld(i + PL, 2 * st); ld(i + PL, 2 * st + 1); }
    }
    if (i < 7) prep(i + 1);
    WAIT_LGKM0();
    BAR();
    if (i == 0) STAMP(kid, 1);
  }
  if (L == 0) {
    pf = c.touch_code();
    wait_vm<0>();
    KEEP(pf);
    BAR();
  }
}

template <int L> DI void g2_h(const Cx& cx, char* out) {
  constexpr int kid = 3 + 2 * L;
  const int lane = cx.lane, c = cx.w - 4, xcd = cx.bid & 7, idx = cx.bid >> 3;
  const int sb = (xcd >> 1) * 8 + (idx & 7), nb = (xcd & 1) * 4 + (idx >> 3);
  const int n0 = nb * 128 + c * 32, s0 = sb * 64, r = lane & 31, hf = lane >> 5;
  const rsrc_t rw = cx.rw;
  const unsigned l16 = lane * 16;
  constexpr int CD = 5, LA = 6;
  u32x4 wo[CD][8], of[8];
  const unsigned swo = OFF_WOF + (nb * 4 + c) * 8192;
  auto ldw = [&](int i, int n) { wo[i % CD][n] = ld16(rw, l16 + (n & 3) * 1024, swo + i * 262144 + (n >> 2) * 4096); SB(); };
#pragma unroll
  for (int n = 0; n < 8 * CD; ++n) ldw(n >> 3, n & 7);
  f32x16 acc[2];
#pragma unroll
  for (int i = 0; i < 16; ++i) { acc[0][i] = 0.f; acc[1][i] = 0.f; }
  asm volatile("" : "+v"(acc[0]), "+v"(acc[1]));
  u32x4 res[2][2], yv[2][2];
  auto ldres = [&](int k) {
    const int s2 = (k >> 1) & 1, g = k & 1;
    if (L == 1) { if (k < 4) res[s2][g] = ld16(rw, (2 * hf * S + r) * 16 + s2 * 512, OFF_X3 + (((n0 >> 3) + g) * S + s0) * 16); }
    else {
      const unsigned so = (k < 4 ? OFF_X1 : OFF_Y) + (((n0 >> 2) + 2 * g) * S + s0) * 8, vo = (4 * hf * S + r) * 8 + s2 * 256;
      const u32x2 lo = ld8(rw, vo, so), hi = ld8(rw, vo, so + S * 8);
      if (k < 4) res[s2][g] = (u32x4){lo[0], lo[1], hi[0], hi[1]}; else yv[s2][g] = (u32x4){lo[0], lo[1], hi[0], hi[1]};
    }
    SB();
  };
  const u32x4* Ob = (const u32x4*)cx.smem + (r >> 4) * 64 + hf * 16 + (r & 15);
  auto rdo = [&](int f) { const int u = f & 15; of[f & 7] = Ob[((f >> 4) * 16 + (u >> 2) * 4 + (u & 1) * 2) * 64 + ((u >> 1) & 1) * 32]; SB(); };
  __builtin_amdgcn_s_setprio(1);
  BAR();
#pragma unroll
  for (int f = 0; f < LA; ++f) rdo(f);
#pragma unroll
  for (int i = 0; i < 8; ++i)
#pragma unroll
    for (int n = 0; n < 16; ++n) {
      const int f = i * 16 + n;
      acc[n & 1] = MFMA32(as_frag(wo[i % CD][n >> 1]), as_frag(of[f & 7]), acc[n & 1]);
      SB();
      if (f + LA < 128) { if (((f + LA) & 15) == 0) BAR(); rdo(f + LA); }
      if ((n & 3) == 3 && i < 6) ldw(CD + (i >> 1), (i & 1) * 4 + (n >> 2));
      if (i == 4 && (n & 3) == 1) ldres(n >> 2);
      if (i == 5 && (n & 3) == 1) ldres(4 + (n >> 2));
    }
  __builtin_amdgcn_s_setprio(0);
  STAMPW(kid, 2, 4);
  u32x4 x[2][2];
#pragma unroll
  for (int s2 = 0; s2 < 2; ++s2)
#pragma unroll
    for (int g = 0; g < 2; ++g)
#pragma unroll
      for (int e = 0; e < 4; ++e) x[s2][g][e] = pk2(bflo(res[s2][g][e]) + acc[s2][8 * g + 2 * e], bfhi(res[s2][g][e]) + acc[s2][8 * g + 2 * e + 1]);
  if (L == 1) {
    if (OMODE == 0) {
#pragma unroll
      for (int s2 = 0; s2 < 2; ++s2)
#pragma unroll
        for (int g = 0; g < 2; ++g) *(u32x4*)(out + ((size_t)(s0 + 32 * s2 + r) * D + n0 + 16 * hf + 8 * g) * 2) = x[s2][g];
    } else {
      u32x4* T = (u32x4*)(cx.smem + G2_RED + 4096);
#pragma unroll
      for (int s2 = 0; s2 < 2; ++s2)
#pragma unroll
        for (int g = 0; g < 2; ++g) T[(32 * s2 + r) * 16 + ((c * 4 + hf * 2 + g) ^ (r & 15))] = x[s2][g];
      cx.half_sync(5);
      const rsrc_t ro = mk_rsrc(out, S * D * 2);
#pragma unroll
      for (int i = 0; i < 4; ++i) {
        const int row = c * 16 + i * 4 + (lane >> 4);
        __builtin_amdgcn_raw_buffer_store_b128(T[row * 16 + ((lane & 15) ^ (row & 15))], ro, ((s0 + row) * D + nb * 128) * 2 + (lane & 15) * 16, 0, OMODE == 1 ? 0 : OMODE == 2 ? 16 : 2);
      }
    }
    STAMPW(kid, 6, 4); DRAIN(); STAMPW(kid, 7, 4);
    return;
  }
  f32x2* Red = (f32x2*)(cx.smem + G2_RED);
  auto rowpart = [&]() {
    float v[4] = {0.f, 0.f, 0.f, 0.f};
#pragma unroll
    for (int e = 0; e < 8; ++e)
#pragma unroll
      for (int s2 = 0; s2 < 2; ++s2) { const unsigned u = x[s2][e >> 2][e & 3]; v[2 * s2] = dot2(u, 0x3f803f80u, v[2 * s2]); v[2 * s2 + 1] = dot2(u, u, v[2 * s2 + 1]); }
    return (f32x2){swap32_sum(v[0], v[2]), swap32_sum(v[1], v[3])};
  };
  Red[c * 64 + lane] = rowpart();
  cx.half_sync(2);
  if (c == 0) {
    char* gx = cx.ws + OFF_GX2 + (sb * 512 + lane) * 8;
    const f32x2 t = (Red[lane] + Red[64 + lane]) + (Red[128 + lane] + Red[192 + lane]);
    publish8(gx + nb * 512, t[0], t[1]);
    STAMPW(kid, 4, 4);
  }
  f32x2 xa[16], xy[16], yf[16];
#pragma unroll
  for (int e = 0; e < 16; ++e) {
    const unsigned xu = x[e >> 3][(e >> 2) & 1][e & 3], yu = yv[e >> 3][(e >> 2) & 1][e & 3];
    xa[e] = (f32x2){bflo(xu), bfhi(xu)}; yf[e] = (f32x2){bflo(yu), bfhi(yu)}; xy[e] = xa[e] * yf[e];
    asm volatile("" : "+v"(xa[e]), "+v"(yf[e]), "+v"(xy[e]));
  }
  if (c == 0) {
    const char* gx = cx.ws + OFF_GX2 + (sb * 512 + lane) * 8;
    __builtin_amdgcn_s_sleep(XDELAY);
    float sm, sq;
#pragma unroll 1
    for (int spin = 0; spin < SPIN_MAX; ++spin) {
      u32x2 g[8];
#pragma unroll
      for (int k = 0; k < 8; ++k) g[k] = poll8(gx + k * 512);
      bool empty = false;
      sm = 0.f; sq = 0.f;
#pragma unroll
      for (int k = 0; k < 8; ++k) { empty |= g[k][1] == ~0u; sm += __uint_as_float(g[k][0]); sq += __uint_as_float(g[k][1]); }
      if (!__any(empty)) break;
      __builtin_amdgcn_s_sleep(4);
    }
    const f32x2 t = ln_stats(sm, sq);
    Red[lane] = (f32x2){-t[0] * t[1], t[1]};
  }
  cx.half_sync(3);
  STAMPW(kid, 3, 4);
#pragma unroll
  for (int s2 = 0; s2 < 2; ++s2) {
    const f32x2 st = Red[32 * s2 + r];
#pragma unroll
    for (int g = 0; g < 2; ++g) {
#pragma unroll
      for (int e = 0; e < 4; ++e) { const int n = s2 * 8 + g * 4 + e; x[s2][g][e] = pk2v(xy[n] * st[1] + (yf[n] * st[0] + xa[n])); }
      st16(rw, OFF_X3 + (((n0 >> 3) + 2 * hf + g) * S + s0 + 32 * s2 + r) * 16, x[s2][g]);
    }
  }
  STAMPW(kid, 6, 4);
  Red[256 + c * 64 + lane] = rowpart();
  wait_vm<0>();
  cx.half_sync(4);
  if (c == 1) {
    const f32x2 t = (Red[256 + lane] + Red[320 + lane]) + (Red[384 + lane] + Red[448 + lane]);
    publish8(cx.ws + OFF_ST3 + ((sb * 8 + nb) * 64 + lane) * 8, t[0], t[1]);
  }
  STAMPW(kid, 7, 4);
  if (c == 0) {
    cx.signal(FL_X3 + sb * 8 + nb);
    cx.await<PDELAY>(FL_X3 + ((xcd & 1) * 4 + (idx & 3)) * 32 + (lane & 31), lane < 32);
  }
  STAMPW(kid, 5, 4);
  BAR();
}

__global__ __launch_bounds__(512, 2) void fused_kernel(const uint16_t* __restrict__ q, const uint16_t* __restrict__ k,
                                                       const uint16_t* __restrict__ v, const uint16_t* __restrict__ shift_k,
                                                       const uint16_t* __restrict__ ssm_k, const uint16_t* __restrict__ ssm_D,
                                                       const uint16_t* __restrict__ wqkv, const uint16_t* __restrict__ wo,
                                                       char* __restrict__ ws, char* __restrict__ out) {
  extern __shared__ __attribute__((aligned(16))) char smem[];
  unsigned long long pc;
  asm volatile("s_getpc_b64 %0" : "=s"(pc));
  STAMP(0, 0);
  Cx c;
  c.ws = ws; c.smem = smem; c.rw = mk_rsrc(ws, WS_USED); c.rc = mk_rsrc((void*)(pc & ~255ull), CODE_BYTES);
  c.lane = threadIdx.x & 63; c.w = __builtin_amdgcn_readfirstlane(threadIdx.x >> 6); c.tid = threadIdx.x & 255; c.bid = blockIdx.x;
  if (c.w < 4) {
    c.mark(0);
    front_m(c, k, v, shift_k, ssm_k, ssm_D); c.mark(17);
    c.fresh(); g1_m<0>(c); c.fresh(); g2_m<0>(c);
    c.fresh(); g1_m<1>(c); c.fresh(); g2_m<1>(c);
    c.endspin();
  } else {
    c.mark(0);
    front_h(c, q, wqkv, wo); c.mark(2);
    c.fresh(); g1_h<0>(c); c.mark(3); c.fresh(); g2_h<0>(c, out); c.mark(4);
    c.fresh(); g1_h<1>(c); c.mark(5); c.fresh(); g2_h<1>(c, out); c.mark(6);
    c.endspin();
  }
}

extern "C" void kernel_launch(void* const* d_in, const int* in_sizes, int n_in, void* d_out, int out_size, void* d_ws,
                              size_t ws_size, hipStream_t stream) {
  (void)hipFuncSetAttribute((const void*)fused_kernel, hipFuncAttributeMaxDynamicSharedMemorySize, LDS_BYTES);
  fused_kernel<<<256, 512, LDS_BYTES, stream>>>((const uint16_t*)d_in[0], (const uint16_t*)d_in[1], (const uint16_t*)d_in[2],
                                                (const uint16_t*)d_in[3], (const uint16_t*)d_in[4], (const uint16_t*)d_in[5],
                                                (const uint16_t*)d_in[7], (const uint16_t*)d_in[8], (char*)d_ws, (char*)d_out);
}
```

```cpp
#include <hip/hip_runtime.h>
#include <stdint.h>

typedef __attribute__((ext_vector_type(8))) short bf16x8;
typedef __attribute__((ext_vector_type(4))) short s16x4;
typedef __attribute__((ext_vector_type(4))) float f32x4;
typedef __attribute__((ext_vector_type(2))) float f32x2;
typedef __attribute__((ext_vector_type(16))) float f32x16;
typedef __attribute__((ext_vector_type(4))) unsigned u32x4;
typedef __attribute__((ext_vector_type(2))) unsigned u32x2;
typedef __attribute__((ext_vector_type(2))) __bf16 bf16x2_t;
typedef __amdgpu_buffer_rsrc_t rsrc_t;
#define DI __device__ __forceinline__
#define LAS __attribute__((address_space(3)))

constexpr int S = 2048, D = 1024;
constexpr unsigned MB = 1024 * 1024;
constexpr unsigned OFF_UB = 0,
                   OFF_X0 = 4 * MB,
                   OFF_W8 = 8 * MB,
                   OFF_WOF = 14 * MB,
                   OFF_Y = 16 * MB,
                   OFF_X1 = 20 * MB,
                   OFF_O = 24 * MB,
                   OFF_X3 = 32 * MB,
                   OFF_SM = 36 * MB, OFF_SL = 39 * MB,
                   WS_USED = 42 * MB;
constexpr int SLAB = 4352;
constexpr unsigned OFF_ST0 = OFF_SM,
                   OFF_CSP = OFF_ST0 + 16384,
                   OFF_GX = OFF_CSP + 16 * 3072 * 4,
                   OFF_GX2 = OFF_GX + 65536,
                   OFF_ST3 = OFF_GX2 + 131072,
                   OFF_EPO = OFF_ST3 + 131072,
                   OFF_FLG = 37 * MB;
constexpr int FL_U = 0, FL_Q = 1024, FL_X1 = 2048, FL_Y = 2304, FL_W = 2560, FL_X3 = 3328, FL_O = 4352  , FL_N = 6400;
constexpr unsigned CODE_BYTES = 50 * 1024;
constexpr int SPIN_MAX = 1 << 17;
constexpr int FREP = 8, G2NF = 44, G2LA = 5, PDELAY = 20, XDELAY = 12, PSLEEP = 8, YST = 6, DPF = 6, GAT = 2, KVW = 8, DU = PDELAY, DX1 = PDELAY, DG1 = 12, DX3 = PDELAY;

DI float bflo(unsigned u) { return __uint_as_float(u << 16); }
DI float bfhi(unsigned u) { return __uint_as_float(u & 0xffff0000u); }
DI unsigned pk2v(f32x2 v) { return __builtin_bit_cast(unsigned, __builtin_convertvector(v, bf16x2_t)); }
DI unsigned pk2(float a, float b) { return pk2v((f32x2){a, b}); }
DI float rbf(float a) { return bflo(pk2(a, a)); }
DI bf16x8 as_frag(u32x4 v) { return __builtin_bit_cast(bf16x8, v); }
DI f32x2 lo2(f32x4 v) { return (f32x2){v[0], v[1]}; }
DI f32x2 hi2(f32x4 v) { return (f32x2){v[2], v[3]}; }
DI float dot2(unsigned a, unsigned b, float c) {
  return __builtin_amdgcn_fdot2_f32_bf16(__builtin_bit_cast(bf16x2_t, a), __builtin_bit_cast(bf16x2_t, b), c, false);
}
DI f32x2 rnd2(f32x2 x) { const unsigned p = pk2v(x); return (f32x2){bflo(p), bfhi(p)}; }
#define MFMA16(a, b, c) __builtin_amdgcn_mfma_f32_16x16x32_bf16((a), (b), (c), 0, 0, 0)
#define MFMA32(a, b, c) __builtin_amdgcn_mfma_f32_32x32x16_bf16((a), (b), (c), 0, 0, 0)
#define WAIT_LGKM0() asm volatile("s_waitcnt lgkmcnt(0)" ::: "memory")
#define BAR() do { __builtin_amdgcn_s_barrier(); asm volatile("" ::: "memory"); } while (0)
#define SB() __builtin_amdgcn_sched_barrier(0)
#define KEEP(x) asm volatile("" ::"v"(x))
template <int N> DI void wait_vm() { asm volatile("s_waitcnt vmcnt(%0)" ::"i"(N) : "memory"); }
DI rsrc_t mk_rsrc(void* p, unsigned bytes) { return __builtin_amdgcn_make_buffer_rsrc(p, 0, bytes, 0x00020000); }
DI void glds16(const void* g, char* l) {
  __builtin_amdgcn_global_load_lds((const __attribute__((address_space(1))) void*)g, (LAS void*)l, 16, 0, 0);
}
template <class T> DI T ldin(const void* p) { return *(const T*)p; }
DI void dma16(rsrc_t r, char* l, unsigned voff, unsigned soff) { __builtin_amdgcn_raw_ptr_buffer_load_lds(r, (LAS void*)l, 16, voff, soff, 0, 16); }
DI u32x4 ld16(rsrc_t r, unsigned voff, unsigned soff) { return __builtin_amdgcn_raw_buffer_load_b128(r, voff, soff, 16); }
DI u32x2 ld8(rsrc_t r, unsigned voff, unsigned soff) { return __builtin_amdgcn_raw_buffer_load_b64(r, voff, soff, 16); }
DI void st16(rsrc_t r, unsigned off, u32x4 v) { __builtin_amdgcn_raw_buffer_store_b128(v, r, off, 0, 16); }
DI void st8(rsrc_t r, unsigned off, u32x2 v) { __builtin_amdgcn_raw_buffer_store_b64(v, r, off, 0, 16); }
DI void put8(char* p, u32x2 v) { __hip_atomic_store((unsigned long long*)p, __builtin_bit_cast(unsigned long long, v), __ATOMIC_RELAXED, __HIP_MEMORY_SCOPE_AGENT); }
DI u32x2 poll8(const char* p) { return __builtin_bit_cast(u32x2, __hip_atomic_load((const unsigned long long*)p, __ATOMIC_RELAXED, __HIP_MEMORY_SCOPE_AGENT)); }
DI void publish8(char* p, float sm, float sq) {
  unsigned qb = __float_as_uint(sq);
  if (qb == ~0u) qb = 0x7fc00000u;
  put8(p, (u32x2){__float_as_uint(sm), qb});
}
DI float opq(float m) { asm("" : "+v"(m)); return m; }
template <int N> DI float row_ror(float x) {
  return __int_as_float(__builtin_amdgcn_update_dpp(0, __float_as_int(x), 0x120 + N, 0xf, 0xf, false));
}
DI float swap16_sum(float a, float b) { const auto p = __builtin_amdgcn_permlane16_swap(__float_as_uint(a), __float_as_uint(b), false, false); return __uint_as_float(p[0]) + __uint_as_float(p[1]); }
DI float swap32_sum(float a, float b) { const auto p = __builtin_amdgcn_permlane32_swap(__float_as_uint(a), __float_as_uint(b), false, false); return __uint_as_float(p[0]) + __uint_as_float(p[1]); }

DI f32x2 ln_stats(float s, float q) {
  const float m = s * (1.0f / 1024.0f);
  const float v = fmaxf(q * (1.0f / 1024.0f) - m * m, 0.0f);
  const float sd = rbf(sqrtf(rbf(rbf(v) + 1e-5f)));
  return (f32x2){rbf(m), 1.0f / sd};
}
constexpr float LOG2E = 1.44269504088896f;
constexpr float LN2 = 0.693147180559945f, EPS2 = 1e-6f * LOG2E;
DI f32x2 phi2(f32x2 a) {
  f32x2 e, m;
  e[0] = __builtin_amdgcn_fmed3f(__builtin_amdgcn_exp2f(a[0]), 0.0f, 1.0f);
  e[1] = __builtin_amdgcn_fmed3f(__builtin_amdgcn_exp2f(a[1]), 0.0f, 1.0f);
  m[0] = fmaxf(a[0], EPS2);
  m[1] = fmaxf(a[1], EPS2);
  return m * LN2 + e;
}

constexpr int LDS_BYTES = 163840, LDS_CNT = LDS_BYTES - 64;
constexpr int CV_PAD = 512, CV_HA = 286 * 16, CV_HB = CV_HA + 4160, CV_CH = CV_HB + 4128 + 32;
constexpr int WR = 89088, WR_SLOT = 24576;
constexpr int CV_X0 = CV_PAD + 4 * CV_CH, CV_ST = CV_X0 + 16384, CV_WB = WR, CV_BN = WR + 32768;
constexpr int G2_NS = 5, G2_RED = G2_NS * 16384;

struct Cx {
  char* ws; char* smem; rsrc_t rw, rc; int lane, w, tid, bid; u32x2 tag;
  DI void half_sync(int n) const {
    const unsigned c = (unsigned)(size_t)(LAS char*)(smem + LDS_CNT + (w >> 2) * 16);
    asm volatile("s_waitcnt lgkmcnt(0)" ::: "memory");
    if (lane == 0) asm volatile("ds_add_u32 %0, %1" ::"v"(c), "v"(1u) : "memory");
    unsigned x;
    do { asm volatile("ds_read_b32 %0, %1\n\ts_waitcnt lgkmcnt(0)" : "=v"(x) : "v"(c) : "memory"); } while (__builtin_amdgcn_readfirstlane(x) < 4u * n);
  }
  DI void signal(int idx) const { if (lane < FREP) put8(ws + OFF_FLG + (lane * FL_N + idx) * 32, tag); }
  template <int DELAY> DI void await(int idx, bool pend) const {
    const char* fp = ws + OFF_FLG + ((bid & (FREP - 1)) * FL_N + idx) * 32;
    if (DELAY) __builtin_amdgcn_s_sleep(DELAY);
#pragma unroll 1
    for (int spin = 0; spin < SPIN_MAX; ++spin) {
      if (pend) { const u32x2 f = poll8(fp); pend = f[0] != tag[0] || f[1] != tag[1]; }
      if (!__any(pend)) break;
      __builtin_amdgcn_s_sleep(PSLEEP);
    }
    asm volatile("" ::: "memory");
  }
  DI void await2(int i0, int i1) const {
    const char* fp = ws + OFF_FLG + (bid & (FREP - 1)) * FL_N * 32;
    bool pend = true;
#pragma unroll 1
    for (int spin = 0; spin < SPIN_MAX; ++spin) {
      if (pend) {
        const u32x2 a = poll8(fp + i0 * 32), b = poll8(fp + i1 * 32);
        pend = a[0] != tag[0] || a[1] != tag[1] || b[0] != tag[0] || b[1] != tag[1];
      }
      if (!__any(pend)) break;
      __builtin_amdgcn_s_sleep(PSLEEP);
    }
    asm volatile("" ::: "memory");
  }
  DI void watch(int half, unsigned n) const {
    const unsigned c = (unsigned)(size_t)(LAS char*)(smem + LDS_CNT + half * 16);
    unsigned x;
    do { __builtin_amdgcn_s_sleep(2); asm volatile("ds_read_b32 %0, %1\n\ts_waitcnt lgkmcnt(0)" : "=v"(x) : "v"(c) : "memory"); } while (__builtin_amdgcn_readfirstlane(x) < 4u * n);
  }
  template <int PACE = 0> DI void w_pair(int n) const {
    const int h = ((bid & 7) >> 1) * 8 + (bid >> 5), q = w & 3;
#pragma unroll
    for (int p = 0; p < 6; ++p) {
      dma16(rw, smem + WR + (n % 3) * WR_SLOT + (6 * q + p) * 1024, lane * 16, OFF_W8 + (h * 128 + n * 16) * 1536 + (6 * q + p) * 1024);
      if (PACE) __builtin_amdgcn_s_sleep(PACE);
    }
  }
  DI void clear_slab(int layer) const {
    const int idx = bid >> 3, band = (bid & 1) * 4 + (idx & 3), h = ((bid & 7) >> 1) * 8 + (idx >> 2);
    const unsigned o = OFF_SL + (layer * 256 + h * 8 + band) * SLAB + tid * 16;
    st16(rw, o, (u32x4){~0u, ~0u, ~0u, ~0u});
    if (tid < 8) st16(rw, o + 4096, (u32x4){~0u, ~0u, ~0u, ~0u});
  }
  DI unsigned touch_code() const {
    unsigned x;
    asm volatile("buffer_load_dword %0, %1, %2, 0 offen" : "=v"(x) : "v"(((bid >> 3) * 16 + (lane & 15)) * 128), "s"(rc) : "memory");
    return x;
  }
  DI void fresh() {
    asm volatile("" : "+v"(tid), "+s"(w), "+s"(bid));
    lane = tid & 63;
  }
  DI void set_tag(u32x2 e) {
    tag = (u32x2){__builtin_amdgcn_readfirstlane(e[0]) * 1664525u + 1013904223u, __builtin_amdgcn_readfirstlane(e[1]) * 22695477u + 1u};
  }
};

DI u32x4 y_pair(const char* smem, int p) {
  unsigned yy[4];
#pragma unroll
  for (int ch = 0; ch < 4; ++ch) yy[ch] = *(const unsigned*)(smem + CV_PAD + ch * CV_CH + CV_HA + p * 4);
  return (u32x4){__builtin_amdgcn_perm(yy[1], yy[0], 0x05040100u), __builtin_amdgcn_perm(yy[3], yy[2], 0x05040100u),
                 __builtin_amdgcn_perm(yy[1], yy[0], 0x07060302u), __builtin_amdgcn_perm(yy[3], yy[2], 0x07060302u)};
}
DI void conv_store(const Cx& c) {
  const int j = (c.bid & 7) * 32 + (c.bid >> 3), t = c.tid + (c.w >> 2) * 256;
  const char* smem = c.smem;
#pragma unroll
  for (int i = 0; i < 2; ++i) {
    const int p = t + 512 * i;
    const u32x4 yo = y_pair(smem, p), x0v = *(const u32x4*)(smem + CV_X0 + p * 16);
    const f32x4 stv = *(const f32x4*)(smem + CV_ST + p * 16);
    u32x4 xo;
#pragma unroll
    for (int e = 0; e < 4; ++e) {
      const float m = stv[e & 2], rs = stv[(e & 2) + 1], xa = bflo(x0v[e]), xb = bfhi(x0v[e]);
      xo[e] = pk2(xa + (xa - m) * rs * bflo(yo[e]), xb + (xb - m) * rs * bfhi(yo[e]));
    }
    st16(c.rw, OFF_X1 + (j * 1024 + p) * 16, xo);
  }
  if (c.w >= 4) {
    const int h = ((c.bid & 7) >> 1) * 8 + (c.bid >> 5);
    c.await<0>(FL_W + (c.lane / 3) * 48 + (c.lane % 3) * 16 + (h >> 1), c.lane < 18);
  }
  wait_vm<0>();
  BAR();
  if (c.w == 0) c.signal(FL_X1 + c.bid);
}

DI void front_m(Cx& c, unsigned pf0, const uint16_t* k, const uint16_t* v, const uint16_t* shift_k, const uint16_t* ssm_k, const uint16_t* ssm_D) {
  const int lane = c.lane, w = c.w, tid = c.tid, bid = c.bid, s0 = bid * 8, j = (bid & 7) * 32 + (bid >> 3);
  char* smem = c.smem;
  u32x4* bounce = (u32x4*)(smem + CV_BN) + w * 256;
  u32x4 uu[4], hh[4];
  unsigned hn[4];
  float Dc;
  u32x2 e0;
  {
    const rsrc_t rk = mk_rsrc((void*)k, S * D * 2), rv = mk_rsrc((void*)v, S * D * 2);
    u32x2 kk[11], vv[8];
    unsigned vo[4];
#pragma unroll
    for (int i = 0; i < 4; ++i) vo[i] = bid * 16384 + tid * 8 + i * 4096;
#pragma unroll
    for (int r = 0; r < 8; ++r) kk[3 + r] = __builtin_amdgcn_raw_buffer_load_b64(rk, vo[r >> 1] + (r & 1) * 2048, 0, 0);
    SB();
#pragma unroll
    for (int r = 0; r < 3; ++r) kk[r] = __builtin_amdgcn_raw_buffer_load_b64(rk, tid * 8, max(s0 - 3 + r, 0) * 2048, 0);
    const u32x4 wa = *(const u32x4*)(shift_k + tid * 16), wb = *(const u32x4*)(shift_k + tid * 16 + 8);
    SB();
#pragma unroll
    for (int r = 0; r < 8; ++r) vv[r] = __builtin_amdgcn_raw_buffer_load_b64(rv, vo[r >> 1] + (r & 1) * 2048, 0, 0);
    SB();
    if (tid < 2) *(unsigned*)(smem + LDS_CNT + tid * 16) = 0u;
    WAIT_LGKM0();
    wait_vm<KVW>();
    KEEP(pf0);
    BAR();
    SB();
    const uint16_t* hrow = ssm_k + (size_t)(4 * j + w) * S;
#pragma unroll
    for (int i = 0; i < 4; ++i) {
      const int ch = i * 64 + lane;
      hh[i] = ldin<u32x4>(hrow + ch * 8);
      hn[i] = ldin<unsigned>(hrow + min(ch * 8 + 8, S - 2));
    }
    Dc = bflo((unsigned)ssm_D[4 * j + w]);
    e0 = poll8(c.ws + OFF_EPO);
    SB();
    const unsigned hm = bid ? ~0u : 0u;
#pragma unroll
    for (int r = 0; r < 3; ++r) { kk[r][0] &= hm; kk[r][1] &= hm; }
    const int sw = (lane >> 1) & 3;
    auto un = [](unsigned x) { return (f32x2){bflo(x), bfhi(x)}; };
    auto rn = [&](f32x2 x) { return un(pk2v(x)); };
    u32x4 ww = wa;
    unsigned kc[7], vc[4];
#pragma unroll
    for (int r = 0; r < 7; ++r) kc[r] = kk[r][0];
#pragma unroll
    for (int r = 0; r < 4; ++r) vc[r] = vv[r][0];
#pragma unroll 1
    for (int it = 0; it < 4; ++it) {
      const f32x2 w0 = {bflo(ww[0]), bflo(ww[2])}, w1 = {bfhi(ww[0]), bfhi(ww[2])}, w2 = {bflo(ww[1]), bflo(ww[3])}, w3 = {bfhi(ww[1]), bfhi(ww[3])};
      f32x2 kf[7];
      unsigned up[4];
#pragma unroll
      for (int r = 0; r < 7; ++r) kf[r] = un(kc[r]);
#pragma unroll
      for (int r = 0; r < 4; ++r)
        up[r] = pk2v(rn(rn(rn(rn(kf[r] * w3) + rn(kf[r + 1] * w2)) + rn(kf[r + 2] * w1)) + rn(kf[r + 3] * w0)) * un(vc[r]));
#pragma unroll
      for (int hi = 0; hi < 2; ++hi) {
        const unsigned sel = hi ? 0x07060302u : 0x05040100u;
        ((u32x2*)&bounce[lane * 4 + (((it & 2) + hi) ^ sw)])[1 - (it & 1)] = (u32x2){__builtin_amdgcn_perm(up[2], up[3], sel), __builtin_amdgcn_perm(up[0], up[1], sel)};
      }
      if (it == 1) ww = wb;
#pragma unroll
      for (int r = 0; r < 7; ++r) kc[r] = it == 0 ? kk[4 + r][0] : it == 1 ? kk[r][1] : kk[4 + r][1];
#pragma unroll
      for (int r = 0; r < 4; ++r) vc[r] = it == 0 ? vv[4 + r][0] : it == 1 ? vv[r][1] : vv[4 + r][1];
    }
#pragma unroll
    for (int i = 0; i < 4; ++i) {
      const int u = 64 * i + lane;
      st16(c.rw, OFF_UB + (bid * 1024 + w * 256 + u) * 16, bounce[(u & ~3) | ((u & 3) ^ ((u >> 3) & 3))]);
    }
    SB();
  }
  char* my = smem + CV_PAD + w * CV_CH;
  if (lane < 30) *(u32x4*)(my + lane * 16) = (u32x4){0u, 0u, 0u, 0u};
  if (lane >= 32 && lane < 34) *(u32x4*)(my + CV_HA + (lane - 32) * 16) = (u32x4){0u, 0u, 0u, 0u};
  if (lane == 34) *(u32x4*)(my + CV_HB) = (u32x4){0u, 0u, 0u, 0u};
  if (lane == 0) *(u32x4*)(my + CV_HB + 16) = (u32x4){0u, 0u, 0u, hh[0][0] << 16};
#pragma unroll
  for (int i = 0; i < 4; ++i) {
    const int ch = i * 64 + lane;
    *(u32x4*)(my + CV_HA + 32 + ch * 16) = hh[i];
    *(u32x4*)(my + CV_HB + 32 + ch * 16) =
        (u32x4){__builtin_amdgcn_alignbyte(hh[i][1], hh[i][0], 2), __builtin_amdgcn_alignbyte(hh[i][2], hh[i][1], 2),
                __builtin_amdgcn_alignbyte(hh[i][3], hh[i][2], 2), __builtin_amdgcn_alignbyte(hn[i], hh[i][3], 2)};
  }
  asm volatile("" : "+v"(Dc), "+v"(e0));
  c.set_tag(e0);
  wait_vm<0>();
  c.signal(FL_U + w * 256 + bid);
  c.await<DU>(FL_U + (j >> 6) * 256 + w * 64 + lane, true);
#pragma unroll
  for (int i = 0; i < 4; ++i) uu[i] = ld16(c.rw, ((i * 16 + (lane >> 2)) * 1024 + (lane & 3)) * 16, OFF_UB + (w * 65536 + 4 * j) * 16);
#pragma unroll
  for (int i = 0; i < 4; ++i) *(u32x4*)(smem + CV_PAD + (lane & 3) * CV_CH + (30 + w * 64 + i * 16 + (lane >> 2)) * 16) = uu[i];
  const int t0 = lane & 15, kq = lane >> 4;
  const LAS unsigned* fbk[4];
#pragma unroll
  for (int i = 0; i < 4; ++i) {
    fbk[i] = (const LAS unsigned*)(my + ((t0 & 1) ? CV_HA : CV_HB)) + 4 * kq + ((1 + t0) >> 1) + 256 * i;
    asm volatile("" : "+v"(fbk[i]));
  }
  const char* gb = my + (2 * t0 + 3 - kq) * 16;
  f32x4 acc[8];
#pragma unroll
  for (int b = 0; b < 8; ++b) { acc[b] = (f32x4){0.f, 0.f, 0.f, 0.f}; asm volatile("" : "+v"(acc[b])); }
  c.half_sync(1);
  bf16x8 F[2][8], G[2][8];
  u32x2 fh[8];
  auto rdn = [&](int buf, int g0, int n) {
    if (n == 0 || n > 16) { const int g1 = n ? n - 16 : 0; G[buf][g1] = *(const bf16x8*)(gb + (32 * g1 + 4 * (7 - g0)) * 16); }
    else {
      const int e1 = (n - 1) >> 1;
      const LAS unsigned* p = fbk[e1 >> 1] + 128 * (e1 & 1) + 16 * g0;
      if ((n - 1) & 1) F[buf][e1] = as_frag((u32x4){fh[e1][0], fh[e1][1], p[2], p[3]});
      else fh[e1] = (u32x2){p[0], p[1]};
    }
  };
  auto cstep = [&](int cb, int g0n) {
    int n = 0;
#pragma unroll
    for (int g1 = 0; g1 < 8; ++g1)
#pragma unroll
      for (int e1 = 0; e1 + g1 < 8; ++e1) {
        acc[g1 + e1] = MFMA16(F[cb][e1], G[cb][g1], acc[g1 + e1]);
        SB();
        if (n < 24) { rdn(cb ^ 1, g0n, n); SB(); }
        ++n;
      }
  };
#pragma unroll
  for (int n = 0; n < 24; ++n) rdn(0, 0, n);
  for (int gp = 0; gp < 4; ++gp) {
    cstep(0, 1);
    cstep(1, 2);
    gb -= 128;
#pragma unroll
    for (int i = 0; i < 4; ++i) fbk[i] += 32;
  }
  asm volatile("" ::"v"(acc[7]), "v"(acc[0]));
#pragma unroll
  for (int b = 0; b < 8; ++b) {
    const int s = 256 * b + 16 * t0 + 4 * kq;
    const u32x2 ur = *(const u32x2*)(my + (30 + (s >> 3)) * 16 + ((s & 4) ? 0 : 8));
    *(u32x2*)(my + CV_HA + s * 2) = (u32x2){pk2v(rnd2(lo2(acc[b])) + rnd2((f32x2){bfhi(ur[1]), bflo(ur[1])} * Dc)),
                                            pk2v(rnd2(hi2(acc[b])) + rnd2((f32x2){bfhi(ur[0]), bflo(ur[0])} * Dc))};
  }
  WAIT_LGKM0();
  BAR();
  conv_store(c);
  const unsigned pf = c.touch_code();
  c.await<DX1>(FL_X1 + w * 64 + lane, true);
  KEEP(pf);
  BAR();
  if (bid == 0 && tid == 0) put8(c.ws + OFF_EPO, c.tag);
}

DI void front_h(Cx& c, unsigned pf, const uint16_t* q, const uint16_t* wqkv, const uint16_t* wo) {
  const int lane = c.lane, w4 = c.w - 4, tid = c.tid, bid = c.bid, s0 = bid * 8, j = (bid & 7) * 32 + (bid >> 3);
  char* smem = c.smem;
  const rsrc_t rw = c.rw;
  u32x4* bounce = (u32x4*)(smem + CV_BN) + c.w * 256;
  float* red = (float*)smem;
  const int gw = bid * 4 + w4;
  char* buf = smem + CV_WB + w4 * 8192;
  const int g = lane >> 4, qr = (lane >> 2) & 3, p = lane & 3, rr = lane >> 3;
  const bool isw = gw < 768;
  const int cg = isw ? gw % 48 : (gw - 768) & 15, og = isw ? gw / 48 : (gw - 768) >> 4;
  auto w_loads = [&]() {
    if (isw) {
      const uint16_t* src = wqkv + (size_t)(og * 64 + rr) * 3072 + cg * 64 + (lane & 7) * 8;
#pragma unroll
      for (int i = 0; i < 8; ++i) glds16(src + (size_t)i * 8 * 3072, buf + i * 1024);
    } else {
      const uint16_t* src = wo + (size_t)(og * 64 + (rr & 3) + 16 * (rr >> 2)) * 1024 + cg * 64 + (lane & 7) * 8;
#pragma unroll
      for (int i = 0; i < 8; ++i) glds16(src + (size_t)((i >> 2) * 32 + (i & 3) * 4) * 1024, buf + i * 1024);
    }
  };
  u32x2 e0 = poll8(c.ws + OFF_EPO);
  SB();
  BAR();
  u32x2 qq[8];
#pragma unroll
  for (int r = 0; r < 8; ++r) qq[r] = ldin<u32x2>(q + (size_t)(s0 + r) * D + tid * 4);
  SB();
  w_loads();
  SB();
  const int sw = (lane >> 1) & 3;
#pragma unroll
  for (int r = 0; r < 4; ++r) bounce[lane * 4 + (r ^ sw)] = (u32x4){qq[2 * r][0], qq[2 * r][1], qq[2 * r + 1][0], qq[2 * r + 1][1]};
#pragma unroll
  for (int i = 0; i < 4; ++i) {
    const int u = 64 * i + lane;
    st16(rw, OFF_X0 + (bid * 1024 + w4 * 256 + u) * 16, bounce[(u & ~3) | ((u & 3) ^ ((u >> 3) & 3))]);
  }
  if (tid < 80) st16(rw, OFF_GX + bid * 1280 + tid * 16, (u32x4){~0u, ~0u, ~0u, ~0u});
  float a[16];
#pragma unroll
  for (int r = 0; r < 8; ++r) {
    a[2 * r] = dot2(qq[r][0], 0x3f803f80u, dot2(qq[r][1], 0x3f803f80u, 0.f));
    a[2 * r + 1] = dot2(qq[r][0], qq[r][0], dot2(qq[r][1], qq[r][1], 0.f));
  }
#pragma unroll
  for (int i = 0; i < 8; ++i) a[i] = swap32_sum(a[i], a[i + 8]);
#pragma unroll
  for (int i = 0; i < 4; ++i) a[i] = swap16_sum(a[i], a[i + 4]);
#pragma unroll
  for (int i = 0; i < 4; ++i) { a[i] += row_ror<1>(a[i]); a[i] += row_ror<2>(a[i]); a[i] += row_ror<4>(a[i]); a[i] += row_ror<8>(a[i]); }
  if ((lane & 15) == 0) *(f32x4*)&red[w4 * 16 + (lane >> 4) * 4] = (f32x4){a[0], a[1], a[2], a[3]};
  c.half_sync(1);
  if (tid < 8) {
    const float sm = (red[2 * tid] + red[16 + 2 * tid]) + (red[32 + 2 * tid] + red[48 + 2 * tid]);
    const float sq = (red[2 * tid + 1] + red[17 + 2 * tid]) + (red[33 + 2 * tid] + red[49 + 2 * tid]);
    st8(rw, OFF_ST0 + (s0 + tid) * 8, __builtin_bit_cast(u32x2, ln_stats(sm, sq)));
  }
  asm volatile("" : "+v"(e0));
  c.set_tag(e0);
  wait_vm<0>();
  KEEP(pf);
  c.signal(FL_Q + w4 * 256 + bid);
  const int hd = (cg & 15) * 2 + (lane >> 5), col = (cg >> 4) * 32 + (lane & 31);
  const unsigned dst = isw ? OFF_W8 + ((hd * 128 + og * 8) * 96 + col) * 16
                           : OFF_WOF + ((((og >> 1) * 32 + cg * 2 + (g >> 1)) * 8 + (og & 1) * 4) * 64 + (lane & 31)) * 16;
  const unsigned dstep = isw ? 1536 : 512;
  const unsigned la = (unsigned)(size_t)(LAS char*)buf + qr * 128 + (isw ? g * 32 + p * 8 : (g >> 1) * 64 + (p & 1) * 32 + ((g & 1) * 2 + (p >> 1)) * 8);
  u32x4 wv[8];
  float cs = 0.f;
#pragma unroll
  for (int i = 0; i < 8; ++i) {
    const u32x2 lo = __builtin_bit_cast(u32x2, __builtin_amdgcn_ds_read_tr16_b64_v4i16((LAS s16x4*)(size_t)(la + i * 1024)));
    const u32x2 hi = __builtin_bit_cast(u32x2, __builtin_amdgcn_ds_read_tr16_b64_v4i16((LAS s16x4*)(size_t)(la + i * 1024 + 512)));
    wv[i] = (u32x4){lo[0], lo[1], hi[0], hi[1]};
  }
#pragma unroll
  for (int i = 0; i < 8; ++i)
#pragma unroll
    for (int e = 0; e < 4; ++e) cs = opq(opq(cs + bflo(wv[i][e])) + bfhi(wv[i][e]));
  asm volatile("" : "+v"(cs));
  c.half_sync(2);
  c.watch(0, 1);
#pragma unroll
  for (int i = 0; i < 8; ++i) st16(rw, dst + i * dstep, wv[i]);
  c.clear_slab(0);
  if (isw) __builtin_amdgcn_raw_buffer_store_b32(__float_as_uint(cs), rw, OFF_CSP + ((hd * 96 + col) * 16 + og) * 4, 0, 16);
  wait_vm<0>();
  if (isw) c.signal(FL_W + gw);
  c.await2(FL_Q + w4 * 64 + lane, FL_Q + (j >> 6) * 256 + w4 * 64 + lane);
#pragma unroll
  for (int i = 0; i < 4; ++i) {
    const int pp = w4 * 256 + i * 64 + lane;
    dma16(rw, smem + CV_X0 + (w4 * 4 + i) * 1024, ((pp >> 2) * 1024 + (pp & 3)) * 16, OFF_X0 + j * 64);
    dma16(rw, smem + CV_ST + (w4 * 4 + i) * 1024, pp * 16, OFF_ST0);
  }
  wait_vm<0>();
  BAR();
  conv_store(c);
  c.w_pair<1>(0); c.w_pair<1>(1); c.w_pair<1>(2);
  wait_vm<0>();
  BAR();
}

template <int L> DI void g1_m(const Cx& c) {
  constexpr bool C4 = L == 0;
  const int lane = c.lane, w = c.w, tid = c.tid, xcd = c.bid & 7, idx = c.bid >> 3;
  const int band = (xcd & 1) * 4 + (idx & 3), h = (xcd >> 1) * 8 + (idx >> 2), r = lane & 15, kq = lane >> 4;
  char* smem = c.smem;
  const float* Rs = (const float*)smem;
  const float* MRs = Rs + 256;
  const float* CSs = Rs + 512;
  const float* R2s = Rs + 640;
  float* P = (float*)smem + 896;
  constexpr unsigned OFF_X = C4 ? OFF_X1 : OFF_X3, KSTEP = 4 * S * 16;
  const unsigned va = C4 ? (kq * 2048 + band * 128 + 32 * w + r) * 16 : (kq * S + band * 256 + 64 * w + r) * 16;
  const unsigned vb = va + 16384;
  u32x4 A[4][4];
  auto lda = [&](int slot, int m, unsigned so) {
    A[slot][m] = C4 ? ld16(c.rw, ((m & 1) ? vb : va) + (m >> 1) * 256, so) : ld16(c.rw, va + m * 256, so);
    SB();
  };
#pragma unroll
  for (int n = 0; n < 16; ++n) lda(n >> 2, n & 3, OFF_X + (n >> 2) * KSTEP);
  const char* bb = smem + WR + kq * 1536 + r * 16;
  f32x4 acc[4][6];
#pragma unroll
  for (int m = 0; m < 4; ++m)
#pragma unroll
    for (int p = 0; p < 6; ++p) { acc[m][p] = (f32x4){0.f, 0.f, 0.f, 0.f}; asm volatile("" : "+v"(acc[m][p])); }
  bf16x8 B[2][6];
  auto fix = [&](int slot, int n) {
    if (C4) {
      asm("v_swap_b32 %0, %1" : "+v"(A[slot][2 * n][2]), "+v"(A[slot][2 * n + 1][0]));
      asm("v_swap_b32 %0, %1" : "+v"(A[slot][2 * n][3]), "+v"(A[slot][2 * n + 1][1]));
      SB();
    }
  };
  auto step = [&](int t, const char* b, bool rd, unsigned so, bool ld) {
#pragma unroll
    for (int i = 0; i < 24; ++i) {
      const int m = i / 6, p = i % 6;
      if (p < 2) acc[m][p] = MFMA16(B[t & 1][p], as_frag(A[t][m]), acc[m][p]);
      else acc[m][p] = MFMA16(as_frag(A[t][m]), B[t & 1][p], acc[m][p]);
      SB();
      if (rd && m < 3 && (p == 1 || p == 3)) { B[(t & 1) ^ 1][2 * m + (p >> 1)] = *(const bf16x8*)(b + (2 * m + (p >> 1)) * 256); SB(); }
      if (ld && p == 5) lda(t, m, so);
      if (i == 8) fix(t, 1);
      if (rd && i == 20) fix((t + 1) & 3, 0);
    }
  };
#pragma unroll
  for (int i = 0; i < 6; ++i) B[0][i] = *(const bf16x8*)(bb + i * 256);
  fix(0, 0);
  asm volatile("s_nop 1");
  SB();
  int so = 0;
  unsigned go = OFF_X + 4 * KSTEP;
  for (int it = 0; it < 7; ++it) {
    step(0, bb + so + 6144, true, go, true);
    step(1, bb + so + 12288, true, go + KSTEP, true);
    step(2, bb + so + 18432, true, go + 2 * KSTEP, true);
    WAIT_LGKM0();
    BAR();
    so = (so == 2 * WR_SLOT) ? 0 : so + WR_SLOT;
    step(3, bb + so, true, go + 3 * KSTEP, true);
    go += 4 * KSTEP;
  }
#pragma unroll
  for (int t = 0; t < 4; ++t)
#pragma unroll
    for (int i = 0; i < 16; ++i) {
      const int m = i >> 2, p = 2 + (i & 3);
      acc[m][p] = MFMA16(as_frag(A[t][m]), B[t & 1][p], acc[m][p]);
      if (t < 3 || i < 8) SB();
      if (t < 3 && i < 8 && (i & 1)) { B[(t & 1) ^ 1][2 + (i >> 1)] = *(const bf16x8*)(bb + so + (t + 1) * 6144 + (2 + (i >> 1)) * 256); SB(); }
      if (i == 4) fix(t, 1);
      if (t < 3 && i == 12) fix(t + 1, 0);
    }
  f32x4 Z[2][2], Zz[2];
#pragma unroll
  for (int i = 0; i < 2; ++i) { Z[i][0] = (f32x4){0.f, 0.f, 0.f, 0.f}; Z[i][1] = Z[i][0]; Zz[i] = Z[i][0]; }
  {
    float ck[4];
#pragma unroll
    for (int p = 0; p < 4; ++p) ck[p] = CSs[32 + p * 16 + r];
    const bf16x8 ones = as_frag((u32x4){0x3f803f80u, 0x3f803f80u, 0x3f803f80u, 0x3f803f80u});
#pragma unroll
    for (int jj = 0; jj < 2; ++jj) {
      u32x4 fr[4];
#pragma unroll
      for (int mm = 0; mm < 2; ++mm) {
        const int m = 2 * jj + mm;
        const f32x4 rs = *(const f32x4*)(Rs + 64 * w + 16 * m + 4 * kq), mr = *(const f32x4*)(MRs + 64 * w + 16 * m + 4 * kq);
        const f32x4 r2 = *(const f32x4*)(R2s + 64 * w + 16 * m + 4 * kq);
#pragma unroll
        for (int p = 0; p < 2; ++p) {
          fr[p][2 * mm] = pk2v(phi2(lo2(acc[m][2 + p]) * lo2(r2) + (lo2(mr) * ck[p] + EPS2)));
          fr[p][2 * mm + 1] = pk2v(phi2(hi2(acc[m][2 + p]) * hi2(r2) + (hi2(mr) * ck[p] + EPS2)));
          fr[2 + p][2 * mm] = pk2v(lo2(acc[m][4 + p]) * lo2(rs) + lo2(mr) * ck[2 + p]);
          fr[2 + p][2 * mm + 1] = pk2v(hi2(acc[m][4 + p]) * hi2(rs) + hi2(mr) * ck[2 + p]);
        }
      }
#pragma unroll
      for (int dh = 0; dh < 2; ++dh) {
        Z[0][dh] = MFMA16(as_frag(fr[dh]), as_frag(fr[2]), Z[0][dh]);
        Z[1][dh] = MFMA16(as_frag(fr[dh]), as_frag(fr[3]), Z[1][dh]);
        Zz[dh] = MFMA16(as_frag(fr[dh]), ones, Zz[dh]);
      }
    }
  }
  {
    float* mine = P + w * 1056;
#pragma unroll
    for (int i = 0; i < 4; ++i) *(f32x4*)(mine + i * 256 + lane * 4) = Z[i >> 1][i & 1];
    if (r == 0) { *(f32x4*)(mine + 1024 + 4 * kq) = Zz[0]; *(f32x4*)(mine + 1040 + 4 * kq) = Zz[1]; }
  }
  c.half_sync(3 * L + 2);
  const unsigned slab = OFF_SL + (L * 256 + h * 8) * SLAB;
  {
    auto put = [&](int u) {
      const f32x4* P4 = (const f32x4*)P + u;
      u32x4 x = __builtin_bit_cast(u32x4, (P4[0] + P4[264]) + (P4[528] + P4[792]));
#pragma unroll
      for (int e = 0; e < 4; ++e) x[e] = min(x[e], 0xfffffffeu);
      st16(c.rw, slab + band * SLAB + u * 16, x);
    };
    put(tid);
    if (tid < 8) put(256 + tid);
  }
  SB();
  {
    bf16x8 Bq[8];
#pragma unroll
    for (int n = 0; n < 8; ++n) Bq[n] = *(const bf16x8*)(bb + so + (n >> 1) * 6144 + (n & 1) * 256);
#pragma unroll
    for (int m = 0; m < 4; ++m)
#pragma unroll
      for (int n = 0; n < 8; ++n) acc[m][n & 1] = MFMA16(Bq[n], as_frag(A[n >> 1][m]), acc[m][n & 1]);
  }
  u32x4 pq[4], g[8], gz;
  auto gather = [&]() {
#pragma unroll
    for (int b = 0; b < 8; ++b) g[b] = ld16(c.rw, tid * 16, slab + b * SLAB);
    gz = ld16(c.rw, (lane >> 3) * SLAB + 4096 + (lane & 7) * 16, slab);
  };
  auto missing = [&]() {
    unsigned mx = max(max(gz[0], gz[1]), max(gz[2], gz[3]));
#pragma unroll
    for (int b = 0; b < 8; ++b) mx = max(max(mx, max(g[b][0], g[b][1])), max(g[b][2], g[b][3]));
    return __any(mx == ~0u);
  };
  {
    const f32x4 c0 = *(const f32x4*)(CSs + 4 * kq), c1 = *(const f32x4*)(CSs + 16 + 4 * kq);
#pragma unroll
    for (int m = 0; m < 4; ++m) {
      const float rs = R2s[64 * w + 16 * m + r], mr = MRs[64 * w + 16 * m + r];
      pq[m] = (u32x4){pk2v(phi2(lo2(acc[m][0]) * rs + (lo2(c0) * mr + EPS2))), pk2v(phi2(hi2(acc[m][0]) * rs + (hi2(c0) * mr + EPS2))),
                      pk2v(phi2(lo2(acc[m][1]) * rs + (lo2(c1) * mr + EPS2))), pk2v(phi2(hi2(acc[m][1]) * rs + (hi2(c1) * mr + EPS2)))};
      asm volatile("" : "+v"(pq[m]));
      if (m == GAT) { SB(); gather(); SB(); }
    }
  }
  u32x2* KV = (u32x2*)(smem + 20480);
  {
#pragma unroll 1
    for (int spin = 0; spin < SPIN_MAX; ++spin) {
      if (!missing()) break;
      asm volatile("" ::: "memory");
      gather();
    }
    f32x4 sum = __builtin_bit_cast(f32x4, g[0]);
#pragma unroll
    for (int b = 1; b < 8; ++b) sum += __builtin_bit_cast(f32x4, g[b]);
    KV[tid] = (u32x2){pk2v(lo2(sum)), pk2v(hi2(sum))};
    if (w == 0) {
      f32x4 z = __builtin_bit_cast(f32x4, gz);
#pragma unroll
      for (int e = 0; e < 4; ++e) { z[e] += row_ror<8>(z[e]); z[e] = swap16_sum(z[e], z[e]); z[e] = swap32_sum(z[e], z[e]); }
      if (lane < 8) KV[256 + lane] = (u32x2){pk2v(lo2(z)), pk2v(hi2(z))};
    }
  }
  c.half_sync(3 * L + 3);
  {
    const u32x2 a0 = KV[lane], a1 = KV[64 + lane], b0 = KV[128 + lane], b1 = KV[192 + lane], z0 = KV[256 + kq], z1 = KV[260 + kq];
    const bf16x8 k0 = as_frag((u32x4){a0[0], a0[1], a1[0], a1[1]}), k1 = as_frag((u32x4){b0[0], b0[1], b1[0], b1[1]});
    const bf16x8 zb = as_frag((u32x4){z0[0], z0[1], z1[0], z1[1]});
    const f32x4 zero = {0.f, 0.f, 0.f, 0.f}, eps = {1e-6f, 1e-6f, 1e-6f, 1e-6f};
#pragma unroll
    for (int m = 0; m < 4; ++m) {
      const f32x4 dn = MFMA16(zb, as_frag(pq[m]), eps), x0 = MFMA16(k0, as_frag(pq[m]), zero), x1 = MFMA16(k1, as_frag(pq[m]), zero);
      const float inv = __builtin_amdgcn_rcpf(dn[0]);
      st16(c.rw, OFF_O + L * 4 * MB + ((((h >> 2) * 128 + band * 16 + w * 4 + m) * 4 + (h & 3)) * 64 + lane) * 16,
           (u32x4){pk2v(lo2(x0) * inv), pk2v(hi2(x0) * inv), pk2v(lo2(x1) * inv), pk2v(hi2(x1) * inv)});
    }
  }
  wait_vm<0>();
  c.signal(FL_O + L * 1024 + (band * 4 + w) * 32 + h);
}

template <int L> DI void g1_h(const Cx& c) {
  constexpr bool C4 = L == 0;
  constexpr unsigned OFF_X = C4 ? OFF_X1 : OFF_X3;
  const int lane = c.lane, lw = c.w - 4, t = c.tid, xcd = c.bid & 7, idx = c.bid >> 3;
  const int band = (xcd & 1) * 4 + (idx & 3), h = (xcd >> 1) * 8 + (idx >> 2);
  char* smem = c.smem;
  char* ws = c.ws;
  const rsrc_t rw = c.rw;
  float* Rs = (float*)smem;
  u32x4 yo[4];
  if (C4) {
#pragma unroll
    for (int i = 0; i < 4; ++i) yo[i] = y_pair(smem, t + 256 * i);
    __builtin_amdgcn_s_sleep(8);
  }
  if (C4) {
    u32x4 xv[4];
#pragma unroll
    for (int i = 0; i < 4; ++i) xv[i] = ld16(rw, ((t >> 2) * 1024 + (t & 3)) * 16, OFF_X + (i * 65536 + band * 128 + h * 4) * 16);
    float v[4] = {0.f, 0.f, 0.f, 0.f};
#pragma unroll
    for (int i = 0; i < 4; ++i)
#pragma unroll
      for (int e = 0; e < 4; ++e) {
        const float lo = bflo(xv[i][e]), hi = bfhi(xv[i][e]);
        v[e & 2] = opq(v[e & 2] + lo); v[(e & 2) + 1] = opq(fmaf(lo, lo, v[(e & 2) + 1]));
        v[e & 2] = opq(v[e & 2] + hi); v[(e & 2) + 1] = opq(fmaf(hi, hi, v[(e & 2) + 1]));
        if (i == 0 && e == 0) { SB(); BAR(); c.w_pair(3); SB(); }
      }
#pragma unroll
    for (int e = 0; e < 4; ++e) { v[e] += row_ror<4>(v[e]); v[e] += row_ror<8>(v[e]); v[e] = swap16_sum(v[e], v[e]); v[e] = swap32_sum(v[e], v[e]); }
    const bool up = (lane & 4) != 0;
    if (lane < 8) publish8(ws + OFF_GX + ((band * 4 + lw) * 256 + h * 8 + 2 * (lane & 3) + (lane >> 2)) * 8, up ? v[2] : v[0], up ? v[3] : v[1]);
    c.clear_slab(1);
  } else { BAR(); c.w_pair(3); }
#pragma unroll
  for (int n = 1; n < 4; ++n) {
    wait_vm<6>();
    BAR();
    if (C4) st16(rw, OFF_Y + ((xcd * 32 + idx) * 1024 + t + 256 * (n - 1)) * 16, yo[n - 1]);
    c.w_pair(n + 3);
  }
  const char* gp = ws + (C4 ? OFF_GX + (band * 1024 + t) * 8 + 4096 : OFF_ST3 + ((band * 4 + lw) * 512 + lane) * 8);
  const char* cp = ws + OFF_CSP + (h * 96 + (t < 96 ? t : 0)) * 64;
  u32x2 g[8];
  f32x4 c0, c1, c2, c3;
  asm volatile("global_load_dwordx4 %0, %4, off sc1\n\tglobal_load_dwordx4 %1, %4, off offset:16 sc1\n\t"
               "global_load_dwordx4 %2, %4, off offset:32 sc1\n\tglobal_load_dwordx4 %3, %4, off offset:48 sc1"
               : "=&v"(c0), "=&v"(c1), "=&v"(c2), "=&v"(c3) : "v"(cp) : "memory");
  if (C4) {
    asm volatile("global_load_dwordx2 %0, %4, off offset:-4096 sc1\n\tglobal_load_dwordx2 %1, %4, off offset:-2048 sc1\n\t"
                 "global_load_dwordx2 %2, %4, off sc1\n\tglobal_load_dwordx2 %3, %4, off offset:2048 sc1"
                 : "=&v"(g[0]), "=&v"(g[1]), "=&v"(g[2]), "=&v"(g[3]) : "v"(gp) : "memory");
    wait_vm<6 + 8>();
  } else {
    asm volatile("global_load_dwordx2 %0, %8, off sc1\n\tglobal_load_dwordx2 %1, %8, off offset:512 sc1\n\tglobal_load_dwordx2 %2, %8, off offset:1024 sc1\n\t"
                 "global_load_dwordx2 %3, %8, off offset:1536 sc1\n\tglobal_load_dwordx2 %4, %8, off offset:2048 sc1\n\tglobal_load_dwordx2 %5, %8, off offset:2560 sc1\n\t"
                 "global_load_dwordx2 %6, %8, off offset:3072 sc1\n\tglobal_load_dwordx2 %7, %8, off offset:3584 sc1"
                 : "=&v"(g[0]), "=&v"(g[1]), "=&v"(g[2]), "=&v"(g[3]), "=&v"(g[4]), "=&v"(g[5]), "=&v"(g[6]), "=&v"(g[7]) : "v"(gp) : "memory");
    wait_vm<6 + 12>();
  }
  BAR();
  if (C4) st16(rw, OFF_Y + ((xcd * 32 + idx) * 1024 + t + 768) * 16, yo[3]);
  c.w_pair(7);
  if (C4) asm volatile("s_waitcnt vmcnt(6)" : "+v"(g[0]), "+v"(g[1]), "+v"(g[2]), "+v"(g[3]), "+v"(c0), "+v"(c1), "+v"(c2), "+v"(c3)::"memory");
  else asm volatile("s_waitcnt vmcnt(6)" : "+v"(g[0]), "+v"(g[1]), "+v"(g[2]), "+v"(g[3]), "+v"(g[4]), "+v"(g[5]), "+v"(g[6]), "+v"(g[7]),
                    "+v"(c0), "+v"(c1), "+v"(c2), "+v"(c3)::"memory");
  BAR();
#pragma unroll 1
  for (int spin = 0; spin < SPIN_MAX; ++spin) {
    bool empty = false;
#pragma unroll
    for (int k = 0; k < (C4 ? 4 : 8); ++k) empty |= g[k][1] == ~0u;
    if (!__any(empty)) break;
    __builtin_amdgcn_s_sleep(4);
#pragma unroll
    for (int k = 0; k < (C4 ? 4 : 8); ++k) g[k] = poll8(gp + (C4 ? (k - 2) * 2048 : k * 512));
  }
  float sm = 0.f, sq = 0.f, cs = 0.f;
#pragma unroll
  for (int k = 0; k < (C4 ? 4 : 8); ++k) { sm = opq(sm + __uint_as_float(g[k][0])); sq = opq(sq + __uint_as_float(g[k][1])); }
#pragma unroll
  for (int k = 0; k < 4; ++k) cs = opq(opq(opq(opq(cs + c0[k]) + c1[k]) + c2[k]) + c3[k]);
  const f32x2 st = ln_stats(sm, sq);
  const int ts = C4 ? (t & ~31) + (t & 1) * 16 + ((t & 31) >> 1) : t;
  Rs[ts] = st[1]; Rs[256 + ts] = -st[0] * st[1]; Rs[640 + ts] = st[1] * LOG2E;
  if (t < 96) Rs[512 + t] = t < 64 ? cs * LOG2E : cs;
  wait_vm<0>();
  {
    const unsigned swo = OFF_WOF + (((xcd & 1) * 4 + (idx >> 3)) * 4 + lw) * 8192;
#pragma unroll
    for (int n = 6; n < 12; ++n) dma16(rw, smem + WR + lw * 6144 + 2 * WR_SLOT + (n % 6) * 1024, lane * 16, swo + ((G2NF + n) >> 3) * 262144 + ((G2NF + n) & 7) * 1024);
  }
  WAIT_LGKM0();
  BAR();
  if (C4 && lw == 0) c.signal(FL_Y + xcd * 32 + idx);
}

template <int L> DI void g2_m(const Cx& c) {
  const int lane = c.lane, w = c.w, xcd = c.bid & 7, idx = c.bid >> 3, sb = (xcd >> 1) * 8 + (idx & 7);
  unsigned pf = c.touch_code();
  c.half_sync(3 * L + 4);
  c.await<DG1>(FL_O + L * 1024 + sb * 32 + (lane & 7) * 4 + w, lane < 8);
  KEEP(pf);
  auto issue = [&](int i) {
#pragma unroll
    for (int st = 0; st < 4; ++st)
      dma16(c.rw, c.smem + (((i % G2_NS) * 4 + w) * 4 + st) * 1024, lane * 16, OFF_O + L * 4 * MB + (i * 512 + sb * 16 + st * 4 + w) * 1024);
  };
  issue(0); issue(1); issue(2);
#pragma unroll
  for (int i = 0; i < 8; ++i) {
    if (i < 6) wait_vm<8>(); else if (i == 6) wait_vm<4>(); else wait_vm<0>();
    BAR();
    if (i + 3 < 8) issue(i + 3);
  }
  if (L == 0) {
    pf = c.touch_code();
    c.w_pair<3>(0); c.w_pair<3>(1); c.w_pair<3>(2);
    wait_vm<0>();
    KEEP(pf);
    BAR();
    c.await<DX3>(FL_X3 + (((xcd & 1) * 4 + (idx & 3)) * 4 + w) * 32 + (lane & 31), lane < 32);
  }
}

template <int L> DI void g2_h(const Cx& cx, char* out) {
  const int lane = cx.lane, c = cx.w - 4, xcd = cx.bid & 7, idx = cx.bid >> 3;
  const int sb = (xcd >> 1) * 8 + (idx & 7), nb = (xcd & 1) * 4 + (idx >> 3);
  const int n0 = nb * 128 + c * 32, s0 = sb * 64, r = lane & 31, hf = lane >> 5;
  const int rr = L == 0 ? 2 * (r & 15) + (r >> 4) : r;
  const rsrc_t rw = cx.rw;
  const unsigned l16 = lane * 16;
  constexpr int NF = G2NF, LA = G2LA, NO = LA + 1;
  u32x4 wo[NF], of[NO];
  const unsigned swo = OFF_WOF + (nb * 4 + c) * 8192;
  auto ldw = [&](int f) { wo[f % NF] = ld16(rw, l16 + (f & 3) * 1024, swo + (f >> 3) * 262144 + ((f >> 2) & 1) * 4096); SB(); };
  auto ldx = [&](unsigned off, bool c4, int k) {
    const int s2 = k >> 1, g = k & 1;
    if (!c4) return ld16(rw, (2 * hf * S + rr) * 16 + s2 * 512, off + (((n0 >> 3) + g) * S + s0) * 16);
    const unsigned so = off + (((n0 >> 2) + 2 * g) * S + s0) * 8, vo = (4 * hf * S + rr) * 8 + s2 * 256;
    const u32x2 lo = ld8(rw, vo, so), hi = ld8(rw, vo, so + S * 8);
    return (u32x4){lo[0], lo[1], hi[0], hi[1]};
  };
  if (DPF) __builtin_amdgcn_s_sleep(DPF);
  u32x4 res[4], yv[2][2];
#pragma unroll
  for (int k = 0; k < 4; ++k) res[k] = ldx(L == 0 ? OFF_X1 : OFF_X3, L == 0, k);
  SB();
#pragma unroll
  for (int n = 0; n < NF; ++n) ldw(n);
  char* park = cx.smem + WR + c * 6144;
#pragma unroll
  for (int n = 0; n < 6; ++n) dma16(rw, park + (n / 6) * 2 * WR_SLOT + (n % 6) * 1024, l16, swo + ((NF + n) >> 3) * 262144 + ((NF + n) & 7) * 1024);
  SB();
  f32x16 acc[2];
#pragma unroll
  for (int i = 0; i < 16; ++i) { const unsigned u = res[i >> 2][i & 3]; acc[i >> 3][2 * (i & 7)] = bflo(u); acc[i >> 3][2 * (i & 7) + 1] = bfhi(u); }
  asm volatile("" : "+v"(acc[0]), "+v"(acc[1]));
  if (L == 0) cx.await<0>(FL_Y + (n0 >> 2) + (lane & 7), lane < 8);
  const u32x4* Ob = (const u32x4*)cx.smem + (r >> 4) * 64 + hf * 16 + (r & 15);
  auto rdo = [&](int f) { const int u = f & 15; of[f % NO] = Ob[(((f >> 4) % G2_NS) * 16 + (u >> 2) * 4 + (u & 1) * 2) * 64 + ((u >> 1) & 1) * 32]; SB(); };
  wait_vm<0>();
  BAR();
#pragma unroll
  for (int f = 0; f < LA; ++f) rdo(f);
#pragma unroll
  for (int i = 0; i < 8; ++i)
#pragma unroll
    for (int n = 0; n < 16; ++n) {
      const int f = i * 16 + n;
      acc[n & 1] = MFMA32(as_frag(wo[(f >> 1) % NF]), as_frag(of[f % NO]), acc[n & 1]);
      SB();
      if (f + LA < 128) { if (((f + LA) & 15) == 0) BAR(); rdo(f + LA); }
      if ((n & 1) && (f >> 1) + NF < 64) {
        const int fr = f >> 1;
        if (fr < 12) { wo[fr] = *(const u32x4*)(park + (fr / 6) * 2 * WR_SLOT + (fr % 6) * 1024 + l16); SB(); } else ldw(fr + NF);
      }
      if (L == 0 && i == YST && (n & 3) == 1) { yv[n >> 3][(n >> 2) & 1] = ldx(OFF_Y, true, n >> 2); SB(); }
    }
  u32x4 x[2][2];
#pragma unroll
  for (int s2 = 0; s2 < 2; ++s2)
#pragma unroll
    for (int g = 0; g < 2; ++g)
#pragma unroll
      for (int e = 0; e < 4; ++e) x[s2][g][e] = pk2(acc[s2][8 * g + 2 * e], acc[s2][8 * g + 2 * e + 1]);
  if (L == 1) {
    {
      u32x4* T = (u32x4*)(cx.smem + WR);
#pragma unroll
      for (int s2 = 0; s2 < 2; ++s2)
#pragma unroll
        for (int g = 0; g < 2; ++g) T[(32 * s2 + r) * 16 + ((c * 4 + hf * 2 + g) ^ (r & 15))] = x[s2][g];
      cx.half_sync(6);
      const rsrc_t ro = mk_rsrc(out, S * D * 2);
#pragma unroll
      for (int i = 0; i < 4; ++i) {
        const int row = c * 16 + i * 4 + (lane >> 4);
        __builtin_amdgcn_raw_buffer_store_b128(T[row * 16 + ((lane & 15) ^ (row & 15))], ro, ((s0 + row) * D + nb * 128) * 2 + (lane & 15) * 16, 0, 16);
      }
    }
    return;
  }
  f32x2* Red = (f32x2*)(cx.smem + G2_RED);
  auto rowpart = [&]() {
    float v[4] = {0.f, 0.f, 0.f, 0.f};
#pragma unroll
    for (int e = 0; e < 8; ++e)
#pragma unroll
      for (int s2 = 0; s2 < 2; ++s2) { const unsigned u = x[s2][e >> 2][e & 3]; v[2 * s2] = dot2(u, 0x3f803f80u, v[2 * s2]); v[2 * s2 + 1] = dot2(u, u, v[2 * s2 + 1]); }
    return (f32x2){swap32_sum(v[0], v[2]), swap32_sum(v[1], v[3])};
  };
  Red[c * 64 + lane] = rowpart();
  cx.half_sync(3);
  char* gx = cx.ws + OFF_GX2 + (sb * 512 + lane) * 8;
  u32x2 g[8];
  if (c == 0) {
    const f32x2 t = (Red[lane] + Red[64 + lane]) + (Red[128 + lane] + Red[192 + lane]);
    publish8(gx + nb * 512, t[0], t[1]);
    __builtin_amdgcn_s_sleep(XDELAY);
#pragma unroll
    for (int k = 0; k < 8; ++k) g[k] = poll8(gx + k * 512);
  }
  SB();
  f32x2 xa[16], xy[16], yf[16];
#pragma unroll
  for (int e = 0; e < 16; ++e) {
    const unsigned xu = x[e >> 3][(e >> 2) & 1][e & 3], yu = yv[e >> 3][(e >> 2) & 1][e & 3];
    xa[e] = (f32x2){bflo(xu), bfhi(xu)}; yf[e] = (f32x2){bflo(yu), bfhi(yu)}; xy[e] = xa[e] * yf[e];
    asm volatile("" : "+v"(xa[e]), "+v"(yf[e]), "+v"(xy[e]));
  }
  if (c == 0) {
    float sm, sq;
#pragma unroll 1
    for (int spin = 0; spin < SPIN_MAX; ++spin) {
      bool empty = false;
      sm = 0.f; sq = 0.f;
#pragma unroll
      for (int k = 0; k < 8; ++k) { empty |= g[k][1] == ~0u; sm += __uint_as_float(g[k][0]); sq += __uint_as_float(g[k][1]); }
      if (!__any(empty)) break;
      __builtin_amdgcn_s_sleep(2);
#pragma unroll
      for (int k = 0; k < 8; ++k) g[k] = poll8(gx + k * 512);
    }
    const f32x2 t = ln_stats(sm, sq);
    Red[lane] = (f32x2){-t[0] * t[1], t[1]};
  }
  cx.half_sync(4);
#pragma unroll
  for (int s2 = 0; s2 < 2; ++s2) {
    const f32x2 st = Red[32 * s2 + r];
#pragma unroll
    for (int g = 0; g < 2; ++g) {
#pragma unroll
      for (int e = 0; e < 4; ++e) { const int n = s2 * 8 + g * 4 + e; x[s2][g][e] = pk2v(xy[n] * st[1] + (yf[n] * st[0] + xa[n])); }
      st16(rw, OFF_X3 + (((n0 >> 3) + 2 * hf + g) * S + s0 + 32 * s2 + rr) * 16, x[s2][g]);
    }
  }
  Red[256 + c * 64 + lane] = rowpart();
  wait_vm<0>();
  cx.signal(FL_X3 + (sb * 8 + nb) * 4 + c);
  cx.half_sync(5);
  if (c == 1) {
    const f32x2 t = (Red[256 + lane] + Red[320 + lane]) + (Red[384 + lane] + Red[448 + lane]);
    publish8(cx.ws + OFF_ST3 + ((sb * 8 + nb) * 64 + 32 * hf + rr) * 8, t[0], t[1]);
  }
  WAIT_LGKM0();
  BAR();
}

__global__ __launch_bounds__(512, 2) void fused_kernel(const uint16_t* __restrict__ q, const uint16_t* __restrict__ k,
                                                       const uint16_t* __restrict__ v, const uint16_t* __restrict__ shift_k,
                                                       const uint16_t* __restrict__ ssm_k, const uint16_t* __restrict__ ssm_D,
                                                       const uint16_t* __restrict__ wqkv, const uint16_t* __restrict__ wo,
                                                       char* __restrict__ ws, char* __restrict__ out) {
  extern __shared__ __attribute__((aligned(16))) char smem[];
  unsigned long long pc;
  asm volatile("s_getpc_b64 %0" : "=s"(pc));
  Cx c;
  c.rc = mk_rsrc((void*)(pc & ~255ull), CODE_BYTES);
  c.lane = threadIdx.x & 63; c.w = __builtin_amdgcn_readfirstlane(threadIdx.x >> 6); c.tid = threadIdx.x & 255; c.bid = blockIdx.x;
  const unsigned pf = c.touch_code();
  SB();
  asm volatile("" ::"s"(q), "s"(k), "s"(v), "s"(shift_k), "s"(ssm_k), "s"(ssm_D), "s"(wqkv), "s"(wo), "s"(ws), "s"(out));
  c.ws = ws; c.smem = smem; c.rw = mk_rsrc(ws, WS_USED);
  if (c.w >= 4) {
    front_h(c, pf, q, wqkv, wo);
    c.fresh(); g1_h<0>(c); c.fresh(); g2_h<0>(c, out);
    c.fresh(); g1_h<1>(c); c.fresh(); g2_h<1>(c, out);
  } else {
    front_m(c, pf, k, v, shift_k, ssm_k, ssm_D);
    c.fresh(); g1_m<0>(c); c.fresh(); g2_m<0>(c);
    c.fresh(); g1_m<1>(c); c.fresh(); g2_m<1>(c);
  }
}

extern "C" void kernel_launch(void* const* d_in, const int* in_sizes, int n_in, void* d_out, int out_size, void* d_ws,
                              size_t ws_size, hipStream_t stream) {
  (void)hipFuncSetAttribute((const void*)fused_kernel, hipFuncAttributeMaxDynamicSharedMemorySize, LDS_BYTES);
  fused_kernel<<<256, 512, LDS_BYTES, stream>>>((const uint16_t*)d_in[0], (const uint16_t*)d_in[1], (const uint16_t*)d_in[2],
                                                (const uint16_t*)d_in[3], (const uint16_t*)d_in[4], (const uint16_t*)d_in[5],
                                                (const uint16_t*)d_in[7], (const uint16_t*)d_in[8], (char*)d_ws, (char*)d_out);
}
```
